```python
import torch

def _softmax(x, axis=-1):
    return torch.softmax(x, dim=axis)
def _gelu(x):
    return torch.nn.functional.gelu(x, approximate="tanh")
def _silu(x):
    return torch.nn.functional.silu(x)
def _sigmoid(x):
    return torch.sigmoid(x)
def _layernorm(x, axis=-1, eps=1e-5):
    m = x.mean(dim=axis, keepdim=True)
    v = x.var(dim=axis, keepdim=True, unbiased=False)
    return (x - m) / torch.sqrt(v + eps)
def _scatter_add(tgt, idx, src, axis=0):
    return tgt.clone().index_add_(axis, idx, src)
def _segment_sum(src, seg, n):
    out = torch.zeros((n,) + src.shape[1:], dtype=src.dtype)
    return out.index_add_(0, seg, src)
def _pad(x, pw):
    flat = tuple(v for pair in reversed(pw) for v in pair)
    return torch.nn.functional.pad(x, flat)

kernel_name = 'interleave_layers__sliding_local_dilated__ssm_conv_h3__prompt'

def setup_inputs():

    _w = (lambda a: (lambda t: t.to(torch.bfloat16) if t.is_floating_point() else t)(torch.as_tensor(a)))
    def _A_setup():
        B, S, D = 1, 2048, 576
        return {
            "x": _w((lambda *s: torch.randn(*s, dtype=torch.bfloat16) * 0.05)(B, S, D)),
            "wqkv": _w((lambda *s: torch.randn(*s, dtype=torch.bfloat16) * 0.05)(D, 3 * D)),
            "wo": _w((lambda *s: torch.randn(*s, dtype=torch.bfloat16) * 0.05)(D, D)),
        }
    _A_in = _A_setup()

    def _B_setup():
        B, S, D = 1, 2048, 576
        ks = 4
        return {
            "q": _w((lambda *s: torch.randn(*s, dtype=torch.bfloat16) * 0.05)(B, S, D)),
            "k": _w((lambda *s: torch.randn(*s, dtype=torch.bfloat16) * 0.4)(B, S, D)),
            "v": _w((lambda *s: torch.randn(*s, dtype=torch.bfloat16) * 0.4)(B, S, D)),
            "shift_k": _w((lambda *s: torch.randn(*s, dtype=torch.bfloat16) * 0.4)(D, ks) * 0.5),
            "ssm_k": _w((lambda *s: torch.randn(*s, dtype=torch.bfloat16) * 0.8)(D, S) * (1.0 / S ** 0.5)),
            "ssm_D": _w((lambda *s: torch.randn(*s, dtype=torch.bfloat16) * 0.4)(D)),
        }
    _B_in = _B_setup()

    return {
        "A_x": _A_in["x"],
        "A_wqkv": _A_in["wqkv"],
        "A_wo": _A_in["wo"],
        "B_q": _B_in["q"],
        "B_k": _B_in["k"],
        "B_v": _B_in["v"],
        "B_shift_k": _B_in["shift_k"],
        "B_ssm_k": _B_in["ssm_k"],
        "B_ssm_D": _B_in["ssm_D"],
    }

def reference(A_x, A_wqkv, A_wo, B_q, B_k, B_v, B_shift_k, B_ssm_k, B_ssm_D):
    def _A_ref(x, wqkv, wo):
        B, S, D = x.shape
        H = 32; Dh = D // H
        W, dil = 8, 2
        qkv = (lambda x, *d: x.permute(*d))((x @ wqkv).reshape(B, S, 3, H, Dh), 2, 0, 3, 1, 4)
        q, k, v = qkv[0], qkv[1], qkv[2]
        scale = Dh ** -0.5
        pos = torch.arange(S)
        off = torch.arange(W) * dil
        idx = pos[:, None] - off[None, :]
        mask = idx >= 0
        idx_c = torch.where(mask, idx, 0).reshape(-1)
        kg = k[:, :, idx_c, :].reshape(B, H, S, W, Dh)
        vg = v[:, :, idx_c, :].reshape(B, H, S, W, Dh)
        logits = torch.einsum("bhsd,bhswd->bhsw", q, kg) * scale
        logits = torch.where(mask[None, None, :, :], logits, -1e9)
        attn = _softmax(logits, axis=-1)
        o = torch.einsum("bhsw,bhswd->bhsd", attn, vg)
        return (lambda x, *d: x.permute(*d))(o, 0, 2, 1, 3).reshape(B, S, D) @ wo

    def _B_ref(q, k, v, shift_k, ssm_k, ssm_D):
        B, S, D = q.shape
        ks = shift_k.shape[-1]
        k_pad = _pad(k, ((0, 0), (ks - 1, 0), (0, 0)))
        k_shift = torch.zeros_like(k)
        for t in range(ks):
            k_shift = k_shift + k_pad[:, t:t + S, :] * shift_k[:, ks - 1 - t][None, None, :]
        kv = k_shift * v
        L = 2 * S
        kv_pad = _pad(kv, ((0, 0), (0, S), (0, 0)))
        kf_pad = _pad(ssm_k, ((0, 0), (0, S)))
        kv_f = (lambda x, axes=(-2,-1): torch.fft.rfft2(x.float(), dim=axes))(kv_pad[:, None, :, :], axes=(1, 2))
        kf_f = (lambda x, axes=(-2,-1): torch.fft.rfft2(x.float(), dim=axes))(kf_pad[:, None, :], axes=(1, 2))
        kf_f = (lambda x, *d: x.permute(*d))(kf_f, 1, 2, 0)
        y_f = kv_f * kf_f[None, :, :, :]
        y = (lambda x, s, axes=(-2,-1): torch.fft.irfft2(x, s=s, dim=axes).to(torch.bfloat16))(y_f, (1, L), axes=(1, 2))[:, 0, :S, :]
        y = y + kv * ssm_D[None, None, :]
        return q * y

    x = A_x
    x = x + _A_ref(_layernorm(x, axis=-1), A_wqkv, A_wo)
    x = x + _B_ref(_layernorm(x, axis=-1), B_k, B_v, B_shift_k, B_ssm_k, B_ssm_D)
    x = x + _A_ref(_layernorm(x, axis=-1), A_wqkv, A_wo)
    x = x + _B_ref(_layernorm(x, axis=-1), B_k, B_v, B_shift_k, B_ssm_k, B_ssm_D)
    return x
```

```cpp
#include <hip/hip_runtime.h>
#include <hip/hip_bf16.h>
#include <stdint.h>

typedef unsigned short u16;
constexpr int S = 2048, D = 576, H = 32, DH = 18;

__device__ __forceinline__ float bf2f(u16 v) { return __uint_as_float(((unsigned)v) << 16); }
__device__ __forceinline__ u16 f2bf(float f) {
    unsigned u = __float_as_uint(f);
    return (u16)((u + 0x7FFFu + ((u >> 16) & 1u)) >> 16);
}
__device__ __forceinline__ float rbf(float f) { return bf2f(f2bf(f)); }

__global__ void k_kv(const u16* k, const u16* v, const u16* sk, float* kv) {
    int idx = blockIdx.x * blockDim.x + threadIdx.x;
    if (idx >= S * D) return;
    int t = idx / D, d = idx % D;
    float acc = 0.f;
    for (int i = 0; i < 4; ++i) {
        int tt = t + i - 3;
        float kk = tt >= 0 ? bf2f(k[tt * D + d]) : 0.f;
        acc = rbf(acc + rbf(kk * bf2f(sk[d * 4 + 3 - i])));
    }
    kv[idx] = rbf(acc * bf2f(v[idx]));
}
__global__ void k_conv(const float* kv, const u16* h, const u16* Dv, float* y) {
    int idx = blockIdx.x * blockDim.x + threadIdx.x;
    if (idx >= S * D) return;
    int t = idx / D, d = idx % D;
    float acc = 0.f;
    for (int s = 0; s <= t; ++s) acc += bf2f(h[d * S + (t - s)]) * kv[s * D + d];
    y[idx] = rbf(rbf(acc) + rbf(kv[idx] * bf2f(Dv[d])));
}
__global__ void k_tof32(const u16* x, float* o) {
    int idx = blockIdx.x * blockDim.x + threadIdx.x;
    if (idx < S * D) o[idx] = bf2f(x[idx]);
}
__global__ void k_ln(const float* x, float* xn) {
    int t = blockIdx.x, l = threadIdx.x;
    float s = 0.f;
    for (int i = l; i < D; i += 64) s += x[t * D + i];
    for (int m = 32; m >= 1; m >>= 1) s += __shfl_xor(s, m);
    float mean = s / D;
    float q = 0.f;
    for (int i = l; i < D; i += 64) { float d = x[t * D + i] - mean; q += d * d; }
    for (int m = 32; m >= 1; m >>= 1) q += __shfl_xor(q, m);
    float r = rsqrtf(q / D + 1e-5f);
    for (int i = l; i < D; i += 64) xn[t * D + i] = rbf((x[t * D + i] - mean) * r);
}
__global__ void k_gemm(const float* a, const u16* w, float* c, int N, const float* res) {
    int n = blockIdx.x * blockDim.x + threadIdx.x;
    int t = blockIdx.y;
    if (n >= N) return;
    float acc = 0.f;
    for (int k = 0; k < D; ++k) acc += a[t * D + k] * bf2f(w[k * N + n]);
    acc = rbf(acc);
    if (res) acc = rbf(acc + res[t * N + n]);
    c[t * N + n] = acc;
}
__global__ void k_attn(const float* qkv, float* o) {
    int idx = blockIdx.x * blockDim.x + threadIdx.x;
    if (idx >= S * H) return;
    int t = idx / H, h = idx % H;
    const float* q = qkv + t * 3 * D + h * DH;
    float lg[8], mx = -1e30f;
    for (int w = 0; w < 8; ++w) {
        int tt = t - 2 * w;
        if (tt >= 0) {
            const float* k = qkv + tt * 3 * D + D + h * DH;
            float s = 0.f;
            for (int d = 0; d < DH; ++d) s += q[d] * k[d];
            lg[w] = s * 0.23570226039551584f;
        } else lg[w] = -1e9f;
        mx = fmaxf(mx, lg[w]);
    }
    float sum = 0.f;
    for (int w = 0; w < 8; ++w) { lg[w] = __expf(lg[w] - mx); sum += lg[w]; }
    float inv = 1.f / sum;
    for (int d = 0; d < DH; ++d) {
        float a = 0.f;
        for (int w = 0; w < 8; ++w) {
            int tt = t - 2 * w; if (tt < 0) tt = 0;
            a += lg[w] * inv * qkv[tt * 3 * D + 2 * D + h * DH + d];
        }
        o[t * D + h * DH + d] = rbf(a);
    }
}
__global__ void k_bl(const float* x, const float* xn, const float* y, float* xo, u16* out) {
    int idx = blockIdx.x * blockDim.x + threadIdx.x;
    if (idx >= S * D) return;
    float v = rbf(x[idx] + rbf(xn[idx] * y[idx]));
    if (xo) xo[idx] = v;
    if (out) out[idx] = f2bf(v);
}

extern "C" void kernel_launch(void* const* d_in, const int* in_sizes, int n_in,
                              void* d_out, int out_size, void* d_ws, size_t ws_size,
                              hipStream_t stream) {
    const u16* x = (const u16*)d_in[0];
    const u16* wqkv = (const u16*)d_in[1];
    const u16* wo = (const u16*)d_in[2];
    const u16* bk = (const u16*)d_in[4];
    const u16* bv = (const u16*)d_in[5];
    const u16* sk = (const u16*)d_in[6];
    const u16* h = (const u16*)d_in[7];
    const u16* Dv = (const u16*)d_in[8];
    float* ws = (float*)d_ws;
    const int N = S * D;
    float* kv = ws; float* y = ws + N; float* xa = ws + 2 * N; float* xn = ws + 3 * N; float* o = ws + 4 * N; float* xb = ws + 5 * N;
    float* qkv = ws + 6 * N;
    int nb = (N + 255) / 256;
    k_kv<<<nb, 256, 0, stream>>>(bk, bv, sk, kv);
    k_conv<<<nb, 256, 0, stream>>>(kv, h, Dv, y);
    k_tof32<<<nb, 256, 0, stream>>>(x, xa);
    for (int layer = 0; layer < 2; ++layer) {
        k_ln<<<S, 64, 0, stream>>>(xa, xn);
        k_gemm<<<dim3(1728 / 64, S), 64, 0, stream>>>(xn, wqkv, qkv, 1728, nullptr);
        k_attn<<<(S * H + 255) / 256, 256, 0, stream>>>(qkv, o);
        k_gemm<<<dim3(576 / 64, S), 64, 0, stream>>>(o, wo, xb, 576, xa);
        k_ln<<<S, 64, 0, stream>>>(xb, xn);
        k_bl<<<nb, 256, 0, stream>>>(xb, xn, y, xa, layer == 1 ? (u16*)d_out : nullptr);
    }
}
```

```cpp
#include <hip/hip_runtime.h>
#include <stdint.h>

typedef unsigned int u32;
typedef unsigned short u16;
typedef unsigned long long u64;
typedef u32 v4u __attribute__((ext_vector_type(4)));
typedef u32 v2u __attribute__((ext_vector_type(2)));
typedef u32 v16u __attribute__((ext_vector_type(16)));
typedef u32 v32u __attribute__((ext_vector_type(32)));
typedef float v4f __attribute__((ext_vector_type(4)));
typedef float v2f __attribute__((ext_vector_type(2)));
typedef float v16f __attribute__((ext_vector_type(16)));
typedef __bf16 v8bf __attribute__((ext_vector_type(8)));
typedef __bf16 v2bf __attribute__((ext_vector_type(2)));
typedef short v4s __attribute__((ext_vector_type(4)));
#define LAS __attribute__((address_space(3)))
typedef __amdgpu_buffer_rsrc_t rsrc_t;
#define DI __device__ __forceinline__

#define NR 0
constexpr u32 FR = 2359296;
constexpr u32 TSL = 18432, WSLAB = 124416, WKS = 6912;
constexpr u32 SLOT = 81920, B_O1 = 0, B_XN2 = 4 * SLOT, B_O2 = 8 * SLOT, B_ST = 12 * SLOT, B_PR = 69632, OFF_YT = 8u << 20;
constexpr u32 OFF_XN1 = OFF_YT + FR, OFF_KVT = OFF_XN1 + FR, KVS = 4352, OFF_WQP = OFF_KVT + 576 * KVS, OFF_WOP = OFF_WQP + 16 * WSLAB;
constexpr u32 OFF_EP = OFF_WOP + 663552, OFF_F1 = NR ? SLOT + B_PR : OFF_EP + 1024, NREP = 8;
constexpr u32 OFF_F2 = OFF_F1 + (NR ? 1 : NREP) * 2048, OFF_DBG = OFF_EP + 1024 + 2 * NREP * 2048;
constexpr u32 WS_END = OFF_DBG + 256 * 8 * 256;
constexpr u32 SENT = 0xFFFFFFFFu;
constexpr int SC1 = 16;

constexpr u32 L_R = WSLAB;
constexpr u32 CB_U = 4128, CBS = 8480, L_YS = L_R + 3 * CBS;
constexpr u32 L_RED = L_R + 2 * CBS, L_KVX = L_YS + 4096, KVXS = 2432, L_FLG = L_KVX + 4 * KVXS, L_STAT = L_R + 7168;
constexpr u32 F_CA = L_FLG, F_C2 = L_FLG + 4, F_HR = L_FLG + 8  , F_K = L_FLG + 16  , F_V = L_FLG + 32  , F_W = L_FLG + 48  , F_VR = L_FLG + 64  , F_SEL = L_FLG + 80  ;
static_assert(L_FLG + 128 <= 163840 && L_KVX % 16 == 0 && L_FLG % 16 == 0, "LDS map");

constexpr u32 spread(u32 o) { return o + (o & ~8191u); }
DI rsrc_t mkrs(const void* p, u32 bytes) { return __builtin_amdgcn_make_buffer_rsrc((void*)p, (short)0, (int)bytes, 0x00020000); }
template <int AUX = 0> DI v4u ld16(rsrc_t r, u32 vo, u32 so = 0) { return __builtin_amdgcn_raw_buffer_load_b128(r, vo, so, AUX); }
template <int AUX = 0> DI v2u ld8(rsrc_t r, u32 vo, u32 so = 0) { return __builtin_amdgcn_raw_buffer_load_b64(r, vo, so, AUX); }
template <int AUX = 0> DI u32 ld4(rsrc_t r, u32 vo, u32 so = 0) { return __builtin_amdgcn_raw_buffer_load_b32(r, vo, so, AUX); }
template <int AUX = SC1> DI void sts16(rsrc_t r, u32 vo, u32 so, v4u v) { __builtin_amdgcn_raw_buffer_store_b128(v, r, vo, so, AUX); asm volatile("s_nop 1" :: "v"(v) : "memory"); }
DI void sts8(rsrc_t r, u32 vo, u32 so, v2u v) { __builtin_amdgcn_raw_buffer_store_b64(v, r, vo, so, SC1); }
DI v4f mfma(v4u a, v4u b, v4f c) {
    return __builtin_amdgcn_mfma_f32_16x16x32_bf16(__builtin_bit_cast(v8bf, a), __builtin_bit_cast(v8bf, b), c, 0, 0, 0);
}
DI v4f mfma16(v2u a, v2u b, v4f c) { return __builtin_amdgcn_mfma_f32_16x16x16bf16_1k(__builtin_bit_cast(v4s, a), __builtin_bit_cast(v4s, b), c, 0, 0, 0); }
DI v16f mfma32(v4u a, v4u b, v16f c) {
    return __builtin_amdgcn_mfma_f32_32x32x16_bf16(__builtin_bit_cast(v8bf, a), __builtin_bit_cast(v8bf, b), c, 0, 0, 0);
}
DI u32 pk(float lo, float hi) { v2f f = {lo, hi}; return __builtin_bit_cast(u32, __builtin_convertvector(f, v2bf)); }
DI float blo(u32 d) { return __builtin_bit_cast(float, d << 16); }
DI float bhi(u32 d) { return __builtin_bit_cast(float, d & 0xFFFF0000u); }
DI u32 san(u32 d) { return d < 0xFFFFFFFEu ? d : 0xFFFFFFFEu; }
DI u32 umax(u32 a, u32 b) { return a > b ? a : b; }
DI u32 umin(u32 a, u32 b) { return a < b ? a : b; }
DI u32 umax3(u32 a, u32 b, u32 c) { return umax(umax(a, b), c); }
DI bool anysent(u32 m) { return __builtin_amdgcn_ballot_w64(m == SENT) != 0; }
DI float f_(u32 x) { return __builtin_bit_cast(float, x); }
DI u32 u_(float x) { return __builtin_bit_cast(u32, x); }
DI v2u sw16(u32 a, u32 b) { return __builtin_amdgcn_permlane16_swap(a, b, false, false); }
DI v2u sw32(u32 a, u32 b) { return __builtin_amdgcn_permlane32_swap(a, b, false, false); }
DI float red_add_g(float v) { v2u r = sw16(u_(v), u_(v)); v = f_(r.x) + f_(r.y); r = sw32(u_(v), u_(v)); return f_(r.x) + f_(r.y); }
#define CFENCE() asm volatile("" ::: "memory")
#define LGKM0() asm volatile("s_waitcnt lgkmcnt(0)" ::: "memory")
DI void bar() { LGKM0(); __builtin_amdgcn_s_barrier(); CFENCE(); }
DI int lane_id() { int l; asm volatile("v_mbcnt_lo_u32_b32 %0, -1, 0\n\tv_mbcnt_hi_u32_b32 %0, -1, %0" : "=v"(l)); return l; }

#define TS(i)
#define JT()
#define TC(k)
#define TSA(k)
#define TSA_OPS
#define TSA_CLOB
#define TSDECL
#define TSTOP
#define TSARG
#define TSARG_R
#define TSPASS
#define TSOUT()

__shared__ __attribute__((aligned(16))) char smem[163840];
DI v4u lr16(u32 a) { return *(LAS v4u*)(size_t)a; }
DI v2u lr8(u32 a) { return *(LAS v2u*)(size_t)a; }
DI void lw16(u32 a, v4u v) { *(LAS v4u*)(size_t)a = v; }
DI void lw8(u32 a, v2u v) { *(LAS v2u*)(size_t)a = v; }
DI v2u vr8(u32 a) { return *(volatile LAS v2u*)(size_t)a; }
DI v4f lrf(u32 a) { return *(LAS v4f*)(size_t)a; }
DI void lwf(u32 a, v4f v) { *(LAS v4f*)(size_t)a = v; }
DI u32 vmov(u32 k) { u32 r; asm volatile("v_mov_b32 %0, %1" : "=v"(r) : "s"(k)); return r; }
DI void flag_add(u32 a, u32 v) {
    JT();
    asm volatile("s_waitcnt lgkmcnt(0)\n\ts_mov_b64 exec, 1\n\tds_add_u32 %0, %1\n\ts_mov_b64 exec, -1" :: "v"(vmov(a)), "v"(vmov(v)) : "memory");
    JT();
}
DI void flag_inc(u32 a) { JT(); asm volatile("s_mov_b64 exec, 1\n\tds_add_u32 %0, %1\n\ts_mov_b64 exec, -1" :: "v"(vmov(a)), "v"(vmov(1u)) : "memory"); JT(); }
DI u32 flag_inc_rtn(u32 a) {
    u32 r;
    JT();
    asm volatile("s_mov_b64 exec, 1\n\tds_add_rtn_u32 %0, %1, %2\n\ts_mov_b64 exec, -1\n\ts_waitcnt lgkmcnt(0)" : "=v"(r) : "v"(vmov(a)), "v"(vmov(1u)) : "memory");
    JT();
    return __builtin_amdgcn_readfirstlane(r);
}
DI void flag_addn(u32 a, u32 v) { JT(); asm volatile("s_mov_b64 exec, 1\n\tds_add_u32 %0, %1\n\ts_mov_b64 exec, -1" :: "v"(vmov(a)), "v"(vmov(v)) : "memory"); JT(); }
DI void flag_put(u32 a, u32 v) { JT(); asm volatile("s_mov_b64 exec, 1\n\tds_write_b32 %0, %1\n\ts_mov_b64 exec, -1" :: "v"(vmov(a)), "v"(vmov(v)) : "memory"); JT(); }
#define SPINB 16
constexpr int LSPIN = 16 << SPINB, SPIN = 1 << SPINB;
template <int SLP = 0> DI void flag_waitv(u32 a, u32 v) {
#pragma unroll 1
    for (int i = 0; i < LSPIN; ++i) {
        u32 c;
        asm volatile("ds_read_b32 %0, %1\n\ts_waitcnt lgkmcnt(0)" : "=v"(c) : "v"(a) : "memory");
        if (__builtin_amdgcn_readfirstlane(c) >= v) break;
        if (SLP) __builtin_amdgcn_s_sleep(SLP);
    }
    JT();
}
template <int SLP = 0> DI void flag_wait(u32 a, u32 v) { flag_waitv<SLP>(vmov(a), v); }
template <int SLP> DI void flag_wait_all(u32 a, u32 v) {
#pragma unroll 1
    for (int i = 0; i < LSPIN; ++i) {
        u32 c;
        asm volatile("ds_read_b32 %0, %1\n\ts_waitcnt lgkmcnt(0)" : "=v"(c) : "v"(a) : "memory");
        if (__builtin_amdgcn_ballot_w64(c < v) == 0) break;
        __builtin_amdgcn_s_sleep(SLP);
    }
    JT();
}
#define DMA_(o, c) "buffer_load_dwordx4 %1, %2, %3 offen offset:" #o c " lds\n\t"
#define DMA_BODY(seq) u32 keep; asm volatile("s_nop 4\n\ts_mov_b32 %0, m0\n\ts_mov_b32 m0, %4\n\ts_nop 0\n\t" seq "s_mov_b32 m0, %0" : "=&s"(keep) : "v"(voff), "s"(r), "s"(soff), "s"(dst) : "memory")
template <int C = 0> DI void dma4k(rsrc_t r, u32 voff, u32 soff, u32 dst) {
    if (C) { DMA_BODY(DMA_(0, " sc1") DMA_(1024, " sc1") DMA_(2048, " sc1") DMA_(3072, " sc1")); }
    else { DMA_BODY(DMA_(0, "") DMA_(1024, "") DMA_(2048, "") DMA_(3072, "")); }
}
#define KVNT ""
DI void dma4u(rsrc_t r, u32 voff, u32 so, u32 dst) {
    u32 keep;
    asm volatile("s_nop 4\n\ts_mov_b32 %0, m0\n\ts_mov_b32 m0, %4\n\ts_nop 0\n\t"
                 "buffer_load_dwordx4 %2, %3, %1 offen sc1" KVNT " lds\n\ts_sub_u32 %1, %1, 1056\n\t"
                 "buffer_load_dwordx4 %2, %3, %1 offen offset:1088 sc1" KVNT " lds\n\ts_sub_u32 %1, %1, 1056\n\t"
                 "buffer_load_dwordx4 %2, %3, %1 offen offset:2176 sc1" KVNT " lds\n\ts_sub_u32 %1, %1, 1056\n\t"
                 "buffer_load_dwordx4 %2, %3, %1 offen offset:3264 sc1" KVNT " lds\n\ts_mov_b32 m0, %0"
                 : "=&s"(keep), "+s"(so) : "v"(voff), "s"(r), "s"(dst) : "memory", "scc");
}
DI v2u trd(u32 a) { return __builtin_bit_cast(v2u, __builtin_amdgcn_ds_read_tr16_b64_v4i16((LAS v4s*)(size_t)a)); }
template <int C = 0> DI void dma2k(rsrc_t r, u32 voff, u32 soff, u32 dst) {
    if (C) { DMA_BODY(DMA_(0, " sc1") DMA_(1024, " sc1")); } else { DMA_BODY(DMA_(0, "") DMA_(1024, "")); }
}
template <int C = 0> DI void dma1k(rsrc_t r, u32 voff, u32 soff, u32 dst) {
    if (C) { DMA_BODY(DMA_(0, " sc1")); } else { DMA_BODY(DMA_(0, "")); }
}
template <int C> DI void dma18k(rsrc_t r, u32 voff, u32 soff, u32 dst) {
#pragma unroll
    for (int i = 0; i < 4; ++i) dma4k<C>(r, voff, soff + i * 4096, dst + i * 4096);
    dma2k<C>(r, voff, soff + 16384, dst + 16384);
}
template <int C> DI void dma36k(rsrc_t r, u32 voff, u32 soff, u32 dst) {
#pragma unroll 1
    for (int i = 0; i < 9; ++i) dma4k<C>(r, voff, soff + i * 4096, dst + i * 4096);
}
#define VMCNT(n) asm volatile("s_waitcnt vmcnt(" #n ")" ::: "memory")
template <int N> DI void vmcnt() { asm volatile("s_waitcnt vmcnt(%0)" :: "n"(N) : "memory"); }
#define PAD()
#define PADX() asm volatile("7:\n\ts_cbranch_execz 7f")
#define PADB(l) asm volatile(#l ":\n\ts_cbranch_execz 7f")
#define PADT() asm volatile("7:\n\ts_cbranch_execz L_back")
#define PADE(l) asm volatile("7:\n\ts_cbranch_execz L_back\n\t" #l ":\n\ts_cbranch_execz 7f")
#define HOP(l) asm volatile("s_mov_b64 exec, 0\n\ts_call_b64 s[92:93], " #l "\n\ts_mov_b64 exec, -1" ::: "s92", "s93")
#define OPQV(x) asm volatile("" : "+v"(x))
#define OPQS(x) asm volatile("" : "+s"(x))
#define SB() __builtin_amdgcn_sched_barrier(0)
template <int SLP> DI void prepoll(rsrc_t r, u32 vo, u32 so) {
#pragma unroll 1
    for (int s_ = 0; s_ < SPIN; ++s_) {
        CFENCE();
        if (!anysent(ld4<SC1>(r, vo, so))) break;
        __builtin_amdgcn_s_sleep(SLP);
    }
}
DI float fmx(float a, float b) { return __builtin_elementwise_maximum(a, b); }
DI v4u cat(v2u a, v2u b) { return v4u{a.x, a.y, b.x, b.y}; }
DI u32 sld(const void* p, int i) { return ((const __attribute__((address_space(4))) u32*)(size_t)p)[i]; }
#define MAP_TI(x, r) (4 * ((x) >> 1) + ((r) & 3))
#define MAP_P(x, r) (8 * ((x) & 1) + ((r) >> 2))
#define D_X 10
#define D_H 24
#define D_O 4
#define D_ST 4
#define CHQ(b) (512 + ((b) & 7) + ((b) >> 5) * 8)
#define D_P 8
#define D_R 2
#define XSLOT(li) ((li) < 8 ? ((li) & 1) | ((li) & 2) << 1 | ((li) & 4) >> 1 : (li) & 11)
#define HPOS 1
#define POLL2 2
#define PR_C 3
#define PR_M 2
#define HPP 1
#define HL 6
#define HQ1 3
#define HA1 9
#define TOUCH 0
#define OAUX 0
#define WOV 0
#define RSP 1
#define SELX -1
#define FO1 0
#define FX2 0
#define FO2 0
#define FST 0
#define HLD 16
#define HS 16
#define HF 16
#define PS 16
#define SCRUB 0
#define SCR0 4
#define SCR1 0
#define SCRM 0
#define D_V 0
#define D_W 7
#define SCRK 0
#define SCRL 0
#define SCR2 0
#define SCRH 0
#define SCRG 1
#define B3M 17
#define B3A 14
#define PR_W 3
#define PDW 3
#define WOW 0
#define YL 2
#define YN 2
#define M2 1
#define SD0 1
#define SD1 1
#define QM 0
#define Q1 ((bid & 7) * 32 + (bid >> 3))
#define Q2 ((bid & 4) ? 128 + (bid >> 5) * 16 + (bid & 3) * 4 + (bid >> 3 & 3) : Q1)
#define QW (QM & 1 ? Q1 : QM & 8 ? Q2 : bid)
#define QO (QM & 2 ? Q2 : bid)
#define QK (QM & 4 ? ((bid & 4) ? 0 : 128 + (bid >> 3) * 4 + (bid & 3)) : bid)
#define HS0 1
#define HQ2 3
#define HQ3 1
#define WOFF 64
#define PB1 0
#define D_A 0
#define D_B 1
#define OPP 0
#define D_Q 1
#define A_CV \
    "buffer_load_dwordx4 %[h0], %[l16], %[rs], %[hab] offen offset:0 sc1\n\t" \
    "buffer_load_dwordx4 %[h1], %[l16], %[rs], %[hab] offen offset:1024 sc1\n\t" \
    "buffer_load_dwordx4 %[h2], %[l16], %[rs], %[hab] offen offset:2048 sc1\n\t" \
    "buffer_load_dwordx4 %[h3], %[l16], %[rs], %[hab] offen offset:3072 sc1\n\t" \
    "s_add_u32 %[hab], %[hab], 4096\n\t" \
    "buffer_load_dwordx4 %[h4], %[l16], %[rs], %[hab] offen offset:0 sc1\n\t" \
    "buffer_load_dwordx4 %[h5], %[l16], %[rs], %[hab] offen offset:1024 sc1\n\t" \
    "buffer_load_dwordx4 %[h6], %[l16], %[rs], %[hab] offen offset:2048 sc1\n\t" \
    "buffer_load_dwordx4 %[h7], %[l16], %[rs], %[hab] offen offset:3072 sc1\n\t" \
    "s_add_u32 %[hab], %[hab], 4096\n\t" \
    "buffer_load_dwordx4 %[h8], %[l16], %[rs], %[hab] offen offset:0 sc1\n\t" \
    "s_cmp_lg_u32 %[cs], 0\n\t" \
    "s_cbranch_scc1 CW_%=\n\t" \
    "v_mov_b32 v0, 1\n\t" \
    "s_waitcnt vmcnt(9)\n\t" \
    "s_mov_b64 exec, 1\n\t" \
    "ds_add_u32 %[fc], v0\n\t" \
    "s_mov_b64 exec, -1\n\t" \
    "s_branch L_cv0_0\n\t" \
    "CW_%=:\n\t" \
    "s_mov_b32 %[hab], %[ls]\n\t" \
    "CL_%=:\n\t" \
    "ds_read_b32 v0, %[fc]\n\t" \
    "s_sub_u32 %[hab], %[hab], 1\n\t" \
    "s_cbranch_scc1 CE_%=\n\t" \
    "s_waitcnt lgkmcnt(0)\n\t" \
    "v_cmp_gt_u32 vcc, 1, v0\n\t" \
    "s_cbranch_vccnz CL_%=\n\t" \
    "CE_%=:\n\t" \
    "s_waitcnt lgkmcnt(0)\n\t" \
    "s_cmp_eq_u32 %[cs], 1\n\t" \
    "s_cbranch_scc1 L_cv1_0\n\t" \
    "s_cmp_eq_u32 %[cs], 2\n\t" \
    "s_cbranch_scc1 L_cv2_0\n\t" \
    "s_cmp_eq_u32 %[cs], 3\n\t" \
    "s_cbranch_scc1 L_cv3_0\n\t" \
    "s_cmp_eq_u32 %[cs], 4\n\t" \
    "s_cbranch_scc1 L_cv4_0\n\t" \
    "s_cmp_eq_u32 %[cs], 5\n\t" \
    "s_cbranch_scc1 L_cv5_0\n\t" \
    "s_cmp_eq_u32 %[cs], 6\n\t" \
    "s_cbranch_scc1 L_cv6_0\n\t" \
    "L_cv0_0:\n\t" \
    "s_cbranch_execz L_cv0_1\n\t" \
    "ds_read_b64 v[0:1], %[ub] offset:0\n\t" \
    "ds_read_b64 v[2:3], %[ub] offset:8\n\t" \
    "ds_read_b64_tr_b16 v[4:5], %[hb] offset:32\n\t" \
    "ds_read_b64_tr_b16 v[6:7], %[hb] offset:0\n\t" \
    "ds_read_b64 v[8:9], %[ub] offset:32\n\t" \
    "ds_read_b64 v[10:11], %[ub] offset:40\n\t" \
    "ds_read_b64_tr_b16 v[12:13], %[hb] offset:1568\n\t" \
    "ds_read_b64_tr_b16 v[14:15], %[hb] offset:1536\n\t" \
    "ds_read_b64_tr_b16 v[16:17], %[hb] offset:1824\n\t" \
    "ds_read_b64_tr_b16 v[18:19], %[hb] offset:1792\n\t" \
    "ds_read_b64_tr_b16 v[20:21], %[hb] offset:2080\n\t" \
    "ds_read_b64_tr_b16 v[22:23], %[hb] offset:2048\n\t" \
    "v_mov_b32 v168, 0\n\t" \
    "v_mov_b32 v169, 0\n\t" \
    "v_mov_b32 v170, 0\n\t" \
    "v_mov_b32 v171, 0\n\t" \
    "v_mov_b32 v172, 0\n\t" \
    "v_mov_b32 v173, 0\n\t" \
    "v_mov_b32 v174, 0\n\t" \
    "v_mov_b32 v175, 0\n\t" \
    "s_waitcnt lgkmcnt(8)\n\t" \
    "v_mfma_f32_16x16x32_bf16 v[24:27], v[0:3], v[4:7], 0\n\t" \
    "ds_read_b64_tr_b16 v[28:29], %[hb] offset:2336\n\t" \
    "ds_read_b64_tr_b16 v[30:31], %[hb] offset:2304\n\t" \
    "ds_read_b64_tr_b16 v[32:33], %[hb] offset:2592\n\t" \
    "s_waitcnt lgkmcnt(9)\n\t" \
    "v_mfma_f32_16x16x32_bf16 v[36:39], v[8:11], v[4:7], 0\n\t" \
    "ds_read_b64_tr_b16 v[34:35], %[hb] offset:2560\n\t" \
    "ds_read_u16_d16_hi v168, %[ua] offset:0\n\t" \
    "ds_read_u16_d16_hi v169, %[ua] offset:32\n\t" \
    "s_waitcnt lgkmcnt(10)\n\t" \
    "v_mfma_f32_16x16x32_bf16 v[40:43], v[0:3], v[12:15], 0\n\t" \
    "ds_read_b64_tr_b16 v[44:45], %[hb] offset:2848\n\t" \
    "L_cv0_1:\n\t" \
    "s_cbranch_execz L_cv0_2\n\t" \
    "v_mfma_f32_16x16x32_bf16 v[48:51], v[8:11], v[12:15], 0\n\t" \
    "ds_read_b64_tr_b16 v[46:47], %[hb] offset:2816\n\t" \
    "ds_read_b64_tr_b16 v[52:53], %[hb] offset:288\n\t" \
    "s_waitcnt lgkmcnt(11)\n\t" \
    "v_mfma_f32_16x16x32_bf16 v[56:59], v[0:3], v[16:19], 0\n\t" \
    "ds_read_b64_tr_b16 v[54:55], %[hb] offset:256\n\t" \
    "v_mfma_f32_16x16x32_bf16 v[60:63], v[8:11], v[16:19], 0\n\t" \
    "ds_read_b64 v[64:65], %[ub] offset:64\n\t" \
    "ds_read_b64 v[66:67], %[ub] offset:72\n\t" \
    "s_waitcnt lgkmcnt(12)\n\t" \
    "v_mfma_f32_16x16x32_bf16 v[68:71], v[0:3], v[20:23], 0\n\t" \
    "ds_read_b64 v[72:73], %[ub] offset:96\n\t" \
    "v_mfma_f32_16x16x32_bf16 v[76:79], v[8:11], v[20:23], 0\n\t" \
    "ds_read_b64 v[74:75], %[ub] offset:104\n\t" \
    "v_cndmask_b32_e64 v163, v27, 0, %[m3]\n\t" \
    "s_waitcnt lgkmcnt(12)\n\t" \
    "v_mfma_f32_16x16x32_bf16 v[80:83], v[0:3], v[28:31], 0\n\t" \
    "v_cndmask_b32_e64 v167, v39, v27, %[m3]\n\t" \
    "v_mfma_f32_16x16x32_bf16 v[84:87], v[8:11], v[28:31], 0\n\t" \
    "v_cndmask_b32_e64 v162, v26, 0, %[m2]\n\t" \
    "v_cndmask_b32_e64 v166, v38, v26, %[m2]\n\t" \
    "s_waitcnt lgkmcnt(10)\n\t" \
    "v_mfma_f32_16x16x32_bf16 v[88:91], v[0:3], v[32:35], 0\n\t" \
    "v_cndmask_b32_e64 v161, v25, 0, %[m1]\n\t" \
    "v_mfma_f32_16x16x32_bf16 v[92:95], v[8:11], v[32:35], 0\n\t" \
    "v_cndmask_b32_e64 v165, v37, v25, %[m1]\n\t" \
    "v_add_f32_dpp v162, v163, v162 wave_ror:1 row_mask:0xf bank_mask:0xf\n\t" \
    "s_waitcnt lgkmcnt(6)\n\t" \
    "v_mfma_f32_16x16x32_bf16 v[96:99], v[0:3], v[44:47], 0\n\t" \
    "v_add_f32_dpp v166, v167, v166 wave_ror:1 row_mask:0xf bank_mask:0xf\n\t" \
    "v_mfma_f32_16x16x32_bf16 v[100:103], v[8:11], v[44:47], 0\n\t" \
    "ds_read_u16_d16_hi v170, %[ua] offset:64\n\t" \
    "ds_read_u16_d16_hi v171, %[ua] offset:96\n\t" \
    "s_waitcnt lgkmcnt(6)\n\t" \
    "L_cv0_2:\n\t" \
    "s_cbranch_execz L_cv0_3\n\t" \
    "v_mfma_f32_16x16x32_bf16 v[104:107], v[0:3], v[52:55], 0\n\t" \
    "v_fmac_f32 v24, %[dd], v168\n\t" \
    "v_mfma_f32_16x16x32_bf16 v[108:111], v[8:11], v[52:55], 0\n\t" \
    "v_fmac_f32 v36, %[dd], v169\n\t" \
    "v_add_f32_dpp v161, v162, v161 wave_ror:1 row_mask:0xf bank_mask:0xf\n\t" \
    "s_waitcnt lgkmcnt(2)\n\t" \
    "v_mfma_f32_16x16x32_bf16 v[104:107], v[64:67], v[4:7], v[104:107]\n\t" \
    "ds_read_b64_tr_b16 v[112:113], %[hb] offset:544\n\t" \
    "v_mfma_f32_16x16x32_bf16 v[108:111], v[72:75], v[4:7], v[108:111]\n\t" \
    "ds_read_b64_tr_b16 v[114:115], %[hb] offset:512\n\t" \
    "v_add_f32_dpp v165, v166, v165 wave_ror:1 row_mask:0xf bank_mask:0xf\n\t" \
    "v_mfma_f32_16x16x32_bf16 v[56:59], v[64:67], v[12:15], v[56:59]\n\t" \
    "v_add_f32_dpp v24, v161, v24 wave_ror:1 row_mask:0xf bank_mask:0xf\n\t" \
    "v_add_f32_dpp v36, v165, v36 wave_ror:1 row_mask:0xf bank_mask:0xf\n\t" \
    "v_mfma_f32_16x16x32_bf16 v[60:63], v[72:75], v[12:15], v[60:63]\n\t" \
    "v_cvt_pk_bf16_f32 v160, v24, v36\n\t" \
    "ds_write_b16 %[ya], v160 offset:0\n\t" \
    "v_mfma_f32_16x16x32_bf16 v[68:71], v[64:67], v[16:19], v[68:71]\n\t" \
    "ds_read_b64 v[116:117], %[ub] offset:128\n\t" \
    "ds_read_b64 v[118:119], %[ub] offset:136\n\t" \
    "v_mfma_f32_16x16x32_bf16 v[76:79], v[72:75], v[16:19], v[76:79]\n\t" \
    "ds_read_b64 v[120:121], %[ub] offset:160\n\t" \
    "ds_read_b64 v[122:123], %[ub] offset:168\n\t" \
    "v_mfma_f32_16x16x32_bf16 v[80:83], v[64:67], v[20:23], v[80:83]\n\t" \
    "ds_write_b16_d16_hi %[ya], v160 offset:128\n\t" \
    "v_cndmask_b32_e64 v163, v107, v39, %[m3]\n\t" \
    "v_mfma_f32_16x16x32_bf16 v[84:87], v[72:75], v[20:23], v[84:87]\n\t" \
    "v_cndmask_b32_e64 v167, v111, v107, %[m3]\n\t" \
    "v_cndmask_b32_e64 v162, v106, v38, %[m2]\n\t" \
    "v_mfma_f32_16x16x32_bf16 v[88:91], v[64:67], v[28:31], v[88:91]\n\t" \
    "v_cndmask_b32_e64 v166, v110, v106, %[m2]\n\t" \
    "v_cndmask_b32_e64 v161, v105, v37, %[m1]\n\t" \
    "L_cv0_3:\n\t" \
    "s_cbranch_execz L_cv0_4\n\t" \
    "v_mfma_f32_16x16x32_bf16 v[92:95], v[72:75], v[28:31], v[92:95]\n\t" \
    "v_cndmask_b32_e64 v165, v109, v105, %[m1]\n\t" \
    "v_add_f32_dpp v162, v163, v162 wave_ror:1 row_mask:0xf bank_mask:0xf\n\t" \
    "v_mfma_f32_16x16x32_bf16 v[96:99], v[64:67], v[32:35], v[96:99]\n\t" \
    "v_add_f32_dpp v166, v167, v166 wave_ror:1 row_mask:0xf bank_mask:0xf\n\t" \
    "s_waitcnt lgkmcnt(8)\n\t" \
    "v_fmac_f32 v104, %[dd], v170\n\t" \
    "v_mfma_f32_16x16x32_bf16 v[100:103], v[72:75], v[32:35], v[100:103]\n\t" \
    "v_fmac_f32 v108, %[dd], v171\n\t" \
    "v_add_f32_dpp v161, v162, v161 wave_ror:1 row_mask:0xf bank_mask:0xf\n\t" \
    "s_waitcnt lgkmcnt(6)\n\t" \
    "v_mfma_f32_16x16x32_bf16 v[124:127], v[0:3], v[112:115], 0\n\t" \
    "v_add_f32_dpp v165, v166, v165 wave_ror:1 row_mask:0xf bank_mask:0xf\n\t" \
    "v_mfma_f32_16x16x32_bf16 v[128:131], v[8:11], v[112:115], 0\n\t" \
    "ds_read_u16_d16_hi v172, %[ua] offset:128\n\t" \
    "ds_read_u16_d16_hi v173, %[ua] offset:160\n\t" \
    "v_mfma_f32_16x16x32_bf16 v[124:127], v[64:67], v[52:55], v[124:127]\n\t" \
    "ds_read_b64_tr_b16 v[132:133], %[hb] offset:800\n\t" \
    "ds_read_b64_tr_b16 v[134:135], %[hb] offset:768\n\t" \
    "v_mfma_f32_16x16x32_bf16 v[128:131], v[72:75], v[52:55], v[128:131]\n\t" \
    "v_add_f32_dpp v104, v161, v104 wave_ror:1 row_mask:0xf bank_mask:0xf\n\t" \
    "v_add_f32_dpp v108, v165, v108 wave_ror:1 row_mask:0xf bank_mask:0xf\n\t" \
    "s_waitcnt lgkmcnt(5)\n\t" \
    "v_mfma_f32_16x16x32_bf16 v[124:127], v[116:119], v[4:7], v[124:127]\n\t" \
    "v_cvt_pk_bf16_f32 v160, v104, v108\n\t" \
    "v_mfma_f32_16x16x32_bf16 v[128:131], v[120:123], v[4:7], v[128:131]\n\t" \
    "ds_write_b16 %[ya], v160 offset:256\n\t" \
    "ds_write_b16_d16_hi %[ya], v160 offset:384\n\t" \
    "v_mfma_f32_16x16x32_bf16 v[68:71], v[116:119], v[12:15], v[68:71]\n\t" \
    "L_cv0_4:\n\t" \
    "s_cbranch_execz L_cv0_5\n\t" \
    "v_mfma_f32_16x16x32_bf16 v[76:79], v[120:123], v[12:15], v[76:79]\n\t" \
    "v_mfma_f32_16x16x32_bf16 v[80:83], v[116:119], v[16:19], v[80:83]\n\t" \
    "ds_read_b64 v[136:137], %[ub] offset:192\n\t" \
    "ds_read_b64 v[138:139], %[ub] offset:200\n\t" \
    "v_mfma_f32_16x16x32_bf16 v[84:87], v[120:123], v[16:19], v[84:87]\n\t" \
    "ds_read_b64 v[140:141], %[ub] offset:224\n\t" \
    "ds_read_b64 v[142:143], %[ub] offset:232\n\t" \
    "v_mfma_f32_16x16x32_bf16 v[88:91], v[116:119], v[20:23], v[88:91]\n\t" \
    "v_cndmask_b32_e64 v163, v127, v111, %[m3]\n\t" \
    "v_cndmask_b32_e64 v167, v131, v127, %[m3]\n\t" \
    "v_mfma_f32_16x16x32_bf16 v[92:95], v[120:123], v[20:23], v[92:95]\n\t" \
    "v_cndmask_b32_e64 v162, v126, v110, %[m2]\n\t" \
    "v_cndmask_b32_e64 v166, v130, v126, %[m2]\n\t" \
    "v_mfma_f32_16x16x32_bf16 v[96:99], v[116:119], v[28:31], v[96:99]\n\t" \
    "v_cndmask_b32_e64 v161, v125, v109, %[m1]\n\t" \
    "v_cndmask_b32_e64 v165, v129, v125, %[m1]\n\t" \
    "v_mfma_f32_16x16x32_bf16 v[100:103], v[120:123], v[28:31], v[100:103]\n\t" \
    "v_add_f32_dpp v162, v163, v162 wave_ror:1 row_mask:0xf bank_mask:0xf\n\t" \
    "v_add_f32_dpp v166, v167, v166 wave_ror:1 row_mask:0xf bank_mask:0xf\n\t" \
    "s_waitcnt lgkmcnt(6)\n\t" \
    "v_mfma_f32_16x16x32_bf16 v[144:147], v[0:3], v[132:135], 0\n\t" \
    "v_fmac_f32 v124, %[dd], v172\n\t" \
    "v_mfma_f32_16x16x32_bf16 v[148:151], v[8:11], v[132:135], 0\n\t" \
    "v_fmac_f32 v128, %[dd], v173\n\t" \
    "v_add_f32_dpp v161, v162, v161 wave_ror:1 row_mask:0xf bank_mask:0xf\n\t" \
    "v_mfma_f32_16x16x32_bf16 v[144:147], v[64:67], v[112:115], v[144:147]\n\t" \
    "ds_read_b64_tr_b16 v[152:153], %[hb] offset:1056\n\t" \
    "ds_read_b64_tr_b16 v[154:155], %[hb] offset:1024\n\t" \
    "v_mfma_f32_16x16x32_bf16 v[148:151], v[72:75], v[112:115], v[148:151]\n\t" \
    "ds_read_u16_d16_hi v174, %[ua] offset:192\n\t" \
    "ds_read_u16_d16_hi v175, %[ua] offset:224\n\t" \
    "v_mfma_f32_16x16x32_bf16 v[144:147], v[116:119], v[52:55], v[144:147]\n\t" \
    "L_cv0_5:\n\t" \
    "s_cbranch_execz L_cv0_6\n\t" \
    "v_add_f32_dpp v165, v166, v165 wave_ror:1 row_mask:0xf bank_mask:0xf\n\t" \
    "v_add_f32_dpp v124, v161, v124 wave_ror:1 row_mask:0xf bank_mask:0xf\n\t" \
    "v_mfma_f32_16x16x32_bf16 v[148:151], v[120:123], v[52:55], v[148:151]\n\t" \
    "v_add_f32_dpp v128, v165, v128 wave_ror:1 row_mask:0xf bank_mask:0xf\n\t" \
    "v_cvt_pk_bf16_f32 v160, v124, v128\n\t" \
    "s_waitcnt lgkmcnt(4)\n\t" \
    "v_mfma_f32_16x16x32_bf16 v[144:147], v[136:139], v[4:7], v[144:147]\n\t" \
    "ds_write_b16 %[ya], v160 offset:512\n\t" \
    "v_mfma_f32_16x16x32_bf16 v[148:151], v[140:143], v[4:7], v[148:151]\n\t" \
    "ds_write_b16_d16_hi %[ya], v160 offset:640\n\t" \
    "v_mfma_f32_16x16x32_bf16 v[80:83], v[136:139], v[12:15], v[80:83]\n\t" \
    "v_mfma_f32_16x16x32_bf16 v[84:87], v[140:143], v[12:15], v[84:87]\n\t" \
    "v_mfma_f32_16x16x32_bf16 v[88:91], v[136:139], v[16:19], v[88:91]\n\t" \
    "ds_read_b64 v[156:157], %[ub] offset:256\n\t" \
    "ds_read_b64 v[158:159], %[ub] offset:264\n\t" \
    "v_mfma_f32_16x16x32_bf16 v[92:95], v[140:143], v[16:19], v[92:95]\n\t" \
    "ds_read_b64 v[44:45], %[ub] offset:288\n\t" \
    "ds_read_b64 v[46:47], %[ub] offset:296\n\t" \
    "v_mfma_f32_16x16x32_bf16 v[96:99], v[136:139], v[20:23], v[96:99]\n\t" \
    "v_cndmask_b32_e64 v163, v147, v131, %[m3]\n\t" \
    "v_cndmask_b32_e64 v167, v151, v147, %[m3]\n\t" \
    "v_mfma_f32_16x16x32_bf16 v[100:103], v[140:143], v[20:23], v[100:103]\n\t" \
    "v_cndmask_b32_e64 v162, v146, v130, %[m2]\n\t" \
    "v_cndmask_b32_e64 v166, v150, v146, %[m2]\n\t" \
    "s_waitcnt lgkmcnt(8)\n\t" \
    "v_mfma_f32_16x16x32_bf16 v[24:27], v[0:3], v[152:155], 0\n\t" \
    "v_cndmask_b32_e64 v161, v145, v129, %[m1]\n\t" \
    "v_mfma_f32_16x16x32_bf16 v[32:35], v[8:11], v[152:155], 0\n\t" \
    "v_cndmask_b32_e64 v165, v149, v145, %[m1]\n\t" \
    "v_add_f32_dpp v162, v163, v162 wave_ror:1 row_mask:0xf bank_mask:0xf\n\t" \
    "v_mfma_f32_16x16x32_bf16 v[24:27], v[64:67], v[132:135], v[24:27]\n\t" \
    "ds_read_b64_tr_b16 v[36:37], %[hb] offset:1312\n\t" \
    "L_cv0_6:\n\t" \
    "s_cbranch_execz L_cv0_7\n\t" \
    "ds_read_b64_tr_b16 v[38:39], %[hb] offset:1280\n\t" \
    "v_mfma_f32_16x16x32_bf16 v[32:35], v[72:75], v[132:135], v[32:35]\n\t" \
    "v_add_f32_dpp v166, v167, v166 wave_ror:1 row_mask:0xf bank_mask:0xf\n\t" \
    "s_waitcnt lgkmcnt(8)\n\t" \
    "v_fmac_f32 v144, %[dd], v174\n\t" \
    "v_mfma_f32_16x16x32_bf16 v[24:27], v[116:119], v[112:115], v[24:27]\n\t" \
    "v_fmac_f32 v148, %[dd], v175\n\t" \
    "v_add_f32_dpp v161, v162, v161 wave_ror:1 row_mask:0xf bank_mask:0xf\n\t" \
    "v_mfma_f32_16x16x32_bf16 v[32:35], v[120:123], v[112:115], v[32:35]\n\t" \
    "ds_read_u16_d16_hi v168, %[ua] offset:256\n\t" \
    "ds_read_u16_d16_hi v169, %[ua] offset:288\n\t" \
    "v_mfma_f32_16x16x32_bf16 v[24:27], v[136:139], v[52:55], v[24:27]\n\t" \
    "v_add_f32_dpp v165, v166, v165 wave_ror:1 row_mask:0xf bank_mask:0xf\n\t" \
    "v_add_f32_dpp v144, v161, v144 wave_ror:1 row_mask:0xf bank_mask:0xf\n\t" \
    "v_mfma_f32_16x16x32_bf16 v[32:35], v[140:143], v[52:55], v[32:35]\n\t" \
    "v_add_f32_dpp v148, v165, v148 wave_ror:1 row_mask:0xf bank_mask:0xf\n\t" \
    "v_cvt_pk_bf16_f32 v160, v144, v148\n\t" \
    "s_waitcnt lgkmcnt(4)\n\t" \
    "v_mfma_f32_16x16x32_bf16 v[24:27], v[156:159], v[4:7], v[24:27]\n\t" \
    "ds_write_b16 %[ya], v160 offset:768\n\t" \
    "v_mfma_f32_16x16x32_bf16 v[32:35], v[44:47], v[4:7], v[32:35]\n\t" \
    "ds_write_b16_d16_hi %[ya], v160 offset:896\n\t" \
    "ds_read_b128 v[176:179], %[yr]\n\t" \
    "v_mfma_f32_16x16x32_bf16 v[88:91], v[156:159], v[12:15], v[88:91]\n\t" \
    "v_mfma_f32_16x16x32_bf16 v[92:95], v[44:47], v[12:15], v[92:95]\n\t" \
    "v_mfma_f32_16x16x32_bf16 v[96:99], v[156:159], v[16:19], v[96:99]\n\t" \
    "ds_read_b64 v[104:105], %[ub] offset:320\n\t" \
    "ds_read_b64 v[106:107], %[ub] offset:328\n\t" \
    "v_mfma_f32_16x16x32_bf16 v[100:103], v[44:47], v[16:19], v[100:103]\n\t" \
    "ds_read_b64 v[28:29], %[ub] offset:352\n\t" \
    "ds_read_b64 v[30:31], %[ub] offset:360\n\t" \
    "s_waitcnt lgkmcnt(9)\n\t" \
    "v_mfma_f32_16x16x32_bf16 v[108:111], v[0:3], v[36:39], 0\n\t" \
    "L_cv0_7:\n\t" \
    "s_cbranch_execz L_cv0_8\n\t" \
    "v_cndmask_b32_e64 v163, v27, v151, %[m3]\n\t" \
    "v_mfma_f32_16x16x32_bf16 v[124:127], v[8:11], v[36:39], 0\n\t" \
    "v_cndmask_b32_e64 v167, v35, v27, %[m3]\n\t" \
    "v_cndmask_b32_e64 v162, v26, v150, %[m2]\n\t" \
    "v_mfma_f32_16x16x32_bf16 v[108:111], v[64:67], v[152:155], v[108:111]\n\t" \
    "v_cndmask_b32_e64 v166, v34, v26, %[m2]\n\t" \
    "v_cndmask_b32_e64 v161, v25, v149, %[m1]\n\t" \
    "v_mfma_f32_16x16x32_bf16 v[124:127], v[72:75], v[152:155], v[124:127]\n\t" \
    "v_cndmask_b32_e64 v165, v33, v25, %[m1]\n\t" \
    "v_add_f32_dpp v162, v163, v162 wave_ror:1 row_mask:0xf bank_mask:0xf\n\t" \
    "v_mfma_f32_16x16x32_bf16 v[108:111], v[116:119], v[132:135], v[108:111]\n\t" \
    "v_add_f32_dpp v166, v167, v166 wave_ror:1 row_mask:0xf bank_mask:0xf\n\t" \
    "s_waitcnt lgkmcnt(7)\n\t" \
    "v_fmac_f32 v24, %[dd], v168\n\t" \
    "v_mfma_f32_16x16x32_bf16 v[124:127], v[120:123], v[132:135], v[124:127]\n\t" \
    "v_fmac_f32 v32, %[dd], v169\n\t" \
    "v_add_f32_dpp v161, v162, v161 wave_ror:1 row_mask:0xf bank_mask:0xf\n\t" \
    "v_mfma_f32_16x16x32_bf16 v[108:111], v[136:139], v[112:115], v[108:111]\n\t" \
    "v_add_f32_dpp v165, v166, v165 wave_ror:1 row_mask:0xf bank_mask:0xf\n\t" \
    "v_add_f32_dpp v24, v161, v24 wave_ror:1 row_mask:0xf bank_mask:0xf\n\t" \
    "v_mfma_f32_16x16x32_bf16 v[124:127], v[140:143], v[112:115], v[124:127]\n\t" \
    "ds_read_u16_d16_hi v170, %[ua] offset:320\n\t" \
    "ds_read_u16_d16_hi v171, %[ua] offset:352\n\t" \
    "v_mfma_f32_16x16x32_bf16 v[108:111], v[156:159], v[52:55], v[108:111]\n\t" \
    "v_add_f32_dpp v32, v165, v32 wave_ror:1 row_mask:0xf bank_mask:0xf\n\t" \
    "v_cvt_pk_bf16_f32 v160, v24, v32\n\t" \
    "v_mfma_f32_16x16x32_bf16 v[124:127], v[44:47], v[52:55], v[124:127]\n\t" \
    "ds_write_b16 %[ya], v160 offset:0\n\t" \
    "ds_write_b16_d16_hi %[ya], v160 offset:128\n\t" \
    "s_waitcnt lgkmcnt(4)\n\t" \
    "v_mfma_f32_16x16x32_bf16 v[108:111], v[104:107], v[4:7], v[108:111]\n\t" \
    "L_cv0_8:\n\t" \
    "s_cbranch_execz L_cv0_9\n\t" \
    "v_mfma_f32_16x16x32_bf16 v[124:127], v[28:31], v[4:7], v[124:127]\n\t" \
    "v_mfma_f32_16x16x32_bf16 v[96:99], v[104:107], v[12:15], v[96:99]\n\t" \
    "ds_read_b64 v[20:21], %[ub] offset:384\n\t" \
    "ds_read_b64 v[22:23], %[ub] offset:392\n\t" \
    "v_mfma_f32_16x16x32_bf16 v[100:103], v[28:31], v[12:15], v[100:103]\n\t" \
    "ds_read_b64 v[128:129], %[ub] offset:416\n\t" \
    "ds_read_b64 v[130:131], %[ub] offset:424\n\t" \
    "v_mfma_f32_16x16x32_bf16 v[40:43], v[64:67], v[36:39], v[40:43]\n\t" \
    "v_mfma_f32_16x16x32_bf16 v[48:51], v[72:75], v[36:39], v[48:51]\n\t" \
    "v_cndmask_b32_e64 v163, v111, v35, %[m3]\n\t" \
    "v_cndmask_b32_e64 v167, v127, v111, %[m3]\n\t" \
    "v_mfma_f32_16x16x32_bf16 v[40:43], v[116:119], v[152:155], v[40:43]\n\t" \
    "v_cndmask_b32_e64 v162, v110, v34, %[m2]\n\t" \
    "v_cndmask_b32_e64 v166, v126, v110, %[m2]\n\t" \
    "v_mfma_f32_16x16x32_bf16 v[48:51], v[120:123], v[152:155], v[48:51]\n\t" \
    "v_cndmask_b32_e64 v161, v109, v33, %[m1]\n\t" \
    "v_cndmask_b32_e64 v165, v125, v109, %[m1]\n\t" \
    "v_mfma_f32_16x16x32_bf16 v[40:43], v[136:139], v[132:135], v[40:43]\n\t" \
    "v_add_f32_dpp v162, v163, v162 wave_ror:1 row_mask:0xf bank_mask:0xf\n\t" \
    "v_add_f32_dpp v166, v167, v166 wave_ror:1 row_mask:0xf bank_mask:0xf\n\t" \
    "v_mfma_f32_16x16x32_bf16 v[48:51], v[140:143], v[132:135], v[48:51]\n\t" \
    "s_waitcnt lgkmcnt(6)\n\t" \
    "v_fmac_f32 v108, %[dd], v170\n\t" \
    "v_mfma_f32_16x16x32_bf16 v[40:43], v[156:159], v[112:115], v[40:43]\n\t" \
    "v_fmac_f32 v124, %[dd], v171\n\t" \
    "v_add_f32_dpp v161, v162, v161 wave_ror:1 row_mask:0xf bank_mask:0xf\n\t" \
    "v_mfma_f32_16x16x32_bf16 v[48:51], v[44:47], v[112:115], v[48:51]\n\t" \
    "ds_read_u16_d16_hi v172, %[ua] offset:384\n\t" \
    "ds_read_u16_d16_hi v173, %[ua] offset:416\n\t" \
    "v_mfma_f32_16x16x32_bf16 v[40:43], v[104:107], v[52:55], v[40:43]\n\t" \
    "v_add_f32_dpp v165, v166, v165 wave_ror:1 row_mask:0xf bank_mask:0xf\n\t" \
    "v_add_f32_dpp v108, v161, v108 wave_ror:1 row_mask:0xf bank_mask:0xf\n\t" \
    "L_cv0_9:\n\t" \
    "s_cbranch_execz L_cv0_10\n\t" \
    "v_mfma_f32_16x16x32_bf16 v[48:51], v[28:31], v[52:55], v[48:51]\n\t" \
    "v_add_f32_dpp v124, v165, v124 wave_ror:1 row_mask:0xf bank_mask:0xf\n\t" \
    "v_cvt_pk_bf16_f32 v160, v108, v124\n\t" \
    "s_waitcnt lgkmcnt(2)\n\t" \
    "v_mfma_f32_16x16x32_bf16 v[40:43], v[20:23], v[4:7], v[40:43]\n\t" \
    "ds_read_b64 v[144:145], %[ub] offset:448\n\t" \
    "v_mfma_f32_16x16x32_bf16 v[48:51], v[128:131], v[4:7], v[48:51]\n\t" \
    "ds_read_b64 v[146:147], %[ub] offset:456\n\t" \
    "ds_read_b64 v[16:17], %[ub] offset:480\n\t" \
    "v_mfma_f32_16x16x32_bf16 v[56:59], v[116:119], v[36:39], v[56:59]\n\t" \
    "ds_read_b64 v[18:19], %[ub] offset:488\n\t" \
    "ds_write_b16 %[ya], v160 offset:256\n\t" \
    "v_mfma_f32_16x16x32_bf16 v[60:63], v[120:123], v[36:39], v[60:63]\n\t" \
    "ds_write_b16_d16_hi %[ya], v160 offset:384\n\t" \
    "v_mfma_f32_16x16x32_bf16 v[56:59], v[136:139], v[152:155], v[56:59]\n\t" \
    "v_mfma_f32_16x16x32_bf16 v[60:63], v[140:143], v[152:155], v[60:63]\n\t" \
    "v_cndmask_b32_e64 v163, v43, v127, %[m3]\n\t" \
    "v_cndmask_b32_e64 v167, v51, v43, %[m3]\n\t" \
    "v_mfma_f32_16x16x32_bf16 v[56:59], v[156:159], v[132:135], v[56:59]\n\t" \
    "v_cndmask_b32_e64 v162, v42, v126, %[m2]\n\t" \
    "v_cndmask_b32_e64 v166, v50, v42, %[m2]\n\t" \
    "v_mfma_f32_16x16x32_bf16 v[60:63], v[44:47], v[132:135], v[60:63]\n\t" \
    "v_cndmask_b32_e64 v161, v41, v125, %[m1]\n\t" \
    "v_cndmask_b32_e64 v165, v49, v41, %[m1]\n\t" \
    "v_mfma_f32_16x16x32_bf16 v[56:59], v[104:107], v[112:115], v[56:59]\n\t" \
    "v_add_f32_dpp v162, v163, v162 wave_ror:1 row_mask:0xf bank_mask:0xf\n\t" \
    "v_add_f32_dpp v166, v167, v166 wave_ror:1 row_mask:0xf bank_mask:0xf\n\t" \
    "v_mfma_f32_16x16x32_bf16 v[60:63], v[28:31], v[112:115], v[60:63]\n\t" \
    "ds_read_u16_d16_hi v174, %[ua] offset:448\n\t" \
    "ds_read_u16_d16_hi v175, %[ua] offset:480\n\t" \
    "v_mfma_f32_16x16x32_bf16 v[56:59], v[20:23], v[52:55], v[56:59]\n\t" \
    "s_waitcnt lgkmcnt(8)\n\t" \
    "L_cv0_10:\n\t" \
    "s_cbranch_execz L_cv0_11\n\t" \
    "v_fmac_f32 v40, %[dd], v172\n\t" \
    "v_mfma_f32_16x16x32_bf16 v[60:63], v[128:131], v[52:55], v[60:63]\n\t" \
    "v_fmac_f32 v48, %[dd], v173\n\t" \
    "v_add_f32_dpp v161, v162, v161 wave_ror:1 row_mask:0xf bank_mask:0xf\n\t" \
    "s_waitcnt lgkmcnt(4)\n\t" \
    "v_mfma_f32_16x16x32_bf16 v[56:59], v[144:147], v[4:7], v[56:59]\n\t" \
    "ds_read_b64 v[0:1], %[ub] offset:512\n\t" \
    "v_mfma_f32_16x16x32_bf16 v[60:63], v[16:19], v[4:7], v[60:63]\n\t" \
    "ds_read_b64 v[2:3], %[ub] offset:520\n\t" \
    "ds_read_b64 v[8:9], %[ub] offset:544\n\t" \
    "v_mfma_f32_16x16x32_bf16 v[68:71], v[136:139], v[36:39], v[68:71]\n\t" \
    "ds_read_b64 v[10:11], %[ub] offset:552\n\t" \
    "v_add_f32_dpp v165, v166, v165 wave_ror:1 row_mask:0xf bank_mask:0xf\n\t" \
    "v_mfma_f32_16x16x32_bf16 v[76:79], v[140:143], v[36:39], v[76:79]\n\t" \
    "v_add_f32_dpp v40, v161, v40 wave_ror:1 row_mask:0xf bank_mask:0xf\n\t" \
    "v_add_f32_dpp v48, v165, v48 wave_ror:1 row_mask:0xf bank_mask:0xf\n\t" \
    "v_mfma_f32_16x16x32_bf16 v[68:71], v[156:159], v[152:155], v[68:71]\n\t" \
    "v_cvt_pk_bf16_f32 v160, v40, v48\n\t" \
    "ds_write_b16 %[ya], v160 offset:512\n\t" \
    "v_mfma_f32_16x16x32_bf16 v[76:79], v[44:47], v[152:155], v[76:79]\n\t" \
    "ds_write_b16_d16_hi %[ya], v160 offset:640\n\t" \
    "v_cndmask_b32_e64 v163, v59, v51, %[m3]\n\t" \
    "v_mfma_f32_16x16x32_bf16 v[68:71], v[104:107], v[132:135], v[68:71]\n\t" \
    "v_cndmask_b32_e64 v167, v63, v59, %[m3]\n\t" \
    "v_cndmask_b32_e64 v162, v58, v50, %[m2]\n\t" \
    "v_mfma_f32_16x16x32_bf16 v[76:79], v[28:31], v[132:135], v[76:79]\n\t" \
    "v_cndmask_b32_e64 v166, v62, v58, %[m2]\n\t" \
    "v_cndmask_b32_e64 v161, v57, v49, %[m1]\n\t" \
    "v_mfma_f32_16x16x32_bf16 v[68:71], v[20:23], v[112:115], v[68:71]\n\t" \
    "v_cndmask_b32_e64 v165, v61, v57, %[m1]\n\t" \
    "v_add_f32_dpp v162, v163, v162 wave_ror:1 row_mask:0xf bank_mask:0xf\n\t" \
    "v_mfma_f32_16x16x32_bf16 v[76:79], v[128:131], v[112:115], v[76:79]\n\t" \
    "L_cv0_11:\n\t" \
    "s_cbranch_execz L_cv0_12\n\t" \
    "ds_read_u16_d16_hi v168, %[ua] offset:512\n\t" \
    "ds_read_u16_d16_hi v169, %[ua] offset:544\n\t" \
    "v_mfma_f32_16x16x32_bf16 v[68:71], v[144:147], v[52:55], v[68:71]\n\t" \
    "v_add_f32_dpp v166, v167, v166 wave_ror:1 row_mask:0xf bank_mask:0xf\n\t" \
    "s_waitcnt lgkmcnt(8)\n\t" \
    "v_fmac_f32 v56, %[dd], v174\n\t" \
    "v_mfma_f32_16x16x32_bf16 v[76:79], v[16:19], v[52:55], v[76:79]\n\t" \
    "v_fmac_f32 v60, %[dd], v175\n\t" \
    "v_add_f32_dpp v161, v162, v161 wave_ror:1 row_mask:0xf bank_mask:0xf\n\t" \
    "s_waitcnt lgkmcnt(4)\n\t" \
    "v_mfma_f32_16x16x32_bf16 v[68:71], v[0:3], v[4:7], v[68:71]\n\t" \
    "ds_read_b64 v[148:149], %[ub] offset:576\n\t" \
    "v_mfma_f32_16x16x32_bf16 v[76:79], v[8:11], v[4:7], v[76:79]\n\t" \
    "ds_read_b64 v[150:151], %[ub] offset:584\n\t" \
    "ds_read_b64 v[24:25], %[ub] offset:608\n\t" \
    "v_mfma_f32_16x16x32_bf16 v[80:83], v[156:159], v[36:39], v[80:83]\n\t" \
    "ds_read_b64 v[26:27], %[ub] offset:616\n\t" \
    "v_add_f32_dpp v165, v166, v165 wave_ror:1 row_mask:0xf bank_mask:0xf\n\t" \
    "v_mfma_f32_16x16x32_bf16 v[84:87], v[44:47], v[36:39], v[84:87]\n\t" \
    "v_add_f32_dpp v56, v161, v56 wave_ror:1 row_mask:0xf bank_mask:0xf\n\t" \
    "v_add_f32_dpp v60, v165, v60 wave_ror:1 row_mask:0xf bank_mask:0xf\n\t" \
    "v_mfma_f32_16x16x32_bf16 v[80:83], v[104:107], v[152:155], v[80:83]\n\t" \
    "v_cvt_pk_bf16_f32 v160, v56, v60\n\t" \
    "ds_write_b16 %[ya], v160 offset:768\n\t" \
    "v_mfma_f32_16x16x32_bf16 v[84:87], v[28:31], v[152:155], v[84:87]\n\t" \
    "ds_write_b16_d16_hi %[ya], v160 offset:896\n\t" \
    "ds_read_b128 v[180:183], %[yr]\n\t" \
    "v_mfma_f32_16x16x32_bf16 v[80:83], v[20:23], v[132:135], v[80:83]\n\t" \
    "v_cndmask_b32_e64 v163, v71, v63, %[m3]\n\t" \
    "v_cndmask_b32_e64 v167, v79, v71, %[m3]\n\t" \
    "v_mfma_f32_16x16x32_bf16 v[84:87], v[128:131], v[132:135], v[84:87]\n\t" \
    "v_cndmask_b32_e64 v162, v70, v62, %[m2]\n\t" \
    "L_cv0_12:\n\t" \
    "s_cbranch_execz L_cv0_13\n\t" \
    "v_cndmask_b32_e64 v166, v78, v70, %[m2]\n\t" \
    "v_mfma_f32_16x16x32_bf16 v[80:83], v[144:147], v[112:115], v[80:83]\n\t" \
    "v_cndmask_b32_e64 v161, v69, v61, %[m1]\n\t" \
    "v_cndmask_b32_e64 v165, v77, v69, %[m1]\n\t" \
    "v_mfma_f32_16x16x32_bf16 v[84:87], v[16:19], v[112:115], v[84:87]\n\t" \
    "ds_read_u16_d16_hi v170, %[ua] offset:576\n\t" \
    "ds_read_u16_d16_hi v171, %[ua] offset:608\n\t" \
    "v_mfma_f32_16x16x32_bf16 v[80:83], v[0:3], v[52:55], v[80:83]\n\t" \
    "v_add_f32_dpp v162, v163, v162 wave_ror:1 row_mask:0xf bank_mask:0xf\n\t" \
    "v_add_f32_dpp v166, v167, v166 wave_ror:1 row_mask:0xf bank_mask:0xf\n\t" \
    "v_mfma_f32_16x16x32_bf16 v[84:87], v[8:11], v[52:55], v[84:87]\n\t" \
    "s_waitcnt lgkmcnt(9)\n\t" \
    "v_fmac_f32 v68, %[dd], v168\n\t" \
    "s_waitcnt lgkmcnt(5)\n\t" \
    "v_mfma_f32_16x16x32_bf16 v[80:83], v[148:151], v[4:7], v[80:83]\n\t" \
    "ds_read_b64 v[12:13], %[ub] offset:640\n\t" \
    "v_mfma_f32_16x16x32_bf16 v[84:87], v[24:27], v[4:7], v[84:87]\n\t" \
    "ds_read_b64 v[14:15], %[ub] offset:648\n\t" \
    "ds_read_b64 v[64:65], %[ub] offset:672\n\t" \
    "v_mfma_f32_16x16x32_bf16 v[88:91], v[104:107], v[36:39], v[88:91]\n\t" \
    "ds_read_b64 v[66:67], %[ub] offset:680\n\t" \
    "v_fmac_f32 v76, %[dd], v169\n\t" \
    "v_mfma_f32_16x16x32_bf16 v[92:95], v[28:31], v[36:39], v[92:95]\n\t" \
    "v_add_f32_dpp v161, v162, v161 wave_ror:1 row_mask:0xf bank_mask:0xf\n\t" \
    "v_add_f32_dpp v165, v166, v165 wave_ror:1 row_mask:0xf bank_mask:0xf\n\t" \
    "v_mfma_f32_16x16x32_bf16 v[88:91], v[20:23], v[152:155], v[88:91]\n\t" \
    "v_add_f32_dpp v68, v161, v68 wave_ror:1 row_mask:0xf bank_mask:0xf\n\t" \
    "v_add_f32_dpp v76, v165, v76 wave_ror:1 row_mask:0xf bank_mask:0xf\n\t" \
    "v_mfma_f32_16x16x32_bf16 v[92:95], v[128:131], v[152:155], v[92:95]\n\t" \
    "v_cvt_pk_bf16_f32 v160, v68, v76\n\t" \
    "ds_write_b16 %[ya], v160 offset:0\n\t" \
    "v_mfma_f32_16x16x32_bf16 v[88:91], v[144:147], v[132:135], v[88:91]\n\t" \
    "L_cv0_13:\n\t" \
    "s_cbranch_execz L_cv0_14\n\t" \
    "ds_write_b16_d16_hi %[ya], v160 offset:128\n\t" \
    "v_cndmask_b32_e64 v163, v83, v79, %[m3]\n\t" \
    "v_mfma_f32_16x16x32_bf16 v[92:95], v[16:19], v[132:135], v[92:95]\n\t" \
    "v_cndmask_b32_e64 v167, v87, v83, %[m3]\n\t" \
    "v_cndmask_b32_e64 v162, v82, v78, %[m2]\n\t" \
    "v_mfma_f32_16x16x32_bf16 v[88:91], v[0:3], v[112:115], v[88:91]\n\t" \
    "v_cndmask_b32_e64 v166, v86, v82, %[m2]\n\t" \
    "v_cndmask_b32_e64 v161, v81, v77, %[m1]\n\t" \
    "v_mfma_f32_16x16x32_bf16 v[92:95], v[8:11], v[112:115], v[92:95]\n\t" \
    "ds_read_u16_d16_hi v172, %[ua] offset:640\n\t" \
    "ds_read_u16_d16_hi v173, %[ua] offset:672\n\t" \
    "v_mfma_f32_16x16x32_bf16 v[88:91], v[148:151], v[52:55], v[88:91]\n\t" \
    "v_cndmask_b32_e64 v165, v85, v81, %[m1]\n\t" \
    "v_add_f32_dpp v162, v163, v162 wave_ror:1 row_mask:0xf bank_mask:0xf\n\t" \
    "v_mfma_f32_16x16x32_bf16 v[92:95], v[24:27], v[52:55], v[92:95]\n\t" \
    "v_add_f32_dpp v166, v167, v166 wave_ror:1 row_mask:0xf bank_mask:0xf\n\t" \
    "s_waitcnt lgkmcnt(8)\n\t" \
    "v_fmac_f32 v80, %[dd], v170\n\t" \
    "s_waitcnt lgkmcnt(4)\n\t" \
    "v_mfma_f32_16x16x32_bf16 v[88:91], v[12:15], v[4:7], v[88:91]\n\t" \
    "ds_read_b64 v[72:73], %[ub] offset:704\n\t" \
    "v_mfma_f32_16x16x32_bf16 v[92:95], v[64:67], v[4:7], v[92:95]\n\t" \
    "ds_read_b64 v[74:75], %[ub] offset:712\n\t" \
    "ds_read_b64 v[32:33], %[ub] offset:736\n\t" \
    "v_mfma_f32_16x16x32_bf16 v[96:99], v[20:23], v[36:39], v[96:99]\n\t" \
    "ds_read_b64 v[34:35], %[ub] offset:744\n\t" \
    "v_fmac_f32 v84, %[dd], v171\n\t" \
    "v_mfma_f32_16x16x32_bf16 v[100:103], v[128:131], v[36:39], v[100:103]\n\t" \
    "v_add_f32_dpp v161, v162, v161 wave_ror:1 row_mask:0xf bank_mask:0xf\n\t" \
    "v_add_f32_dpp v165, v166, v165 wave_ror:1 row_mask:0xf bank_mask:0xf\n\t" \
    "v_mfma_f32_16x16x32_bf16 v[96:99], v[144:147], v[152:155], v[96:99]\n\t" \
    "v_add_f32_dpp v80, v161, v80 wave_ror:1 row_mask:0xf bank_mask:0xf\n\t" \
    "L_cv0_14:\n\t" \
    "s_cbranch_execz L_cv0_15\n\t" \
    "v_add_f32_dpp v84, v165, v84 wave_ror:1 row_mask:0xf bank_mask:0xf\n\t" \
    "v_mfma_f32_16x16x32_bf16 v[100:103], v[16:19], v[152:155], v[100:103]\n\t" \
    "v_cvt_pk_bf16_f32 v160, v80, v84\n\t" \
    "ds_write_b16 %[ya], v160 offset:256\n\t" \
    "v_mfma_f32_16x16x32_bf16 v[96:99], v[0:3], v[132:135], v[96:99]\n\t" \
    "ds_write_b16_d16_hi %[ya], v160 offset:384\n\t" \
    "v_cndmask_b32_e64 v163, v91, v87, %[m3]\n\t" \
    "v_mfma_f32_16x16x32_bf16 v[100:103], v[8:11], v[132:135], v[100:103]\n\t" \
    "v_cndmask_b32_e64 v167, v95, v91, %[m3]\n\t" \
    "v_cndmask_b32_e64 v162, v90, v86, %[m2]\n\t" \
    "v_mfma_f32_16x16x32_bf16 v[96:99], v[148:151], v[112:115], v[96:99]\n\t" \
    "v_cndmask_b32_e64 v166, v94, v90, %[m2]\n\t" \
    "v_cndmask_b32_e64 v161, v89, v85, %[m1]\n\t" \
    "v_mfma_f32_16x16x32_bf16 v[100:103], v[24:27], v[112:115], v[100:103]\n\t" \
    "ds_read_u16_d16_hi v174, %[ua] offset:704\n\t" \
    "ds_read_u16_d16_hi v175, %[ua] offset:736\n\t" \
    "v_mfma_f32_16x16x32_bf16 v[96:99], v[12:15], v[52:55], v[96:99]\n\t" \
    "v_cndmask_b32_e64 v165, v93, v89, %[m1]\n\t" \
    "v_add_f32_dpp v162, v163, v162 wave_ror:1 row_mask:0xf bank_mask:0xf\n\t" \
    "v_mfma_f32_16x16x32_bf16 v[100:103], v[64:67], v[52:55], v[100:103]\n\t" \
    "v_add_f32_dpp v166, v167, v166 wave_ror:1 row_mask:0xf bank_mask:0xf\n\t" \
    "s_waitcnt lgkmcnt(8)\n\t" \
    "v_fmac_f32 v88, %[dd], v172\n\t" \
    "s_waitcnt lgkmcnt(4)\n\t" \
    "v_mfma_f32_16x16x32_bf16 v[96:99], v[72:75], v[4:7], v[96:99]\n\t" \
    "v_fmac_f32 v92, %[dd], v173\n\t" \
    "v_mfma_f32_16x16x32_bf16 v[100:103], v[32:35], v[4:7], v[100:103]\n\t" \
    "v_add_f32_dpp v161, v162, v161 wave_ror:1 row_mask:0xf bank_mask:0xf\n\t" \
    "v_add_f32_dpp v165, v166, v165 wave_ror:1 row_mask:0xf bank_mask:0xf\n\t" \
    "s_nop 0\n\t" \
    "v_add_f32_dpp v88, v161, v88 wave_ror:1 row_mask:0xf bank_mask:0xf\n\t" \
    "v_add_f32_dpp v92, v165, v92 wave_ror:1 row_mask:0xf bank_mask:0xf\n\t" \
    "v_cvt_pk_bf16_f32 v160, v88, v92\n\t" \
    "L_cv0_15:\n\t" \
    "s_cbranch_execz L_cv0_16\n\t" \
    "ds_write_b16 %[ya], v160 offset:512\n\t" \
    "ds_write_b16_d16_hi %[ya], v160 offset:640\n\t" \
    "v_cndmask_b32_e64 v163, v99, v95, %[m3]\n\t" \
    "v_cndmask_b32_e64 v167, v103, v99, %[m3]\n\t" \
    "v_cndmask_b32_e64 v162, v98, v94, %[m2]\n\t" \
    "v_cndmask_b32_e64 v166, v102, v98, %[m2]\n\t" \
    "v_cndmask_b32_e64 v161, v97, v93, %[m1]\n\t" \
    "v_cndmask_b32_e64 v165, v101, v97, %[m1]\n\t" \
    "v_add_f32_dpp v162, v163, v162 wave_ror:1 row_mask:0xf bank_mask:0xf\n\t" \
    "v_add_f32_dpp v166, v167, v166 wave_ror:1 row_mask:0xf bank_mask:0xf\n\t" \
    "s_waitcnt lgkmcnt(2)\n\t" \
    "v_fmac_f32 v96, %[dd], v174\n\t" \
    "v_fmac_f32 v100, %[dd], v175\n\t" \
    "v_add_f32_dpp v161, v162, v161 wave_ror:1 row_mask:0xf bank_mask:0xf\n\t" \
    "v_add_f32_dpp v165, v166, v165 wave_ror:1 row_mask:0xf bank_mask:0xf\n\t" \
    "s_nop 0\n\t" \
    "v_add_f32_dpp v96, v161, v96 wave_ror:1 row_mask:0xf bank_mask:0xf\n\t" \
    "v_add_f32_dpp v100, v165, v100 wave_ror:1 row_mask:0xf bank_mask:0xf\n\t" \
    "v_cvt_pk_bf16_f32 v160, v96, v100\n\t" \
    "ds_write_b16 %[ya], v160 offset:768\n\t" \
    "ds_write_b16_d16_hi %[ya], v160 offset:896\n\t" \
    "ds_read_b128 v[184:187], %[yr]\n\t" \
    "s_waitcnt vmcnt(0)\n\t" \
    "buffer_store_dwordx4 v[176:179], %[l16], %[rs], %[yo] offen offset:0 sc1\n\t" \
    "buffer_store_dwordx4 v[180:183], %[l16], %[rs], %[yo] offen offset:1024 sc1\n\t" \
    "s_waitcnt lgkmcnt(0)\n\t" \
    "buffer_store_dwordx4 v[184:187], %[l16], %[rs], %[yo] offen offset:2048 sc1\n\t" \
    "s_nop 1\n\t" \
    "L_cv0_16:\n\t" \
    "s_cbranch_execz L_back\n\t" \
    "s_branch CVE_%=\n\t" \
    "L_cv1_0:\n\t" \
    "s_cbranch_execz L_cv1_1\n\t" \
    "ds_read_b64 v[0:1], %[ub] offset:0\n\t" \
    "ds_read_b64 v[2:3], %[ub] offset:8\n\t" \
    "ds_read_b64_tr_b16 v[4:5], %[hb] offset:32\n\t" \
    "ds_read_b64_tr_b16 v[6:7], %[hb] offset:0\n\t" \
    "ds_read_b64 v[8:9], %[ub] offset:32\n\t" \
    "ds_read_b64 v[10:11], %[ub] offset:40\n\t" \
    "ds_read_b64_tr_b16 v[12:13], %[hb] offset:800\n\t" \
    "ds_read_b64_tr_b16 v[14:15], %[hb] offset:768\n\t" \
    "ds_read_b64_tr_b16 v[16:17], %[hb] offset:1056\n\t" \
    "ds_read_b64_tr_b16 v[18:19], %[hb] offset:1024\n\t" \
    "ds_read_b64_tr_b16 v[20:21], %[hb] offset:1312\n\t" \
    "ds_read_b64_tr_b16 v[22:23], %[hb] offset:1280\n\t" \
    "v_mov_b32 v168, 0\n\t" \
    "v_mov_b32 v169, 0\n\t" \
    "v_mov_b32 v170, 0\n\t" \
    "v_mov_b32 v171, 0\n\t" \
    "v_mov_b32 v172, 0\n\t" \
    "v_mov_b32 v173, 0\n\t" \
    "v_mov_b32 v174, 0\n\t" \
    "v_mov_b32 v175, 0\n\t" \
    "s_waitcnt lgkmcnt(8)\n\t" \
    "v_mfma_f32_16x16x32_bf16 v[24:27], v[0:3], v[4:7], 0\n\t" \
    "ds_read_b64_tr_b16 v[28:29], %[hb] offset:288\n\t" \
    "ds_read_b64_tr_b16 v[30:31], %[hb] offset:256\n\t" \
    "ds_read_b64 v[32:33], %[ub] offset:64\n\t" \
    "s_waitcnt lgkmcnt(9)\n\t" \
    "v_mfma_f32_16x16x32_bf16 v[36:39], v[8:11], v[4:7], 0\n\t" \
    "ds_read_b64 v[34:35], %[ub] offset:72\n\t" \
    "ds_read_b64 v[40:41], %[ub] offset:96\n\t" \
    "ds_read_b64 v[42:43], %[ub] offset:104\n\t" \
    "ds_read_u16_d16_hi v168, %[ua] offset:0\n\t" \
    "ds_read_u16_d16_hi v169, %[ua] offset:32\n\t" \
    "s_waitcnt lgkmcnt(12)\n\t" \
    "L_cv1_1:\n\t" \
    "s_cbranch_execz L_cv1_2\n\t" \
    "v_mfma_f32_16x16x32_bf16 v[44:47], v[0:3], v[12:15], 0\n\t" \
    "ds_read_b64_tr_b16 v[48:49], %[hb] offset:544\n\t" \
    "v_mfma_f32_16x16x32_bf16 v[52:55], v[8:11], v[12:15], 0\n\t" \
    "ds_read_b64_tr_b16 v[50:51], %[hb] offset:512\n\t" \
    "s_waitcnt lgkmcnt(12)\n\t" \
    "v_mfma_f32_16x16x32_bf16 v[56:59], v[0:3], v[16:19], 0\n\t" \
    "v_mfma_f32_16x16x32_bf16 v[60:63], v[8:11], v[16:19], 0\n\t" \
    "v_cndmask_b32_e64 v163, v27, 0, %[m3]\n\t" \
    "v_cndmask_b32_e64 v167, v39, v27, %[m3]\n\t" \
    "s_waitcnt lgkmcnt(10)\n\t" \
    "v_mfma_f32_16x16x32_bf16 v[64:67], v[0:3], v[20:23], 0\n\t" \
    "ds_read_b64 v[68:69], %[ub] offset:128\n\t" \
    "s_waitcnt lgkmcnt(9)\n\t" \
    "v_mfma_f32_16x16x32_bf16 v[72:75], v[0:3], v[28:31], 0\n\t" \
    "ds_read_b64 v[70:71], %[ub] offset:136\n\t" \
    "v_mfma_f32_16x16x32_bf16 v[76:79], v[8:11], v[28:31], 0\n\t" \
    "ds_read_b64 v[80:81], %[ub] offset:160\n\t" \
    "ds_read_b64 v[82:83], %[ub] offset:168\n\t" \
    "s_waitcnt lgkmcnt(8)\n\t" \
    "v_mfma_f32_16x16x32_bf16 v[72:75], v[32:35], v[4:7], v[72:75]\n\t" \
    "ds_read_u16_d16_hi v170, %[ua] offset:64\n\t" \
    "v_mfma_f32_16x16x32_bf16 v[76:79], v[40:43], v[4:7], v[76:79]\n\t" \
    "ds_read_u16_d16_hi v171, %[ua] offset:96\n\t" \
    "v_cndmask_b32_e64 v162, v26, 0, %[m2]\n\t" \
    "v_mfma_f32_16x16x32_bf16 v[56:59], v[32:35], v[12:15], v[56:59]\n\t" \
    "v_cndmask_b32_e64 v166, v38, v26, %[m2]\n\t" \
    "v_cndmask_b32_e64 v161, v25, 0, %[m1]\n\t" \
    "v_mfma_f32_16x16x32_bf16 v[60:63], v[40:43], v[12:15], v[60:63]\n\t" \
    "v_cndmask_b32_e64 v165, v37, v25, %[m1]\n\t" \
    "v_add_f32_dpp v162, v163, v162 wave_ror:1 row_mask:0xf bank_mask:0xf\n\t" \
    "v_mfma_f32_16x16x32_bf16 v[64:67], v[32:35], v[16:19], v[64:67]\n\t" \
    "ds_read_b64 v[84:85], %[ub] offset:192\n\t" \
    "ds_read_b64 v[86:87], %[ub] offset:200\n\t" \
    "s_waitcnt lgkmcnt(8)\n\t" \
    "L_cv1_2:\n\t" \
    "s_cbranch_execz L_cv1_3\n\t" \
    "v_mfma_f32_16x16x32_bf16 v[88:91], v[0:3], v[48:51], 0\n\t" \
    "ds_read_b64 v[92:93], %[ub] offset:224\n\t" \
    "v_mfma_f32_16x16x32_bf16 v[96:99], v[8:11], v[48:51], 0\n\t" \
    "ds_read_b64 v[94:95], %[ub] offset:232\n\t" \
    "ds_read_u16_d16_hi v172, %[ua] offset:128\n\t" \
    "v_mfma_f32_16x16x32_bf16 v[88:91], v[32:35], v[28:31], v[88:91]\n\t" \
    "ds_read_u16_d16_hi v173, %[ua] offset:160\n\t" \
    "v_add_f32_dpp v166, v167, v166 wave_ror:1 row_mask:0xf bank_mask:0xf\n\t" \
    "v_mfma_f32_16x16x32_bf16 v[96:99], v[40:43], v[28:31], v[96:99]\n\t" \
    "v_fmac_f32 v24, %[dd], v168\n\t" \
    "v_fmac_f32 v36, %[dd], v169\n\t" \
    "s_waitcnt lgkmcnt(8)\n\t" \
    "v_mfma_f32_16x16x32_bf16 v[88:91], v[68:71], v[4:7], v[88:91]\n\t" \
    "v_add_f32_dpp v161, v162, v161 wave_ror:1 row_mask:0xf bank_mask:0xf\n\t" \
    "v_mfma_f32_16x16x32_bf16 v[96:99], v[80:83], v[4:7], v[96:99]\n\t" \
    "ds_read_b64 v[100:101], %[ub] offset:256\n\t" \
    "ds_read_b64 v[102:103], %[ub] offset:264\n\t" \
    "v_mfma_f32_16x16x32_bf16 v[64:67], v[68:71], v[12:15], v[64:67]\n\t" \
    "ds_read_b64 v[104:105], %[ub] offset:288\n\t" \
    "ds_read_b64 v[106:107], %[ub] offset:296\n\t" \
    "v_mfma_f32_16x16x32_bf16 v[44:47], v[32:35], v[48:51], v[44:47]\n\t" \
    "v_add_f32_dpp v165, v166, v165 wave_ror:1 row_mask:0xf bank_mask:0xf\n\t" \
    "v_add_f32_dpp v24, v161, v24 wave_ror:1 row_mask:0xf bank_mask:0xf\n\t" \
    "v_mfma_f32_16x16x32_bf16 v[52:55], v[40:43], v[48:51], v[52:55]\n\t" \
    "ds_read_u16_d16_hi v174, %[ua] offset:192\n\t" \
    "ds_read_u16_d16_hi v175, %[ua] offset:224\n\t" \
    "v_mfma_f32_16x16x32_bf16 v[44:47], v[68:71], v[28:31], v[44:47]\n\t" \
    "ds_read_b64 v[108:109], %[ub] offset:320\n\t" \
    "ds_read_b64 v[110:111], %[ub] offset:328\n\t" \
    "v_mfma_f32_16x16x32_bf16 v[52:55], v[80:83], v[28:31], v[52:55]\n\t" \
    "ds_read_b64 v[112:113], %[ub2] offset:32\n\t" \
    "ds_read_b64 v[114:115], %[ub2] offset:40\n\t" \
    "L_cv1_3:\n\t" \
    "s_cbranch_execz L_cv1_4\n\t" \
    "s_waitcnt lgkmcnt(12)\n\t" \
    "v_mfma_f32_16x16x32_bf16 v[44:47], v[84:87], v[4:7], v[44:47]\n\t" \
    "ds_read_b64_tr_b16 v[116:117], %[hb2] offset:2848\n\t" \
    "v_mfma_f32_16x16x32_bf16 v[52:55], v[92:95], v[4:7], v[52:55]\n\t" \
    "ds_read_b64_tr_b16 v[118:119], %[hb2] offset:2816\n\t" \
    "ds_read_b64 v[120:121], %[ub2] offset:0\n\t" \
    "v_mfma_f32_16x16x32_bf16 v[56:59], v[68:71], v[48:51], v[56:59]\n\t" \
    "ds_read_b64 v[122:123], %[ub2] offset:8\n\t" \
    "ds_read_b64_tr_b16 v[124:125], %[hb2] offset:3104\n\t" \
    "v_mfma_f32_16x16x32_bf16 v[60:63], v[80:83], v[48:51], v[60:63]\n\t" \
    "ds_read_b64_tr_b16 v[126:127], %[hb2] offset:3072\n\t" \
    "ds_read_b64_tr_b16 v[128:129], %[hb2] offset:3360\n\t" \
    "v_mfma_f32_16x16x32_bf16 v[56:59], v[84:87], v[28:31], v[56:59]\n\t" \
    "ds_read_b64_tr_b16 v[130:131], %[hb2] offset:3328\n\t" \
    "ds_read_b64_tr_b16 v[132:133], %[hb2] offset:3616\n\t" \
    "v_mfma_f32_16x16x32_bf16 v[60:63], v[92:95], v[28:31], v[60:63]\n\t" \
    "ds_read_b64_tr_b16 v[134:135], %[hb2] offset:3584\n\t" \
    "v_add_f32_dpp v36, v165, v36 wave_ror:1 row_mask:0xf bank_mask:0xf\n\t" \
    "s_waitcnt lgkmcnt(14)\n\t" \
    "v_mfma_f32_16x16x32_bf16 v[56:59], v[100:103], v[4:7], v[56:59]\n\t" \
    "ds_read_b64_tr_b16 v[136:137], %[hb2] offset:3872\n\t" \
    "v_mfma_f32_16x16x32_bf16 v[60:63], v[104:107], v[4:7], v[60:63]\n\t" \
    "ds_read_b64_tr_b16 v[138:139], %[hb2] offset:3840\n\t" \
    "v_cvt_pk_bf16_f32 v160, v24, v36\n\t" \
    "v_mfma_f32_16x16x32_bf16 v[64:67], v[84:87], v[48:51], v[64:67]\n\t" \
    "ds_read_b64 v[140:141], %[ub2] offset:96\n\t" \
    "ds_read_b64 v[142:143], %[ub2] offset:104\n\t" \
    "ds_read_u16_d16_hi v168, %[ua] offset:256\n\t" \
    "ds_read_u16_d16_hi v169, %[ua] offset:288\n\t" \
    "v_mfma_f32_16x16x32_bf16 v[64:67], v[100:103], v[28:31], v[64:67]\n\t" \
    "ds_read_b64_tr_b16 v[144:145], %[hb2] offset:2592\n\t" \
    "ds_read_b64_tr_b16 v[146:147], %[hb2] offset:2560\n\t" \
    "s_waitcnt lgkmcnt(14)\n\t" \
    "L_cv1_4:\n\t" \
    "s_cbranch_execz L_cv1_5\n\t" \
    "v_mfma_f32_16x16x32_bf16 v[64:67], v[108:111], v[4:7], v[64:67]\n\t" \
    "ds_read_b64 v[148:149], %[ub2] offset:64\n\t" \
    "v_mfma_f32_16x16x32_bf16 v[152:155], v[112:115], v[116:119], 0\n\t" \
    "ds_read_b64 v[150:151], %[ub2] offset:72\n\t" \
    "ds_write_b16 %[ya], v160 offset:0\n\t" \
    "s_waitcnt lgkmcnt(14)\n\t" \
    "v_mfma_f32_16x16x32_bf16 v[156:159], v[120:123], v[124:127], 0\n\t" \
    "ds_write_b16_d16_hi %[ya], v160 offset:128\n\t" \
    "v_mfma_f32_16x16x32_bf16 v[20:23], v[112:115], v[124:127], 0\n\t" \
    "v_cndmask_b32_e64 v163, v75, v39, %[m3]\n\t" \
    "v_cndmask_b32_e64 v167, v79, v75, %[m3]\n\t" \
    "s_waitcnt lgkmcnt(14)\n\t" \
    "v_mfma_f32_16x16x32_bf16 v[16:19], v[120:123], v[128:131], 0\n\t" \
    "v_cndmask_b32_e64 v162, v74, v38, %[m2]\n\t" \
    "v_mfma_f32_16x16x32_bf16 v[0:3], v[112:115], v[128:131], 0\n\t" \
    "v_cndmask_b32_e64 v166, v78, v74, %[m2]\n\t" \
    "v_cndmask_b32_e64 v161, v73, v37, %[m1]\n\t" \
    "s_waitcnt lgkmcnt(12)\n\t" \
    "v_mfma_f32_16x16x32_bf16 v[8:11], v[120:123], v[132:135], 0\n\t" \
    "v_cndmask_b32_e64 v165, v77, v73, %[m1]\n\t" \
    "v_mfma_f32_16x16x32_bf16 v[12:15], v[112:115], v[132:135], 0\n\t" \
    "ds_read_b64 v[32:33], %[ub2] offset:160\n\t" \
    "ds_read_b64 v[34:35], %[ub2] offset:168\n\t" \
    "s_waitcnt lgkmcnt(12)\n\t" \
    "v_mfma_f32_16x16x32_bf16 v[40:43], v[120:123], v[136:139], 0\n\t" \
    "ds_read_b64_tr_b16 v[68:69], %[hb2] offset:2336\n\t" \
    "v_mfma_f32_16x16x32_bf16 v[80:83], v[112:115], v[136:139], 0\n\t" \
    "ds_read_b64_tr_b16 v[70:71], %[hb2] offset:2304\n\t" \
    "ds_read_b64 v[92:93], %[ub2] offset:128\n\t" \
    "s_waitcnt lgkmcnt(7)\n\t" \
    "v_mfma_f32_16x16x32_bf16 v[152:155], v[140:143], v[144:147], v[152:155]\n\t" \
    "ds_read_b64 v[94:95], %[ub2] offset:136\n\t" \
    "v_mfma_f32_16x16x32_bf16 v[156:159], v[148:151], v[116:119], v[156:159]\n\t" \
    "v_add_f32_dpp v162, v163, v162 wave_ror:1 row_mask:0xf bank_mask:0xf\n\t" \
    "L_cv1_5:\n\t" \
    "s_cbranch_execz L_cv1_6\n\t" \
    "v_add_f32_dpp v166, v167, v166 wave_ror:1 row_mask:0xf bank_mask:0xf\n\t" \
    "v_mfma_f32_16x16x32_bf16 v[20:23], v[140:143], v[116:119], v[20:23]\n\t" \
    "v_fmac_f32 v72, %[dd], v170\n\t" \
    "v_fmac_f32 v76, %[dd], v171\n\t" \
    "v_mfma_f32_16x16x32_bf16 v[16:19], v[148:151], v[124:127], v[16:19]\n\t" \
    "v_add_f32_dpp v161, v162, v161 wave_ror:1 row_mask:0xf bank_mask:0xf\n\t" \
    "v_add_f32_dpp v165, v166, v165 wave_ror:1 row_mask:0xf bank_mask:0xf\n\t" \
    "v_mfma_f32_16x16x32_bf16 v[0:3], v[140:143], v[124:127], v[0:3]\n\t" \
    "v_add_f32_dpp v72, v161, v72 wave_ror:1 row_mask:0xf bank_mask:0xf\n\t" \
    "v_add_f32_dpp v76, v165, v76 wave_ror:1 row_mask:0xf bank_mask:0xf\n\t" \
    "v_mfma_f32_16x16x32_bf16 v[8:11], v[148:151], v[128:131], v[8:11]\n\t" \
    "v_cvt_pk_bf16_f32 v160, v72, v76\n\t" \
    "ds_write_b16 %[ya], v160 offset:256\n\t" \
    "v_mfma_f32_16x16x32_bf16 v[12:15], v[140:143], v[128:131], v[12:15]\n\t" \
    "ds_read_b64 v[24:25], %[ub2] offset:224\n\t" \
    "ds_read_b64 v[26:27], %[ub2] offset:232\n\t" \
    "ds_read_u16_d16_hi v170, %[ua] offset:320\n\t" \
    "v_mfma_f32_16x16x32_bf16 v[40:43], v[148:151], v[132:135], v[40:43]\n\t" \
    "ds_read_b64_tr_b16 v[104:105], %[hb2] offset:2080\n\t" \
    "ds_read_b64_tr_b16 v[106:107], %[hb2] offset:2048\n\t" \
    "v_mfma_f32_16x16x32_bf16 v[80:83], v[140:143], v[132:135], v[80:83]\n\t" \
    "ds_read_b64 v[84:85], %[ub2] offset:192\n\t" \
    "ds_read_b64 v[86:87], %[ub2] offset:200\n\t" \
    "s_waitcnt lgkmcnt(8)\n\t" \
    "v_mfma_f32_16x16x32_bf16 v[152:155], v[32:35], v[68:71], v[152:155]\n\t" \
    "ds_write_b16_d16_hi %[ya], v160 offset:384\n\t" \
    "v_mfma_f32_16x16x32_bf16 v[156:159], v[92:95], v[144:147], v[156:159]\n\t" \
    "v_cndmask_b32_e64 v163, v91, v79, %[m3]\n\t" \
    "v_cndmask_b32_e64 v167, v99, v91, %[m3]\n\t" \
    "v_mfma_f32_16x16x32_bf16 v[20:23], v[32:35], v[144:147], v[20:23]\n\t" \
    "v_cndmask_b32_e64 v162, v90, v78, %[m2]\n\t" \
    "v_cndmask_b32_e64 v166, v98, v90, %[m2]\n\t" \
    "L_cv1_6:\n\t" \
    "s_cbranch_execz L_cv1_7\n\t" \
    "v_mfma_f32_16x16x32_bf16 v[16:19], v[92:95], v[116:119], v[16:19]\n\t" \
    "v_cndmask_b32_e64 v161, v89, v77, %[m1]\n\t" \
    "v_cndmask_b32_e64 v165, v97, v89, %[m1]\n\t" \
    "v_mfma_f32_16x16x32_bf16 v[0:3], v[32:35], v[116:119], v[0:3]\n\t" \
    "v_add_f32_dpp v162, v163, v162 wave_ror:1 row_mask:0xf bank_mask:0xf\n\t" \
    "v_add_f32_dpp v166, v167, v166 wave_ror:1 row_mask:0xf bank_mask:0xf\n\t" \
    "v_mfma_f32_16x16x32_bf16 v[8:11], v[92:95], v[124:127], v[8:11]\n\t" \
    "v_fmac_f32 v88, %[dd], v172\n\t" \
    "v_fmac_f32 v96, %[dd], v173\n\t" \
    "v_mfma_f32_16x16x32_bf16 v[12:15], v[32:35], v[124:127], v[12:15]\n\t" \
    "ds_read_b64 v[48:49], %[ub2] offset:288\n\t" \
    "ds_read_b64 v[50:51], %[ub2] offset:296\n\t" \
    "v_mfma_f32_16x16x32_bf16 v[40:43], v[92:95], v[128:131], v[40:43]\n\t" \
    "ds_read_b64_tr_b16 v[100:101], %[hb2] offset:1824\n\t" \
    "ds_read_b64_tr_b16 v[102:103], %[hb2] offset:1792\n\t" \
    "v_mfma_f32_16x16x32_bf16 v[80:83], v[32:35], v[128:131], v[80:83]\n\t" \
    "ds_read_b64 v[28:29], %[ub2] offset:256\n\t" \
    "ds_read_b64 v[30:31], %[ub2] offset:264\n\t" \
    "s_waitcnt lgkmcnt(7)\n\t" \
    "v_mfma_f32_16x16x32_bf16 v[152:155], v[24:27], v[104:107], v[152:155]\n\t" \
    "v_add_f32_dpp v161, v162, v161 wave_ror:1 row_mask:0xf bank_mask:0xf\n\t" \
    "v_mfma_f32_16x16x32_bf16 v[156:159], v[84:87], v[68:71], v[156:159]\n\t" \
    "v_add_f32_dpp v165, v166, v165 wave_ror:1 row_mask:0xf bank_mask:0xf\n\t" \
    "v_add_f32_dpp v88, v161, v88 wave_ror:1 row_mask:0xf bank_mask:0xf\n\t" \
    "v_mfma_f32_16x16x32_bf16 v[20:23], v[24:27], v[68:71], v[20:23]\n\t" \
    "v_add_f32_dpp v96, v165, v96 wave_ror:1 row_mask:0xf bank_mask:0xf\n\t" \
    "v_cvt_pk_bf16_f32 v160, v88, v96\n\t" \
    "v_mfma_f32_16x16x32_bf16 v[16:19], v[84:87], v[144:147], v[16:19]\n\t" \
    "ds_write_b16 %[ya], v160 offset:512\n\t" \
    "ds_write_b16_d16_hi %[ya], v160 offset:640\n\t" \
    "v_mfma_f32_16x16x32_bf16 v[0:3], v[24:27], v[144:147], v[0:3]\n\t" \
    "ds_read_b128 v[176:179], %[yr]\n\t" \
    "L_cv1_7:\n\t" \
    "s_cbranch_execz L_cv1_8\n\t" \
    "v_cndmask_b32_e64 v163, v47, v99, %[m3]\n\t" \
    "v_mfma_f32_16x16x32_bf16 v[8:11], v[84:87], v[116:119], v[8:11]\n\t" \
    "v_cndmask_b32_e64 v167, v55, v47, %[m3]\n\t" \
    "v_cndmask_b32_e64 v162, v46, v98, %[m2]\n\t" \
    "v_mfma_f32_16x16x32_bf16 v[12:15], v[24:27], v[116:119], v[12:15]\n\t" \
    "ds_read_b64 v[108:109], %[ub2] offset:352\n\t" \
    "ds_read_b64 v[110:111], %[ub2] offset:360\n\t" \
    "v_mfma_f32_16x16x32_bf16 v[40:43], v[84:87], v[124:127], v[40:43]\n\t" \
    "ds_read_b64_tr_b16 v[4:5], %[hb2] offset:1568\n\t" \
    "ds_read_b64_tr_b16 v[6:7], %[hb2] offset:1536\n\t" \
    "v_mfma_f32_16x16x32_bf16 v[80:83], v[24:27], v[124:127], v[80:83]\n\t" \
    "ds_read_b64 v[120:121], %[ub2] offset:320\n\t" \
    "ds_read_b64 v[122:123], %[ub2] offset:328\n\t" \
    "s_waitcnt lgkmcnt(9)\n\t" \
    "v_mfma_f32_16x16x32_bf16 v[152:155], v[48:51], v[100:103], v[152:155]\n\t" \
    "v_cndmask_b32_e64 v166, v54, v46, %[m2]\n\t" \
    "v_mfma_f32_16x16x32_bf16 v[156:159], v[28:31], v[104:107], v[156:159]\n\t" \
    "v_cndmask_b32_e64 v161, v45, v97, %[m1]\n\t" \
    "v_cndmask_b32_e64 v165, v53, v45, %[m1]\n\t" \
    "v_mfma_f32_16x16x32_bf16 v[20:23], v[48:51], v[104:107], v[20:23]\n\t" \
    "v_add_f32_dpp v162, v163, v162 wave_ror:1 row_mask:0xf bank_mask:0xf\n\t" \
    "v_add_f32_dpp v166, v167, v166 wave_ror:1 row_mask:0xf bank_mask:0xf\n\t" \
    "v_mfma_f32_16x16x32_bf16 v[16:19], v[28:31], v[68:71], v[16:19]\n\t" \
    "v_fmac_f32 v44, %[dd], v174\n\t" \
    "v_fmac_f32 v52, %[dd], v175\n\t" \
    "v_mfma_f32_16x16x32_bf16 v[0:3], v[48:51], v[68:71], v[0:3]\n\t" \
    "v_add_f32_dpp v161, v162, v161 wave_ror:1 row_mask:0xf bank_mask:0xf\n\t" \
    "v_add_f32_dpp v165, v166, v165 wave_ror:1 row_mask:0xf bank_mask:0xf\n\t" \
    "v_mfma_f32_16x16x32_bf16 v[8:11], v[28:31], v[144:147], v[8:11]\n\t" \
    "v_add_f32_dpp v44, v161, v44 wave_ror:1 row_mask:0xf bank_mask:0xf\n\t" \
    "v_add_f32_dpp v52, v165, v52 wave_ror:1 row_mask:0xf bank_mask:0xf\n\t" \
    "v_mfma_f32_16x16x32_bf16 v[12:15], v[48:51], v[144:147], v[12:15]\n\t" \
    "L_cv1_8:\n\t" \
    "s_cbranch_execz L_cv1_9\n\t" \
    "ds_read_b64 v[112:113], %[ub2] offset:416\n\t" \
    "ds_read_b64 v[114:115], %[ub2] offset:424\n\t" \
    "v_mfma_f32_16x16x32_bf16 v[40:43], v[28:31], v[116:119], v[40:43]\n\t" \
    "ds_read_b64_tr_b16 v[136:137], %[hb2] offset:1312\n\t" \
    "ds_read_b64_tr_b16 v[138:139], %[hb2] offset:1280\n\t" \
    "v_mfma_f32_16x16x32_bf16 v[80:83], v[48:51], v[116:119], v[80:83]\n\t" \
    "ds_read_b64 v[36:37], %[ub2] offset:384\n\t" \
    "ds_read_b64 v[38:39], %[ub2] offset:392\n\t" \
    "s_waitcnt lgkmcnt(6)\n\t" \
    "v_mfma_f32_16x16x32_bf16 v[152:155], v[108:111], v[4:7], v[152:155]\n\t" \
    "v_cvt_pk_bf16_f32 v160, v44, v52\n\t" \
    "v_mfma_f32_16x16x32_bf16 v[156:159], v[120:123], v[100:103], v[156:159]\n\t" \
    "ds_write_b16 %[ya], v160 offset:0\n\t" \
    "ds_write_b16_d16_hi %[ya], v160 offset:128\n\t" \
    "v_mfma_f32_16x16x32_bf16 v[20:23], v[108:111], v[100:103], v[20:23]\n\t" \
    "v_cndmask_b32_e64 v163, v59, v55, %[m3]\n\t" \
    "v_cndmask_b32_e64 v167, v63, v59, %[m3]\n\t" \
    "v_mfma_f32_16x16x32_bf16 v[16:19], v[120:123], v[104:107], v[16:19]\n\t" \
    "v_cndmask_b32_e64 v162, v58, v54, %[m2]\n\t" \
    "v_cndmask_b32_e64 v166, v62, v58, %[m2]\n\t" \
    "v_mfma_f32_16x16x32_bf16 v[0:3], v[108:111], v[104:107], v[0:3]\n\t" \
    "v_cndmask_b32_e64 v161, v57, v53, %[m1]\n\t" \
    "v_cndmask_b32_e64 v165, v61, v57, %[m1]\n\t" \
    "v_mfma_f32_16x16x32_bf16 v[8:11], v[120:123], v[68:71], v[8:11]\n\t" \
    "v_add_f32_dpp v162, v163, v162 wave_ror:1 row_mask:0xf bank_mask:0xf\n\t" \
    "v_add_f32_dpp v166, v167, v166 wave_ror:1 row_mask:0xf bank_mask:0xf\n\t" \
    "v_mfma_f32_16x16x32_bf16 v[12:15], v[108:111], v[68:71], v[12:15]\n\t" \
    "ds_read_b64 v[72:73], %[ub2] offset:448\n\t" \
    "ds_read_b64 v[74:75], %[ub2] offset:456\n\t" \
    "v_mfma_f32_16x16x32_bf16 v[40:43], v[120:123], v[144:147], v[40:43]\n\t" \
    "ds_read_b64 v[148:149], %[ub2] offset:480\n\t" \
    "ds_read_b64 v[150:151], %[ub2] offset:488\n\t" \
    "L_cv1_9:\n\t" \
    "s_cbranch_execz L_cv1_10\n\t" \
    "v_mfma_f32_16x16x32_bf16 v[80:83], v[108:111], v[144:147], v[80:83]\n\t" \
    "v_fmac_f32 v56, %[dd], v168\n\t" \
    "v_fmac_f32 v60, %[dd], v169\n\t" \
    "s_waitcnt lgkmcnt(6)\n\t" \
    "v_mfma_f32_16x16x32_bf16 v[152:155], v[112:115], v[136:139], v[152:155]\n\t" \
    "v_add_f32_dpp v161, v162, v161 wave_ror:1 row_mask:0xf bank_mask:0xf\n\t" \
    "v_mfma_f32_16x16x32_bf16 v[156:159], v[36:39], v[4:7], v[156:159]\n\t" \
    "v_add_f32_dpp v165, v166, v165 wave_ror:1 row_mask:0xf bank_mask:0xf\n\t" \
    "v_add_f32_dpp v56, v161, v56 wave_ror:1 row_mask:0xf bank_mask:0xf\n\t" \
    "v_mfma_f32_16x16x32_bf16 v[20:23], v[112:115], v[4:7], v[20:23]\n\t" \
    "v_add_f32_dpp v60, v165, v60 wave_ror:1 row_mask:0xf bank_mask:0xf\n\t" \
    "v_cvt_pk_bf16_f32 v160, v56, v60\n\t" \
    "v_mfma_f32_16x16x32_bf16 v[16:19], v[36:39], v[100:103], v[16:19]\n\t" \
    "ds_write_b16 %[ya], v160 offset:256\n\t" \
    "ds_write_b16_d16_hi %[ya], v160 offset:384\n\t" \
    "v_mfma_f32_16x16x32_bf16 v[0:3], v[112:115], v[100:103], v[0:3]\n\t" \
    "v_cndmask_b32_e64 v163, v67, v63, %[m3]\n\t" \
    "v_cndmask_b32_e64 v162, v66, v62, %[m2]\n\t" \
    "v_mfma_f32_16x16x32_bf16 v[8:11], v[36:39], v[104:107], v[8:11]\n\t" \
    "ds_read_b64 v[140:141], %[ub2] offset:512\n\t" \
    "ds_read_b64 v[142:143], %[ub2] offset:520\n\t" \
    "v_mfma_f32_16x16x32_bf16 v[12:15], v[112:115], v[104:107], v[12:15]\n\t" \
    "ds_read_b64 v[132:133], %[ub2] offset:544\n\t" \
    "ds_read_b64 v[134:135], %[ub2] offset:552\n\t" \
    "v_mfma_f32_16x16x32_bf16 v[40:43], v[36:39], v[68:71], v[40:43]\n\t" \
    "v_cndmask_b32_e64 v161, v65, v61, %[m1]\n\t" \
    "v_add_f32_dpp v162, v163, v162 wave_ror:1 row_mask:0xf bank_mask:0xf\n\t" \
    "v_mfma_f32_16x16x32_bf16 v[80:83], v[112:115], v[68:71], v[80:83]\n\t" \
    "v_fmac_f32 v64, %[dd], v170\n\t" \
    "v_add_f32_dpp v161, v162, v161 wave_ror:1 row_mask:0xf bank_mask:0xf\n\t" \
    "s_waitcnt lgkmcnt(6)\n\t" \
    "v_mfma_f32_16x16x32_bf16 v[156:159], v[72:75], v[136:139], v[156:159]\n\t" \
    "L_cv1_10:\n\t" \
    "s_cbranch_execz L_cv1_11\n\t" \
    "v_add_f32_dpp v64, v161, v64 wave_ror:1 row_mask:0xf bank_mask:0xf\n\t" \
    "v_mfma_f32_16x16x32_bf16 v[20:23], v[148:151], v[136:139], v[20:23]\n\t" \
    "v_cvt_pk_bf16_f32 v160, v64, v64\n\t" \
    "ds_write_b16 %[ya], v160 offset:512\n\t" \
    "v_mfma_f32_16x16x32_bf16 v[16:19], v[72:75], v[4:7], v[16:19]\n\t" \
    "ds_read_b64 v[92:93], %[ub2] offset:576\n\t" \
    "ds_read_b64 v[94:95], %[ub2] offset:584\n\t" \
    "v_mfma_f32_16x16x32_bf16 v[0:3], v[148:151], v[4:7], v[0:3]\n\t" \
    "ds_read_b64 v[32:33], %[ub2] offset:608\n\t" \
    "ds_read_b64 v[34:35], %[ub2] offset:616\n\t" \
    "v_mfma_f32_16x16x32_bf16 v[8:11], v[72:75], v[100:103], v[8:11]\n\t" \
    "ds_read_b128 v[180:183], %[yr]\n\t" \
    "v_mfma_f32_16x16x32_bf16 v[12:15], v[148:151], v[100:103], v[12:15]\n\t" \
    "v_mfma_f32_16x16x32_bf16 v[40:43], v[72:75], v[104:107], v[40:43]\n\t" \
    "ds_read_b64 v[128:129], %[ub2] offset:640\n\t" \
    "ds_read_b64 v[130:131], %[ub2] offset:648\n\t" \
    "v_mfma_f32_16x16x32_bf16 v[80:83], v[148:151], v[104:107], v[80:83]\n\t" \
    "ds_read_b64 v[76:77], %[ub2] offset:672\n\t" \
    "ds_read_b64 v[78:79], %[ub2] offset:680\n\t" \
    "s_waitcnt lgkmcnt(10)\n\t" \
    "v_mfma_f32_16x16x32_bf16 v[16:19], v[140:143], v[136:139], v[16:19]\n\t" \
    "ds_read_b64_tr_b16 v[88:89], %[hb2] offset:1056\n\t" \
    "v_mfma_f32_16x16x32_bf16 v[0:3], v[132:135], v[136:139], v[0:3]\n\t" \
    "ds_read_b64_tr_b16 v[90:91], %[hb2] offset:1024\n\t" \
    "ds_read_b64_tr_b16 v[84:85], %[hb2] offset:800\n\t" \
    "v_mfma_f32_16x16x32_bf16 v[8:11], v[140:143], v[4:7], v[8:11]\n\t" \
    "ds_read_b64_tr_b16 v[86:87], %[hb2] offset:768\n\t" \
    "ds_read_b64_tr_b16 v[24:25], %[hb2] offset:544\n\t" \
    "v_mfma_f32_16x16x32_bf16 v[12:15], v[132:135], v[4:7], v[12:15]\n\t" \
    "ds_read_b64_tr_b16 v[26:27], %[hb2] offset:512\n\t" \
    "ds_read_b64_tr_b16 v[124:125], %[hb2] offset:288\n\t" \
    "v_mfma_f32_16x16x32_bf16 v[40:43], v[140:143], v[100:103], v[40:43]\n\t" \
    "L_cv1_11:\n\t" \
    "s_cbranch_execz L_cv1_12\n\t" \
    "ds_read_b64_tr_b16 v[126:127], %[hb2] offset:256\n\t" \
    "ds_read_b64 v[28:29], %[ub2] offset:736\n\t" \
    "v_mfma_f32_16x16x32_bf16 v[80:83], v[132:135], v[100:103], v[80:83]\n\t" \
    "ds_read_b64 v[30:31], %[ub2] offset:744\n\t" \
    "ds_read_b64_tr_b16 v[48:49], %[hb2] offset:32\n\t" \
    "s_waitcnt lgkmcnt(14)\n\t" \
    "v_mfma_f32_16x16x32_bf16 v[8:11], v[92:95], v[136:139], v[8:11]\n\t" \
    "ds_read_b64_tr_b16 v[50:51], %[hb2] offset:0\n\t" \
    "v_mfma_f32_16x16x32_bf16 v[12:15], v[32:35], v[136:139], v[12:15]\n\t" \
    "v_mfma_f32_16x16x32_bf16 v[40:43], v[92:95], v[4:7], v[40:43]\n\t" \
    "v_mfma_f32_16x16x32_bf16 v[80:83], v[32:35], v[4:7], v[80:83]\n\t" \
    "s_waitcnt lgkmcnt(12)\n\t" \
    "v_mfma_f32_16x16x32_bf16 v[40:43], v[128:131], v[136:139], v[40:43]\n\t" \
    "v_mfma_f32_16x16x32_bf16 v[80:83], v[76:79], v[136:139], v[80:83]\n\t" \
    "ds_read_b64 v[116:117], %[ub2] offset:704\n\t" \
    "ds_read_b64 v[118:119], %[ub2] offset:712\n\t" \
    "s_waitcnt lgkmcnt(10)\n\t" \
    "v_mfma_f32_16x16x32_bf16 v[152:155], v[148:151], v[88:91], v[152:155]\n\t" \
    "v_mfma_f32_16x16x32_bf16 v[152:155], v[132:135], v[84:87], v[152:155]\n\t" \
    "ds_read_b64 v[96:97], %[ub2] offset:768\n\t" \
    "ds_read_b64 v[98:99], %[ub2] offset:776\n\t" \
    "s_waitcnt lgkmcnt(8)\n\t" \
    "v_mfma_f32_16x16x32_bf16 v[152:155], v[32:35], v[24:27], v[152:155]\n\t" \
    "ds_read_b64 v[44:45], %[ub2] offset:800\n\t" \
    "v_mfma_f32_16x16x32_bf16 v[152:155], v[76:79], v[124:127], v[152:155]\n\t" \
    "ds_read_b64 v[46:47], %[ub2] offset:808\n\t" \
    "s_waitcnt lgkmcnt(6)\n\t" \
    "v_mfma_f32_16x16x32_bf16 v[152:155], v[28:31], v[48:51], v[152:155]\n\t" \
    "L_cv1_12:\n\t" \
    "s_cbranch_execz L_cv1_13\n\t" \
    "v_mfma_f32_16x16x32_bf16 v[156:159], v[140:143], v[88:91], v[156:159]\n\t" \
    "v_mfma_f32_16x16x32_bf16 v[20:23], v[132:135], v[88:91], v[20:23]\n\t" \
    "v_mfma_f32_16x16x32_bf16 v[156:159], v[92:95], v[84:87], v[156:159]\n\t" \
    "v_mfma_f32_16x16x32_bf16 v[20:23], v[32:35], v[84:87], v[20:23]\n\t" \
    "v_mfma_f32_16x16x32_bf16 v[156:159], v[128:131], v[24:27], v[156:159]\n\t" \
    "v_mfma_f32_16x16x32_bf16 v[20:23], v[76:79], v[24:27], v[20:23]\n\t" \
    "ds_read_u16_d16_hi v171, %[ua2] offset:768\n\t" \
    "ds_read_u16_d16_hi v172, %[ua2] offset:800\n\t" \
    "s_waitcnt lgkmcnt(6)\n\t" \
    "v_mfma_f32_16x16x32_bf16 v[156:159], v[116:119], v[124:127], v[156:159]\n\t" \
    "ds_read_b64 v[120:121], %[ub2] offset:832\n\t" \
    "v_mfma_f32_16x16x32_bf16 v[20:23], v[28:31], v[124:127], v[20:23]\n\t" \
    "ds_read_b64 v[122:123], %[ub2] offset:840\n\t" \
    "ds_read_b64 v[108:109], %[ub2] offset:864\n\t" \
    "s_waitcnt lgkmcnt(5)\n\t" \
    "v_mfma_f32_16x16x32_bf16 v[156:159], v[96:99], v[48:51], v[156:159]\n\t" \
    "ds_read_b64 v[110:111], %[ub2] offset:872\n\t" \
    "v_mfma_f32_16x16x32_bf16 v[20:23], v[44:47], v[48:51], v[20:23]\n\t" \
    "v_mfma_f32_16x16x32_bf16 v[16:19], v[92:95], v[88:91], v[16:19]\n\t" \
    "v_mfma_f32_16x16x32_bf16 v[0:3], v[32:35], v[88:91], v[0:3]\n\t" \
    "v_mfma_f32_16x16x32_bf16 v[16:19], v[128:131], v[84:87], v[16:19]\n\t" \
    "v_mfma_f32_16x16x32_bf16 v[0:3], v[76:79], v[84:87], v[0:3]\n\t" \
    "s_nop 1\n\t" \
    "v_cndmask_b32_e64 v163, v159, v155, %[m3]\n\t" \
    "s_nop 0\n\t" \
    "v_cndmask_b32_e64 v167, v23, v159, %[m3]\n\t" \
    "v_mfma_f32_16x16x32_bf16 v[16:19], v[116:119], v[24:27], v[16:19]\n\t" \
    "v_cndmask_b32_e64 v162, v158, v154, %[m2]\n\t" \
    "v_cndmask_b32_e64 v166, v22, v158, %[m2]\n\t" \
    "v_mfma_f32_16x16x32_bf16 v[0:3], v[28:31], v[24:27], v[0:3]\n\t" \
    "ds_read_u16_d16_hi v173, %[ua2] offset:832\n\t" \
    "ds_read_u16_d16_hi v174, %[ua2] offset:864\n\t" \
    "v_mfma_f32_16x16x32_bf16 v[16:19], v[96:99], v[124:127], v[16:19]\n\t" \
    "L_cv1_13:\n\t" \
    "s_cbranch_execz L_cv1_14\n\t" \
    "ds_read_b64 v[144:145], %[ub2] offset:896\n\t" \
    "ds_read_b64 v[146:147], %[ub2] offset:904\n\t" \
    "v_mfma_f32_16x16x32_bf16 v[0:3], v[44:47], v[124:127], v[0:3]\n\t" \
    "ds_read_b64 v[52:53], %[ub2] offset:928\n\t" \
    "ds_read_b64 v[54:55], %[ub2] offset:936\n\t" \
    "s_waitcnt lgkmcnt(6)\n\t" \
    "v_mfma_f32_16x16x32_bf16 v[16:19], v[120:123], v[48:51], v[16:19]\n\t" \
    "v_cndmask_b32_e64 v161, v157, v153, %[m1]\n\t" \
    "v_mfma_f32_16x16x32_bf16 v[0:3], v[108:111], v[48:51], v[0:3]\n\t" \
    "v_cndmask_b32_e64 v165, v21, v157, %[m1]\n\t" \
    "v_add_f32_dpp v162, v163, v162 wave_ror:1 row_mask:0xf bank_mask:0xf\n\t" \
    "v_mfma_f32_16x16x32_bf16 v[8:11], v[128:131], v[88:91], v[8:11]\n\t" \
    "v_add_f32_dpp v166, v167, v166 wave_ror:1 row_mask:0xf bank_mask:0xf\n\t" \
    "v_fmac_f32 v156, %[dd2], v171\n\t" \
    "v_mfma_f32_16x16x32_bf16 v[12:15], v[76:79], v[88:91], v[12:15]\n\t" \
    "v_fmac_f32 v20, %[dd2], v172\n\t" \
    "v_add_f32_dpp v161, v162, v161 wave_ror:1 row_mask:0xf bank_mask:0xf\n\t" \
    "v_mfma_f32_16x16x32_bf16 v[8:11], v[116:119], v[84:87], v[8:11]\n\t" \
    "v_add_f32_dpp v165, v166, v165 wave_ror:1 row_mask:0xf bank_mask:0xf\n\t" \
    "v_add_f32_dpp v156, v161, v156 wave_ror:1 row_mask:0xf bank_mask:0xf\n\t" \
    "v_mfma_f32_16x16x32_bf16 v[12:15], v[28:31], v[84:87], v[12:15]\n\t" \
    "v_add_f32_dpp v20, v165, v20 wave_ror:1 row_mask:0xf bank_mask:0xf\n\t" \
    "v_cvt_pk_bf16_f32 v160, v156, v20\n\t" \
    "v_mfma_f32_16x16x32_bf16 v[8:11], v[96:99], v[24:27], v[8:11]\n\t" \
    "ds_write_b16 %[ya], v160 offset:0\n\t" \
    "ds_write_b16_d16_hi %[ya], v160 offset:128\n\t" \
    "v_mfma_f32_16x16x32_bf16 v[12:15], v[44:47], v[24:27], v[12:15]\n\t" \
    "ds_read_u16_d16_hi v175, %[ua2] offset:896\n\t" \
    "ds_read_u16_d16_hi v168, %[ua2] offset:928\n\t" \
    "v_mfma_f32_16x16x32_bf16 v[8:11], v[120:123], v[124:127], v[8:11]\n\t" \
    "ds_read_b64 v[56:57], %[ub2] offset:960\n\t" \
    "ds_read_b64 v[58:59], %[ub2] offset:968\n\t" \
    "L_cv1_14:\n\t" \
    "s_cbranch_execz L_cv1_15\n\t" \
    "v_mfma_f32_16x16x32_bf16 v[12:15], v[108:111], v[124:127], v[12:15]\n\t" \
    "ds_read_b64 v[36:37], %[ub2] offset:992\n\t" \
    "ds_read_b64 v[38:39], %[ub2] offset:1000\n\t" \
    "s_waitcnt lgkmcnt(8)\n\t" \
    "v_mfma_f32_16x16x32_bf16 v[8:11], v[144:147], v[48:51], v[8:11]\n\t" \
    "v_cndmask_b32_e64 v163, v19, v23, %[m3]\n\t" \
    "v_mfma_f32_16x16x32_bf16 v[12:15], v[52:55], v[48:51], v[12:15]\n\t" \
    "v_cndmask_b32_e64 v167, v3, v19, %[m3]\n\t" \
    "v_cndmask_b32_e64 v162, v18, v22, %[m2]\n\t" \
    "v_mfma_f32_16x16x32_bf16 v[40:43], v[116:119], v[88:91], v[40:43]\n\t" \
    "v_cndmask_b32_e64 v166, v2, v18, %[m2]\n\t" \
    "v_cndmask_b32_e64 v161, v17, v21, %[m1]\n\t" \
    "v_mfma_f32_16x16x32_bf16 v[80:83], v[28:31], v[88:91], v[80:83]\n\t" \
    "v_cndmask_b32_e64 v165, v1, v17, %[m1]\n\t" \
    "v_add_f32_dpp v162, v163, v162 wave_ror:1 row_mask:0xf bank_mask:0xf\n\t" \
    "v_mfma_f32_16x16x32_bf16 v[40:43], v[96:99], v[84:87], v[40:43]\n\t" \
    "v_add_f32_dpp v166, v167, v166 wave_ror:1 row_mask:0xf bank_mask:0xf\n\t" \
    "v_fmac_f32 v16, %[dd2], v173\n\t" \
    "v_mfma_f32_16x16x32_bf16 v[80:83], v[44:47], v[84:87], v[80:83]\n\t" \
    "v_fmac_f32 v0, %[dd2], v174\n\t" \
    "v_add_f32_dpp v161, v162, v161 wave_ror:1 row_mask:0xf bank_mask:0xf\n\t" \
    "v_mfma_f32_16x16x32_bf16 v[40:43], v[120:123], v[24:27], v[40:43]\n\t" \
    "v_add_f32_dpp v165, v166, v165 wave_ror:1 row_mask:0xf bank_mask:0xf\n\t" \
    "v_add_f32_dpp v16, v161, v16 wave_ror:1 row_mask:0xf bank_mask:0xf\n\t" \
    "v_mfma_f32_16x16x32_bf16 v[80:83], v[108:111], v[24:27], v[80:83]\n\t" \
    "ds_read_u16_d16_hi v169, %[ua2] offset:960\n\t" \
    "ds_read_u16_d16_hi v170, %[ua2] offset:992\n\t" \
    "v_mfma_f32_16x16x32_bf16 v[40:43], v[144:147], v[124:127], v[40:43]\n\t" \
    "v_add_f32_dpp v0, v165, v0 wave_ror:1 row_mask:0xf bank_mask:0xf\n\t" \
    "v_cvt_pk_bf16_f32 v160, v16, v0\n\t" \
    "v_mfma_f32_16x16x32_bf16 v[80:83], v[52:55], v[124:127], v[80:83]\n\t" \
    "ds_write_b16 %[ya], v160 offset:256\n\t" \
    "L_cv1_15:\n\t" \
    "s_cbranch_execz L_cv1_16\n\t" \
    "ds_write_b16_d16_hi %[ya], v160 offset:384\n\t" \
    "s_waitcnt lgkmcnt(4)\n\t" \
    "v_mfma_f32_16x16x32_bf16 v[40:43], v[56:59], v[48:51], v[40:43]\n\t" \
    "v_cndmask_b32_e64 v163, v11, v3, %[m3]\n\t" \
    "v_mfma_f32_16x16x32_bf16 v[80:83], v[36:39], v[48:51], v[80:83]\n\t" \
    "v_cndmask_b32_e64 v167, v15, v11, %[m3]\n\t" \
    "v_cndmask_b32_e64 v162, v10, v2, %[m2]\n\t" \
    "v_cndmask_b32_e64 v166, v14, v10, %[m2]\n\t" \
    "v_cndmask_b32_e64 v161, v9, v1, %[m1]\n\t" \
    "v_cndmask_b32_e64 v165, v13, v9, %[m1]\n\t" \
    "v_add_f32_dpp v162, v163, v162 wave_ror:1 row_mask:0xf bank_mask:0xf\n\t" \
    "v_add_f32_dpp v166, v167, v166 wave_ror:1 row_mask:0xf bank_mask:0xf\n\t" \
    "v_fmac_f32 v8, %[dd2], v175\n\t" \
    "v_fmac_f32 v12, %[dd2], v168\n\t" \
    "v_add_f32_dpp v161, v162, v161 wave_ror:1 row_mask:0xf bank_mask:0xf\n\t" \
    "v_add_f32_dpp v165, v166, v165 wave_ror:1 row_mask:0xf bank_mask:0xf\n\t" \
    "s_nop 0\n\t" \
    "v_add_f32_dpp v8, v161, v8 wave_ror:1 row_mask:0xf bank_mask:0xf\n\t" \
    "v_add_f32_dpp v12, v165, v12 wave_ror:1 row_mask:0xf bank_mask:0xf\n\t" \
    "v_cvt_pk_bf16_f32 v160, v8, v12\n\t" \
    "ds_write_b16 %[ya], v160 offset:512\n\t" \
    "ds_write_b16_d16_hi %[ya], v160 offset:640\n\t" \
    "v_cndmask_b32_e64 v163, v43, v15, %[m3]\n\t" \
    "v_cndmask_b32_e64 v167, v83, v43, %[m3]\n\t" \
    "v_cndmask_b32_e64 v162, v42, v14, %[m2]\n\t" \
    "v_cndmask_b32_e64 v166, v82, v42, %[m2]\n\t" \
    "v_cndmask_b32_e64 v161, v41, v13, %[m1]\n\t" \
    "v_cndmask_b32_e64 v165, v81, v41, %[m1]\n\t" \
    "v_add_f32_dpp v162, v163, v162 wave_ror:1 row_mask:0xf bank_mask:0xf\n\t" \
    "v_add_f32_dpp v166, v167, v166 wave_ror:1 row_mask:0xf bank_mask:0xf\n\t" \
    "s_waitcnt lgkmcnt(4)\n\t" \
    "v_fmac_f32 v40, %[dd2], v169\n\t" \
    "v_fmac_f32 v80, %[dd2], v170\n\t" \
    "L_cv1_16:\n\t" \
    "s_cbranch_execz L_cv1_17\n\t" \
    "v_add_f32_dpp v161, v162, v161 wave_ror:1 row_mask:0xf bank_mask:0xf\n\t" \
    "v_add_f32_dpp v165, v166, v165 wave_ror:1 row_mask:0xf bank_mask:0xf\n\t" \
    "s_nop 0\n\t" \
    "v_add_f32_dpp v40, v161, v40 wave_ror:1 row_mask:0xf bank_mask:0xf\n\t" \
    "v_add_f32_dpp v80, v165, v80 wave_ror:1 row_mask:0xf bank_mask:0xf\n\t" \
    "v_cvt_pk_bf16_f32 v160, v40, v80\n\t" \
    "ds_write_b16 %[ya], v160 offset:768\n\t" \
    "ds_write_b16_d16_hi %[ya], v160 offset:896\n\t" \
    "ds_read_b128 v[184:187], %[yr]\n\t" \
    "s_waitcnt vmcnt(0)\n\t" \
    "s_mov_b32 exec_hi, 0xffff\n\t" \
    "buffer_store_dwordx4 v[176:179], %[l16], %[rs], %[yo] offen offset:0 sc1\n\t" \
    "s_mov_b32 exec_hi, -1\n\t" \
    "s_mov_b32 exec_hi, 0xff\n\t" \
    "buffer_store_dwordx4 v[180:183], %[l16], %[rs], %[yo] offen offset:768 sc1\n\t" \
    "s_mov_b32 exec_hi, -1\n\t" \
    "s_waitcnt lgkmcnt(0)\n\t" \
    "buffer_store_dwordx4 v[184:187], %[l16], %[rs], %[yo2] offen offset:3072 sc1\n\t" \
    "s_nop 1\n\t" \
    "L_cv1_17:\n\t" \
    "s_cbranch_execz L_back\n\t" \
    "s_branch CVE_%=\n\t" \
    "L_cv2_0:\n\t" \
    "s_cbranch_execz L_cv2_1\n\t" \
    "ds_read_b64 v[0:1], %[ub] offset:0\n\t" \
    "ds_read_b64 v[2:3], %[ub] offset:8\n\t" \
    "ds_read_b64_tr_b16 v[4:5], %[hb] offset:1568\n\t" \
    "ds_read_b64_tr_b16 v[6:7], %[hb] offset:1536\n\t" \
    "ds_read_b64 v[8:9], %[ub] offset:32\n\t" \
    "ds_read_b64 v[10:11], %[ub] offset:40\n\t" \
    "ds_read_b64_tr_b16 v[12:13], %[hb] offset:1824\n\t" \
    "ds_read_b64_tr_b16 v[14:15], %[hb] offset:1792\n\t" \
    "ds_read_b64_tr_b16 v[16:17], %[hb] offset:2080\n\t" \
    "ds_read_b64_tr_b16 v[18:19], %[hb] offset:2048\n\t" \
    "ds_read_b64_tr_b16 v[20:21], %[hb] offset:2336\n\t" \
    "ds_read_b64_tr_b16 v[22:23], %[hb] offset:2304\n\t" \
    "v_mov_b32 v168, 0\n\t" \
    "v_mov_b32 v169, 0\n\t" \
    "v_mov_b32 v170, 0\n\t" \
    "v_mov_b32 v171, 0\n\t" \
    "v_mov_b32 v172, 0\n\t" \
    "v_mov_b32 v173, 0\n\t" \
    "v_mov_b32 v174, 0\n\t" \
    "v_mov_b32 v175, 0\n\t" \
    "s_waitcnt lgkmcnt(8)\n\t" \
    "v_mfma_f32_16x16x32_bf16 v[24:27], v[0:3], v[4:7], 0\n\t" \
    "ds_read_b64_tr_b16 v[28:29], %[hb] offset:2592\n\t" \
    "ds_read_b64_tr_b16 v[30:31], %[hb] offset:2560\n\t" \
    "ds_read_b64_tr_b16 v[32:33], %[hb] offset:2848\n\t" \
    "s_waitcnt lgkmcnt(9)\n\t" \
    "v_mfma_f32_16x16x32_bf16 v[36:39], v[8:11], v[4:7], 0\n\t" \
    "ds_read_b64_tr_b16 v[34:35], %[hb] offset:2816\n\t" \
    "s_waitcnt lgkmcnt(8)\n\t" \
    "v_mfma_f32_16x16x32_bf16 v[40:43], v[0:3], v[12:15], 0\n\t" \
    "ds_read_b64_tr_b16 v[44:45], %[hb] offset:3104\n\t" \
    "v_mfma_f32_16x16x32_bf16 v[48:51], v[8:11], v[12:15], 0\n\t" \
    "ds_read_b64_tr_b16 v[46:47], %[hb] offset:3072\n\t" \
    "L_cv2_1:\n\t" \
    "s_cbranch_execz L_cv2_2\n\t" \
    "ds_read_b64_tr_b16 v[52:53], %[hb] offset:3360\n\t" \
    "s_waitcnt lgkmcnt(9)\n\t" \
    "v_mfma_f32_16x16x32_bf16 v[56:59], v[0:3], v[16:19], 0\n\t" \
    "ds_read_b64_tr_b16 v[54:55], %[hb] offset:3328\n\t" \
    "v_mfma_f32_16x16x32_bf16 v[60:63], v[8:11], v[16:19], 0\n\t" \
    "ds_read_b64 v[64:65], %[ub] offset:64\n\t" \
    "ds_read_b64 v[66:67], %[ub] offset:72\n\t" \
    "s_waitcnt lgkmcnt(10)\n\t" \
    "v_mfma_f32_16x16x32_bf16 v[68:71], v[0:3], v[20:23], 0\n\t" \
    "ds_read_b64 v[72:73], %[ub] offset:96\n\t" \
    "v_mfma_f32_16x16x32_bf16 v[76:79], v[8:11], v[20:23], 0\n\t" \
    "ds_read_b64 v[74:75], %[ub] offset:104\n\t" \
    "s_waitcnt lgkmcnt(10)\n\t" \
    "v_mfma_f32_16x16x32_bf16 v[80:83], v[0:3], v[28:31], 0\n\t" \
    "v_mfma_f32_16x16x32_bf16 v[84:87], v[8:11], v[28:31], 0\n\t" \
    "s_waitcnt lgkmcnt(8)\n\t" \
    "v_mfma_f32_16x16x32_bf16 v[88:91], v[0:3], v[32:35], 0\n\t" \
    "v_mfma_f32_16x16x32_bf16 v[92:95], v[8:11], v[32:35], 0\n\t" \
    "s_waitcnt lgkmcnt(6)\n\t" \
    "v_mfma_f32_16x16x32_bf16 v[96:99], v[0:3], v[44:47], 0\n\t" \
    "v_mfma_f32_16x16x32_bf16 v[100:103], v[8:11], v[44:47], 0\n\t" \
    "s_waitcnt lgkmcnt(2)\n\t" \
    "v_mfma_f32_16x16x32_bf16 v[104:107], v[0:3], v[52:55], 0\n\t" \
    "v_mfma_f32_16x16x32_bf16 v[40:43], v[64:67], v[4:7], v[40:43]\n\t" \
    "s_waitcnt lgkmcnt(0)\n\t" \
    "v_mfma_f32_16x16x32_bf16 v[48:51], v[72:75], v[4:7], v[48:51]\n\t" \
    "ds_read_b64 v[108:109], %[ub] offset:128\n\t" \
    "v_mfma_f32_16x16x32_bf16 v[56:59], v[64:67], v[12:15], v[56:59]\n\t" \
    "ds_read_b64 v[110:111], %[ub] offset:136\n\t" \
    "ds_read_b64 v[112:113], %[ub] offset:160\n\t" \
    "v_mfma_f32_16x16x32_bf16 v[60:63], v[72:75], v[12:15], v[60:63]\n\t" \
    "ds_read_b64 v[114:115], %[ub] offset:168\n\t" \
    "v_mfma_f32_16x16x32_bf16 v[68:71], v[64:67], v[16:19], v[68:71]\n\t" \
    "L_cv2_2:\n\t" \
    "s_cbranch_execz L_cv2_3\n\t" \
    "v_mfma_f32_16x16x32_bf16 v[76:79], v[72:75], v[16:19], v[76:79]\n\t" \
    "v_mfma_f32_16x16x32_bf16 v[80:83], v[64:67], v[20:23], v[80:83]\n\t" \
    "v_mfma_f32_16x16x32_bf16 v[84:87], v[72:75], v[20:23], v[84:87]\n\t" \
    "v_mfma_f32_16x16x32_bf16 v[88:91], v[64:67], v[28:31], v[88:91]\n\t" \
    "v_mfma_f32_16x16x32_bf16 v[92:95], v[72:75], v[28:31], v[92:95]\n\t" \
    "v_mfma_f32_16x16x32_bf16 v[96:99], v[64:67], v[32:35], v[96:99]\n\t" \
    "v_mfma_f32_16x16x32_bf16 v[100:103], v[72:75], v[32:35], v[100:103]\n\t" \
    "v_mfma_f32_16x16x32_bf16 v[104:107], v[64:67], v[44:47], v[104:107]\n\t" \
    "ds_read_b64 v[116:117], %[ub] offset:192\n\t" \
    "ds_read_b64 v[118:119], %[ub] offset:200\n\t" \
    "s_waitcnt lgkmcnt(2)\n\t" \
    "v_mfma_f32_16x16x32_bf16 v[56:59], v[108:111], v[4:7], v[56:59]\n\t" \
    "ds_read_b64 v[120:121], %[ub] offset:224\n\t" \
    "v_mfma_f32_16x16x32_bf16 v[60:63], v[112:115], v[4:7], v[60:63]\n\t" \
    "ds_read_b64 v[122:123], %[ub] offset:232\n\t" \
    "v_mfma_f32_16x16x32_bf16 v[68:71], v[108:111], v[12:15], v[68:71]\n\t" \
    "v_mfma_f32_16x16x32_bf16 v[76:79], v[112:115], v[12:15], v[76:79]\n\t" \
    "v_mfma_f32_16x16x32_bf16 v[80:83], v[108:111], v[16:19], v[80:83]\n\t" \
    "v_mfma_f32_16x16x32_bf16 v[84:87], v[112:115], v[16:19], v[84:87]\n\t" \
    "v_mfma_f32_16x16x32_bf16 v[88:91], v[108:111], v[20:23], v[88:91]\n\t" \
    "v_mfma_f32_16x16x32_bf16 v[92:95], v[112:115], v[20:23], v[92:95]\n\t" \
    "v_mfma_f32_16x16x32_bf16 v[96:99], v[108:111], v[28:31], v[96:99]\n\t" \
    "ds_read_b64 v[124:125], %[ub] offset:256\n\t" \
    "ds_read_b64 v[126:127], %[ub] offset:264\n\t" \
    "v_mfma_f32_16x16x32_bf16 v[100:103], v[112:115], v[28:31], v[100:103]\n\t" \
    "ds_read_b64 v[128:129], %[ub] offset:288\n\t" \
    "ds_read_b64 v[130:131], %[ub] offset:296\n\t" \
    "v_mfma_f32_16x16x32_bf16 v[104:107], v[108:111], v[32:35], v[104:107]\n\t" \
    "s_waitcnt lgkmcnt(4)\n\t" \
    "v_mfma_f32_16x16x32_bf16 v[68:71], v[116:119], v[4:7], v[68:71]\n\t" \
    "L_cv2_3:\n\t" \
    "s_cbranch_execz L_cv2_4\n\t" \
    "v_mfma_f32_16x16x32_bf16 v[76:79], v[120:123], v[4:7], v[76:79]\n\t" \
    "v_mfma_f32_16x16x32_bf16 v[80:83], v[116:119], v[12:15], v[80:83]\n\t" \
    "v_mfma_f32_16x16x32_bf16 v[84:87], v[120:123], v[12:15], v[84:87]\n\t" \
    "v_mfma_f32_16x16x32_bf16 v[88:91], v[116:119], v[16:19], v[88:91]\n\t" \
    "ds_read_b64_tr_b16 v[132:133], %[hb] offset:1312\n\t" \
    "ds_read_b64_tr_b16 v[134:135], %[hb] offset:1280\n\t" \
    "v_mfma_f32_16x16x32_bf16 v[92:95], v[120:123], v[16:19], v[92:95]\n\t" \
    "v_mfma_f32_16x16x32_bf16 v[96:99], v[116:119], v[20:23], v[96:99]\n\t" \
    "ds_read_b64_tr_b16 v[136:137], %[hb] offset:1056\n\t" \
    "ds_read_b64_tr_b16 v[138:139], %[hb] offset:1024\n\t" \
    "v_mfma_f32_16x16x32_bf16 v[100:103], v[120:123], v[20:23], v[100:103]\n\t" \
    "v_mfma_f32_16x16x32_bf16 v[104:107], v[116:119], v[28:31], v[104:107]\n\t" \
    "ds_read_b64_tr_b16 v[140:141], %[hb] offset:800\n\t" \
    "ds_read_b64_tr_b16 v[142:143], %[hb] offset:768\n\t" \
    "s_waitcnt lgkmcnt(6)\n\t" \
    "v_mfma_f32_16x16x32_bf16 v[80:83], v[124:127], v[4:7], v[80:83]\n\t" \
    "v_mfma_f32_16x16x32_bf16 v[84:87], v[128:131], v[4:7], v[84:87]\n\t" \
    "ds_read_b64_tr_b16 v[144:145], %[hb] offset:544\n\t" \
    "ds_read_b64_tr_b16 v[146:147], %[hb] offset:512\n\t" \
    "v_mfma_f32_16x16x32_bf16 v[88:91], v[124:127], v[12:15], v[88:91]\n\t" \
    "v_mfma_f32_16x16x32_bf16 v[92:95], v[128:131], v[12:15], v[92:95]\n\t" \
    "ds_read_b64_tr_b16 v[148:149], %[hb] offset:288\n\t" \
    "ds_read_b64_tr_b16 v[150:151], %[hb] offset:256\n\t" \
    "v_mfma_f32_16x16x32_bf16 v[96:99], v[124:127], v[16:19], v[96:99]\n\t" \
    "v_mfma_f32_16x16x32_bf16 v[100:103], v[128:131], v[16:19], v[100:103]\n\t" \
    "ds_read_b64 v[152:153], %[ub] offset:320\n\t" \
    "ds_read_b64 v[154:155], %[ub] offset:328\n\t" \
    "v_mfma_f32_16x16x32_bf16 v[104:107], v[124:127], v[20:23], v[104:107]\n\t" \
    "ds_read_b64_tr_b16 v[156:157], %[hb] offset:32\n\t" \
    "ds_read_b64_tr_b16 v[158:159], %[hb] offset:0\n\t" \
    "s_waitcnt lgkmcnt(12)\n\t" \
    "v_mfma_f32_16x16x32_bf16 v[52:55], v[0:3], v[132:135], 0\n\t" \
    "L_cv2_4:\n\t" \
    "s_cbranch_execz L_cv2_5\n\t" \
    "ds_read_b64 v[44:45], %[ub] offset:352\n\t" \
    "v_mfma_f32_16x16x32_bf16 v[32:35], v[8:11], v[132:135], 0\n\t" \
    "ds_read_b64 v[46:47], %[ub] offset:360\n\t" \
    "s_waitcnt lgkmcnt(12)\n\t" \
    "v_mfma_f32_16x16x32_bf16 v[52:55], v[64:67], v[136:139], v[52:55]\n\t" \
    "v_mfma_f32_16x16x32_bf16 v[32:35], v[72:75], v[136:139], v[32:35]\n\t" \
    "s_waitcnt lgkmcnt(10)\n\t" \
    "v_mfma_f32_16x16x32_bf16 v[52:55], v[108:111], v[140:143], v[52:55]\n\t" \
    "v_mfma_f32_16x16x32_bf16 v[32:35], v[112:115], v[140:143], v[32:35]\n\t" \
    "ds_read_b64 v[28:29], %[ub] offset:384\n\t" \
    "ds_read_b64 v[30:31], %[ub] offset:392\n\t" \
    "s_waitcnt lgkmcnt(10)\n\t" \
    "v_mfma_f32_16x16x32_bf16 v[52:55], v[116:119], v[144:147], v[52:55]\n\t" \
    "ds_read_b64 v[20:21], %[ub] offset:416\n\t" \
    "v_mfma_f32_16x16x32_bf16 v[32:35], v[120:123], v[144:147], v[32:35]\n\t" \
    "ds_read_b64 v[22:23], %[ub] offset:424\n\t" \
    "ds_read_u16_d16_hi v168, %[ua] offset:352\n\t" \
    "s_waitcnt lgkmcnt(11)\n\t" \
    "v_mfma_f32_16x16x32_bf16 v[52:55], v[124:127], v[148:151], v[52:55]\n\t" \
    "ds_read_b64 v[0:1], %[ub] offset:448\n\t" \
    "v_mfma_f32_16x16x32_bf16 v[32:35], v[128:131], v[148:151], v[32:35]\n\t" \
    "ds_read_b64 v[2:3], %[ub] offset:456\n\t" \
    "s_waitcnt lgkmcnt(7)\n\t" \
    "v_mfma_f32_16x16x32_bf16 v[52:55], v[152:155], v[156:159], v[52:55]\n\t" \
    "v_mfma_f32_16x16x32_bf16 v[32:35], v[44:47], v[156:159], v[32:35]\n\t" \
    "v_mfma_f32_16x16x32_bf16 v[88:91], v[152:155], v[4:7], v[88:91]\n\t" \
    "v_mfma_f32_16x16x32_bf16 v[92:95], v[44:47], v[4:7], v[92:95]\n\t" \
    "v_mfma_f32_16x16x32_bf16 v[96:99], v[152:155], v[12:15], v[96:99]\n\t" \
    "L_cv2_5:\n\t" \
    "s_cbranch_execz L_cv2_6\n\t" \
    "v_mfma_f32_16x16x32_bf16 v[100:103], v[44:47], v[12:15], v[100:103]\n\t" \
    "s_nop 3\n\t" \
    "v_cndmask_b32_e64 v163, v35, v55, %[m3]\n\t" \
    "v_cndmask_b32_e64 v162, v34, v54, %[m2]\n\t" \
    "v_mfma_f32_16x16x32_bf16 v[104:107], v[152:155], v[16:19], v[104:107]\n\t" \
    "v_cndmask_b32_e64 v161, v33, v53, %[m1]\n\t" \
    "v_add_f32_dpp v162, v163, v162 wave_ror:1 row_mask:0xf bank_mask:0xf\n\t" \
    "s_waitcnt lgkmcnt(3)\n\t" \
    "v_mfma_f32_16x16x32_bf16 v[96:99], v[28:31], v[4:7], v[96:99]\n\t" \
    "s_waitcnt lgkmcnt(2)\n\t" \
    "v_fmac_f32 v32, %[dd], v168\n\t" \
    "v_mfma_f32_16x16x32_bf16 v[100:103], v[20:23], v[4:7], v[100:103]\n\t" \
    "v_add_f32_dpp v161, v162, v161 wave_ror:1 row_mask:0xf bank_mask:0xf\n\t" \
    "s_nop 1\n\t" \
    "v_add_f32_dpp v32, v161, v32 wave_ror:1 row_mask:0xf bank_mask:0xf\n\t" \
    "v_mfma_f32_16x16x32_bf16 v[104:107], v[28:31], v[12:15], v[104:107]\n\t" \
    "v_cvt_pk_bf16_f32 v160, v32, v32\n\t" \
    "ds_write_b16 %[ya], v160 offset:0\n\t" \
    "s_waitcnt lgkmcnt(1)\n\t" \
    "v_mfma_f32_16x16x32_bf16 v[104:107], v[0:3], v[4:7], v[104:107]\n\t" \
    "v_mfma_f32_16x16x32_bf16 v[24:27], v[64:67], v[132:135], v[24:27]\n\t" \
    "v_mfma_f32_16x16x32_bf16 v[36:39], v[72:75], v[132:135], v[36:39]\n\t" \
    "v_mfma_f32_16x16x32_bf16 v[24:27], v[108:111], v[136:139], v[24:27]\n\t" \
    "v_mfma_f32_16x16x32_bf16 v[36:39], v[112:115], v[136:139], v[36:39]\n\t" \
    "v_mfma_f32_16x16x32_bf16 v[24:27], v[116:119], v[140:143], v[24:27]\n\t" \
    "v_mfma_f32_16x16x32_bf16 v[36:39], v[120:123], v[140:143], v[36:39]\n\t" \
    "v_mfma_f32_16x16x32_bf16 v[24:27], v[124:127], v[144:147], v[24:27]\n\t" \
    "v_mfma_f32_16x16x32_bf16 v[36:39], v[128:131], v[144:147], v[36:39]\n\t" \
    "ds_read_u16_d16_hi v169, %[ua] offset:384\n\t" \
    "ds_read_u16_d16_hi v170, %[ua] offset:416\n\t" \
    "v_mfma_f32_16x16x32_bf16 v[24:27], v[152:155], v[148:151], v[24:27]\n\t" \
    "L_cv2_6:\n\t" \
    "s_cbranch_execz L_cv2_7\n\t" \
    "v_mfma_f32_16x16x32_bf16 v[36:39], v[44:47], v[148:151], v[36:39]\n\t" \
    "v_mfma_f32_16x16x32_bf16 v[24:27], v[28:31], v[156:159], v[24:27]\n\t" \
    "v_mfma_f32_16x16x32_bf16 v[36:39], v[20:23], v[156:159], v[36:39]\n\t" \
    "ds_read_b64 v[8:9], %[ub] offset:480\n\t" \
    "ds_read_b64 v[10:11], %[ub] offset:488\n\t" \
    "v_mfma_f32_16x16x32_bf16 v[40:43], v[108:111], v[132:135], v[40:43]\n\t" \
    "v_mfma_f32_16x16x32_bf16 v[48:51], v[112:115], v[132:135], v[48:51]\n\t" \
    "v_mfma_f32_16x16x32_bf16 v[40:43], v[116:119], v[136:139], v[40:43]\n\t" \
    "v_mfma_f32_16x16x32_bf16 v[48:51], v[120:123], v[136:139], v[48:51]\n\t" \
    "s_nop 0\n\t" \
    "v_cndmask_b32_e64 v163, v27, v35, %[m3]\n\t" \
    "v_cndmask_b32_e64 v167, v39, v27, %[m3]\n\t" \
    "v_mfma_f32_16x16x32_bf16 v[40:43], v[124:127], v[140:143], v[40:43]\n\t" \
    "v_cndmask_b32_e64 v162, v26, v34, %[m2]\n\t" \
    "v_cndmask_b32_e64 v166, v38, v26, %[m2]\n\t" \
    "v_mfma_f32_16x16x32_bf16 v[48:51], v[128:131], v[140:143], v[48:51]\n\t" \
    "v_cndmask_b32_e64 v161, v25, v33, %[m1]\n\t" \
    "v_cndmask_b32_e64 v165, v37, v25, %[m1]\n\t" \
    "v_mfma_f32_16x16x32_bf16 v[40:43], v[152:155], v[144:147], v[40:43]\n\t" \
    "v_add_f32_dpp v162, v163, v162 wave_ror:1 row_mask:0xf bank_mask:0xf\n\t" \
    "v_add_f32_dpp v166, v167, v166 wave_ror:1 row_mask:0xf bank_mask:0xf\n\t" \
    "v_mfma_f32_16x16x32_bf16 v[48:51], v[44:47], v[144:147], v[48:51]\n\t" \
    "ds_read_u16_d16_hi v171, %[ua] offset:448\n\t" \
    "ds_read_u16_d16_hi v172, %[ua] offset:480\n\t" \
    "v_mfma_f32_16x16x32_bf16 v[40:43], v[28:31], v[148:151], v[40:43]\n\t" \
    "s_waitcnt lgkmcnt(4)\n\t" \
    "v_fmac_f32 v24, %[dd], v169\n\t" \
    "v_mfma_f32_16x16x32_bf16 v[48:51], v[20:23], v[148:151], v[48:51]\n\t" \
    "v_fmac_f32 v36, %[dd], v170\n\t" \
    "v_add_f32_dpp v161, v162, v161 wave_ror:1 row_mask:0xf bank_mask:0xf\n\t" \
    "v_mfma_f32_16x16x32_bf16 v[40:43], v[0:3], v[156:159], v[40:43]\n\t" \
    "ds_read_b64 v[16:17], %[ub] offset:512\n\t" \
    "L_cv2_7:\n\t" \
    "s_cbranch_execz L_cv2_8\n\t" \
    "ds_read_b64 v[18:19], %[ub] offset:520\n\t" \
    "s_waitcnt lgkmcnt(4)\n\t" \
    "v_mfma_f32_16x16x32_bf16 v[48:51], v[8:11], v[156:159], v[48:51]\n\t" \
    "ds_read_b64 v[52:53], %[ub] offset:544\n\t" \
    "v_mfma_f32_16x16x32_bf16 v[56:59], v[116:119], v[132:135], v[56:59]\n\t" \
    "ds_read_b64 v[54:55], %[ub] offset:552\n\t" \
    "v_add_f32_dpp v165, v166, v165 wave_ror:1 row_mask:0xf bank_mask:0xf\n\t" \
    "v_mfma_f32_16x16x32_bf16 v[60:63], v[120:123], v[132:135], v[60:63]\n\t" \
    "v_add_f32_dpp v24, v161, v24 wave_ror:1 row_mask:0xf bank_mask:0xf\n\t" \
    "v_add_f32_dpp v36, v165, v36 wave_ror:1 row_mask:0xf bank_mask:0xf\n\t" \
    "v_mfma_f32_16x16x32_bf16 v[56:59], v[124:127], v[136:139], v[56:59]\n\t" \
    "v_cvt_pk_bf16_f32 v160, v24, v36\n\t" \
    "ds_write_b16 %[ya], v160 offset:128\n\t" \
    "v_mfma_f32_16x16x32_bf16 v[60:63], v[128:131], v[136:139], v[60:63]\n\t" \
    "ds_write_b16_d16_hi %[ya], v160 offset:256\n\t" \
    "v_cndmask_b32_e64 v163, v43, v39, %[m3]\n\t" \
    "v_mfma_f32_16x16x32_bf16 v[56:59], v[152:155], v[140:143], v[56:59]\n\t" \
    "v_cndmask_b32_e64 v167, v51, v43, %[m3]\n\t" \
    "v_cndmask_b32_e64 v162, v42, v38, %[m2]\n\t" \
    "v_mfma_f32_16x16x32_bf16 v[60:63], v[44:47], v[140:143], v[60:63]\n\t" \
    "v_cndmask_b32_e64 v166, v50, v42, %[m2]\n\t" \
    "v_cndmask_b32_e64 v161, v41, v37, %[m1]\n\t" \
    "v_mfma_f32_16x16x32_bf16 v[56:59], v[28:31], v[144:147], v[56:59]\n\t" \
    "v_cndmask_b32_e64 v165, v49, v41, %[m1]\n\t" \
    "v_add_f32_dpp v162, v163, v162 wave_ror:1 row_mask:0xf bank_mask:0xf\n\t" \
    "v_mfma_f32_16x16x32_bf16 v[60:63], v[20:23], v[144:147], v[60:63]\n\t" \
    "ds_read_u16_d16_hi v173, %[ua] offset:512\n\t" \
    "ds_read_u16_d16_hi v174, %[ua] offset:544\n\t" \
    "v_mfma_f32_16x16x32_bf16 v[56:59], v[0:3], v[148:151], v[56:59]\n\t" \
    "v_add_f32_dpp v166, v167, v166 wave_ror:1 row_mask:0xf bank_mask:0xf\n\t" \
    "s_waitcnt lgkmcnt(8)\n\t" \
    "v_fmac_f32 v40, %[dd], v171\n\t" \
    "L_cv2_8:\n\t" \
    "s_cbranch_execz L_cv2_9\n\t" \
    "v_mfma_f32_16x16x32_bf16 v[60:63], v[8:11], v[148:151], v[60:63]\n\t" \
    "v_fmac_f32 v48, %[dd], v172\n\t" \
    "v_add_f32_dpp v161, v162, v161 wave_ror:1 row_mask:0xf bank_mask:0xf\n\t" \
    "s_waitcnt lgkmcnt(4)\n\t" \
    "v_mfma_f32_16x16x32_bf16 v[56:59], v[16:19], v[156:159], v[56:59]\n\t" \
    "ds_read_b64 v[12:13], %[ub] offset:576\n\t" \
    "v_mfma_f32_16x16x32_bf16 v[60:63], v[52:55], v[156:159], v[60:63]\n\t" \
    "ds_read_b64 v[14:15], %[ub] offset:584\n\t" \
    "ds_read_b64 v[4:5], %[ub] offset:608\n\t" \
    "v_mfma_f32_16x16x32_bf16 v[68:71], v[124:127], v[132:135], v[68:71]\n\t" \
    "ds_read_b64 v[6:7], %[ub] offset:616\n\t" \
    "v_add_f32_dpp v165, v166, v165 wave_ror:1 row_mask:0xf bank_mask:0xf\n\t" \
    "v_mfma_f32_16x16x32_bf16 v[76:79], v[128:131], v[132:135], v[76:79]\n\t" \
    "v_add_f32_dpp v40, v161, v40 wave_ror:1 row_mask:0xf bank_mask:0xf\n\t" \
    "v_add_f32_dpp v48, v165, v48 wave_ror:1 row_mask:0xf bank_mask:0xf\n\t" \
    "v_mfma_f32_16x16x32_bf16 v[68:71], v[152:155], v[136:139], v[68:71]\n\t" \
    "v_cvt_pk_bf16_f32 v160, v40, v48\n\t" \
    "ds_write_b16 %[ya], v160 offset:384\n\t" \
    "v_mfma_f32_16x16x32_bf16 v[76:79], v[44:47], v[136:139], v[76:79]\n\t" \
    "ds_write_b16_d16_hi %[ya], v160 offset:512\n\t" \
    "v_cndmask_b32_e64 v163, v59, v51, %[m3]\n\t" \
    "v_mfma_f32_16x16x32_bf16 v[68:71], v[28:31], v[140:143], v[68:71]\n\t" \
    "v_cndmask_b32_e64 v167, v63, v59, %[m3]\n\t" \
    "v_cndmask_b32_e64 v162, v58, v50, %[m2]\n\t" \
    "v_mfma_f32_16x16x32_bf16 v[76:79], v[20:23], v[140:143], v[76:79]\n\t" \
    "v_cndmask_b32_e64 v166, v62, v58, %[m2]\n\t" \
    "v_cndmask_b32_e64 v161, v57, v49, %[m1]\n\t" \
    "v_mfma_f32_16x16x32_bf16 v[68:71], v[0:3], v[144:147], v[68:71]\n\t" \
    "v_cndmask_b32_e64 v165, v61, v57, %[m1]\n\t" \
    "v_add_f32_dpp v162, v163, v162 wave_ror:1 row_mask:0xf bank_mask:0xf\n\t" \
    "v_mfma_f32_16x16x32_bf16 v[76:79], v[8:11], v[144:147], v[76:79]\n\t" \
    "ds_read_u16_d16_hi v175, %[ua] offset:576\n\t" \
    "L_cv2_9:\n\t" \
    "s_cbranch_execz L_cv2_10\n\t" \
    "ds_read_u16_d16_hi v168, %[ua] offset:608\n\t" \
    "v_mfma_f32_16x16x32_bf16 v[68:71], v[16:19], v[148:151], v[68:71]\n\t" \
    "v_add_f32_dpp v166, v167, v166 wave_ror:1 row_mask:0xf bank_mask:0xf\n\t" \
    "s_waitcnt lgkmcnt(8)\n\t" \
    "v_fmac_f32 v56, %[dd], v173\n\t" \
    "v_mfma_f32_16x16x32_bf16 v[76:79], v[52:55], v[148:151], v[76:79]\n\t" \
    "v_fmac_f32 v60, %[dd], v174\n\t" \
    "v_add_f32_dpp v161, v162, v161 wave_ror:1 row_mask:0xf bank_mask:0xf\n\t" \
    "s_waitcnt lgkmcnt(4)\n\t" \
    "v_mfma_f32_16x16x32_bf16 v[68:71], v[12:15], v[156:159], v[68:71]\n\t" \
    "ds_read_b64 v[64:65], %[ub] offset:640\n\t" \
    "v_mfma_f32_16x16x32_bf16 v[76:79], v[4:7], v[156:159], v[76:79]\n\t" \
    "ds_read_b64 v[66:67], %[ub] offset:648\n\t" \
    "ds_read_b64 v[72:73], %[ub] offset:672\n\t" \
    "v_mfma_f32_16x16x32_bf16 v[80:83], v[152:155], v[132:135], v[80:83]\n\t" \
    "ds_read_b64 v[74:75], %[ub] offset:680\n\t" \
    "v_add_f32_dpp v165, v166, v165 wave_ror:1 row_mask:0xf bank_mask:0xf\n\t" \
    "v_mfma_f32_16x16x32_bf16 v[84:87], v[44:47], v[132:135], v[84:87]\n\t" \
    "v_add_f32_dpp v56, v161, v56 wave_ror:1 row_mask:0xf bank_mask:0xf\n\t" \
    "v_add_f32_dpp v60, v165, v60 wave_ror:1 row_mask:0xf bank_mask:0xf\n\t" \
    "v_mfma_f32_16x16x32_bf16 v[80:83], v[28:31], v[136:139], v[80:83]\n\t" \
    "v_cvt_pk_bf16_f32 v160, v56, v60\n\t" \
    "ds_write_b16 %[ya], v160 offset:640\n\t" \
    "v_mfma_f32_16x16x32_bf16 v[84:87], v[20:23], v[136:139], v[84:87]\n\t" \
    "ds_write_b16_d16_hi %[ya], v160 offset:768\n\t" \
    "ds_read_b128 v[176:179], %[yr]\n\t" \
    "v_mfma_f32_16x16x32_bf16 v[80:83], v[0:3], v[140:143], v[80:83]\n\t" \
    "v_cndmask_b32_e64 v163, v71, v63, %[m3]\n\t" \
    "v_cndmask_b32_e64 v167, v79, v71, %[m3]\n\t" \
    "v_mfma_f32_16x16x32_bf16 v[84:87], v[8:11], v[140:143], v[84:87]\n\t" \
    "v_cndmask_b32_e64 v162, v70, v62, %[m2]\n\t" \
    "v_cndmask_b32_e64 v166, v78, v70, %[m2]\n\t" \
    "L_cv2_10:\n\t" \
    "s_cbranch_execz L_cv2_11\n\t" \
    "v_mfma_f32_16x16x32_bf16 v[80:83], v[16:19], v[144:147], v[80:83]\n\t" \
    "v_cndmask_b32_e64 v161, v69, v61, %[m1]\n\t" \
    "v_cndmask_b32_e64 v165, v77, v69, %[m1]\n\t" \
    "v_mfma_f32_16x16x32_bf16 v[84:87], v[52:55], v[144:147], v[84:87]\n\t" \
    "ds_read_u16_d16_hi v169, %[ua] offset:640\n\t" \
    "ds_read_u16_d16_hi v170, %[ua] offset:672\n\t" \
    "v_mfma_f32_16x16x32_bf16 v[80:83], v[12:15], v[148:151], v[80:83]\n\t" \
    "v_add_f32_dpp v162, v163, v162 wave_ror:1 row_mask:0xf bank_mask:0xf\n\t" \
    "v_add_f32_dpp v166, v167, v166 wave_ror:1 row_mask:0xf bank_mask:0xf\n\t" \
    "v_mfma_f32_16x16x32_bf16 v[84:87], v[4:7], v[148:151], v[84:87]\n\t" \
    "s_waitcnt lgkmcnt(9)\n\t" \
    "v_fmac_f32 v68, %[dd], v175\n\t" \
    "s_waitcnt lgkmcnt(5)\n\t" \
    "v_mfma_f32_16x16x32_bf16 v[80:83], v[64:67], v[156:159], v[80:83]\n\t" \
    "ds_read_b64 v[108:109], %[ub] offset:704\n\t" \
    "v_mfma_f32_16x16x32_bf16 v[84:87], v[72:75], v[156:159], v[84:87]\n\t" \
    "ds_read_b64 v[110:111], %[ub] offset:712\n\t" \
    "ds_read_b64 v[112:113], %[ub] offset:736\n\t" \
    "v_mfma_f32_16x16x32_bf16 v[88:91], v[28:31], v[132:135], v[88:91]\n\t" \
    "ds_read_b64 v[114:115], %[ub] offset:744\n\t" \
    "v_fmac_f32 v76, %[dd], v168\n\t" \
    "v_mfma_f32_16x16x32_bf16 v[92:95], v[20:23], v[132:135], v[92:95]\n\t" \
    "v_add_f32_dpp v161, v162, v161 wave_ror:1 row_mask:0xf bank_mask:0xf\n\t" \
    "v_add_f32_dpp v165, v166, v165 wave_ror:1 row_mask:0xf bank_mask:0xf\n\t" \
    "v_mfma_f32_16x16x32_bf16 v[88:91], v[0:3], v[136:139], v[88:91]\n\t" \
    "v_add_f32_dpp v68, v161, v68 wave_ror:1 row_mask:0xf bank_mask:0xf\n\t" \
    "v_add_f32_dpp v76, v165, v76 wave_ror:1 row_mask:0xf bank_mask:0xf\n\t" \
    "v_mfma_f32_16x16x32_bf16 v[92:95], v[8:11], v[136:139], v[92:95]\n\t" \
    "v_cvt_pk_bf16_f32 v160, v68, v76\n\t" \
    "ds_write_b16 %[ya], v160 offset:0\n\t" \
    "v_mfma_f32_16x16x32_bf16 v[88:91], v[16:19], v[140:143], v[88:91]\n\t" \
    "ds_write_b16_d16_hi %[ya], v160 offset:128\n\t" \
    "L_cv2_11:\n\t" \
    "s_cbranch_execz L_cv2_12\n\t" \
    "v_cndmask_b32_e64 v163, v83, v79, %[m3]\n\t" \
    "v_mfma_f32_16x16x32_bf16 v[92:95], v[52:55], v[140:143], v[92:95]\n\t" \
    "v_cndmask_b32_e64 v167, v87, v83, %[m3]\n\t" \
    "v_cndmask_b32_e64 v162, v82, v78, %[m2]\n\t" \
    "v_mfma_f32_16x16x32_bf16 v[88:91], v[12:15], v[144:147], v[88:91]\n\t" \
    "v_cndmask_b32_e64 v166, v86, v82, %[m2]\n\t" \
    "v_cndmask_b32_e64 v161, v81, v77, %[m1]\n\t" \
    "v_mfma_f32_16x16x32_bf16 v[92:95], v[4:7], v[144:147], v[92:95]\n\t" \
    "ds_read_u16_d16_hi v171, %[ua] offset:704\n\t" \
    "ds_read_u16_d16_hi v172, %[ua] offset:736\n\t" \
    "v_mfma_f32_16x16x32_bf16 v[88:91], v[64:67], v[148:151], v[88:91]\n\t" \
    "v_cndmask_b32_e64 v165, v85, v81, %[m1]\n\t" \
    "v_add_f32_dpp v162, v163, v162 wave_ror:1 row_mask:0xf bank_mask:0xf\n\t" \
    "v_mfma_f32_16x16x32_bf16 v[92:95], v[72:75], v[148:151], v[92:95]\n\t" \
    "v_add_f32_dpp v166, v167, v166 wave_ror:1 row_mask:0xf bank_mask:0xf\n\t" \
    "s_waitcnt lgkmcnt(8)\n\t" \
    "v_fmac_f32 v80, %[dd], v169\n\t" \
    "s_waitcnt lgkmcnt(4)\n\t" \
    "v_mfma_f32_16x16x32_bf16 v[88:91], v[108:111], v[156:159], v[88:91]\n\t" \
    "ds_read_b64 v[116:117], %[ub] offset:768\n\t" \
    "v_mfma_f32_16x16x32_bf16 v[92:95], v[112:115], v[156:159], v[92:95]\n\t" \
    "ds_read_b64 v[118:119], %[ub] offset:776\n\t" \
    "ds_read_b64 v[120:121], %[ub] offset:800\n\t" \
    "v_mfma_f32_16x16x32_bf16 v[96:99], v[0:3], v[132:135], v[96:99]\n\t" \
    "ds_read_b64 v[122:123], %[ub] offset:808\n\t" \
    "v_fmac_f32 v84, %[dd], v170\n\t" \
    "v_mfma_f32_16x16x32_bf16 v[100:103], v[8:11], v[132:135], v[100:103]\n\t" \
    "v_add_f32_dpp v161, v162, v161 wave_ror:1 row_mask:0xf bank_mask:0xf\n\t" \
    "v_add_f32_dpp v165, v166, v165 wave_ror:1 row_mask:0xf bank_mask:0xf\n\t" \
    "v_mfma_f32_16x16x32_bf16 v[96:99], v[16:19], v[136:139], v[96:99]\n\t" \
    "v_add_f32_dpp v80, v161, v80 wave_ror:1 row_mask:0xf bank_mask:0xf\n\t" \
    "v_add_f32_dpp v84, v165, v84 wave_ror:1 row_mask:0xf bank_mask:0xf\n\t" \
    "L_cv2_12:\n\t" \
    "s_cbranch_execz L_cv2_13\n\t" \
    "v_mfma_f32_16x16x32_bf16 v[100:103], v[52:55], v[136:139], v[100:103]\n\t" \
    "v_cvt_pk_bf16_f32 v160, v80, v84\n\t" \
    "ds_write_b16 %[ya], v160 offset:256\n\t" \
    "v_mfma_f32_16x16x32_bf16 v[96:99], v[12:15], v[140:143], v[96:99]\n\t" \
    "ds_write_b16_d16_hi %[ya], v160 offset:384\n\t" \
    "ds_read_b128 v[180:183], %[yr]\n\t" \
    "v_mfma_f32_16x16x32_bf16 v[100:103], v[4:7], v[140:143], v[100:103]\n\t" \
    "ds_read_b64 v[32:33], %[ub] offset:832\n\t" \
    "ds_read_b64 v[34:35], %[ub] offset:840\n\t" \
    "v_mfma_f32_16x16x32_bf16 v[96:99], v[64:67], v[144:147], v[96:99]\n\t" \
    "v_cndmask_b32_e64 v163, v91, v87, %[m3]\n\t" \
    "v_cndmask_b32_e64 v167, v95, v91, %[m3]\n\t" \
    "v_mfma_f32_16x16x32_bf16 v[100:103], v[72:75], v[144:147], v[100:103]\n\t" \
    "ds_read_u16_d16_hi v173, %[ua] offset:768\n\t" \
    "ds_read_u16_d16_hi v174, %[ua] offset:800\n\t" \
    "v_mfma_f32_16x16x32_bf16 v[96:99], v[108:111], v[148:151], v[96:99]\n\t" \
    "v_cndmask_b32_e64 v162, v90, v86, %[m2]\n\t" \
    "v_cndmask_b32_e64 v166, v94, v90, %[m2]\n\t" \
    "v_mfma_f32_16x16x32_bf16 v[100:103], v[112:115], v[148:151], v[100:103]\n\t" \
    "v_cndmask_b32_e64 v161, v89, v85, %[m1]\n\t" \
    "v_cndmask_b32_e64 v165, v93, v89, %[m1]\n\t" \
    "s_waitcnt lgkmcnt(7)\n\t" \
    "v_mfma_f32_16x16x32_bf16 v[96:99], v[116:119], v[156:159], v[96:99]\n\t" \
    "v_add_f32_dpp v162, v163, v162 wave_ror:1 row_mask:0xf bank_mask:0xf\n\t" \
    "v_mfma_f32_16x16x32_bf16 v[100:103], v[120:123], v[156:159], v[100:103]\n\t" \
    "v_add_f32_dpp v166, v167, v166 wave_ror:1 row_mask:0xf bank_mask:0xf\n\t" \
    "v_fmac_f32 v88, %[dd], v171\n\t" \
    "v_mfma_f32_16x16x32_bf16 v[104:107], v[16:19], v[132:135], v[104:107]\n\t" \
    "v_fmac_f32 v92, %[dd], v172\n\t" \
    "v_add_f32_dpp v161, v162, v161 wave_ror:1 row_mask:0xf bank_mask:0xf\n\t" \
    "v_mfma_f32_16x16x32_bf16 v[104:107], v[12:15], v[136:139], v[104:107]\n\t" \
    "ds_read_u16_d16_hi v175, %[ua] offset:832\n\t" \
    "L_cv2_13:\n\t" \
    "s_cbranch_execz L_cv2_14\n\t" \
    "v_add_f32_dpp v165, v166, v165 wave_ror:1 row_mask:0xf bank_mask:0xf\n\t" \
    "v_mfma_f32_16x16x32_bf16 v[104:107], v[64:67], v[140:143], v[104:107]\n\t" \
    "v_add_f32_dpp v88, v161, v88 wave_ror:1 row_mask:0xf bank_mask:0xf\n\t" \
    "v_add_f32_dpp v92, v165, v92 wave_ror:1 row_mask:0xf bank_mask:0xf\n\t" \
    "v_mfma_f32_16x16x32_bf16 v[104:107], v[108:111], v[144:147], v[104:107]\n\t" \
    "v_cvt_pk_bf16_f32 v160, v88, v92\n\t" \
    "ds_write_b16 %[ya], v160 offset:0\n\t" \
    "v_mfma_f32_16x16x32_bf16 v[104:107], v[116:119], v[148:151], v[104:107]\n\t" \
    "ds_write_b16_d16_hi %[ya], v160 offset:128\n\t" \
    "v_cndmask_b32_e64 v163, v99, v95, %[m3]\n\t" \
    "s_waitcnt lgkmcnt(5)\n\t" \
    "v_mfma_f32_16x16x32_bf16 v[104:107], v[32:35], v[156:159], v[104:107]\n\t" \
    "v_cndmask_b32_e64 v167, v103, v99, %[m3]\n\t" \
    "v_cndmask_b32_e64 v162, v98, v94, %[m2]\n\t" \
    "v_cndmask_b32_e64 v166, v102, v98, %[m2]\n\t" \
    "v_cndmask_b32_e64 v161, v97, v93, %[m1]\n\t" \
    "v_cndmask_b32_e64 v165, v101, v97, %[m1]\n\t" \
    "v_add_f32_dpp v162, v163, v162 wave_ror:1 row_mask:0xf bank_mask:0xf\n\t" \
    "v_add_f32_dpp v166, v167, v166 wave_ror:1 row_mask:0xf bank_mask:0xf\n\t" \
    "s_waitcnt lgkmcnt(3)\n\t" \
    "v_fmac_f32 v96, %[dd], v173\n\t" \
    "v_fmac_f32 v100, %[dd], v174\n\t" \
    "v_add_f32_dpp v161, v162, v161 wave_ror:1 row_mask:0xf bank_mask:0xf\n\t" \
    "v_add_f32_dpp v165, v166, v165 wave_ror:1 row_mask:0xf bank_mask:0xf\n\t" \
    "s_nop 0\n\t" \
    "v_add_f32_dpp v96, v161, v96 wave_ror:1 row_mask:0xf bank_mask:0xf\n\t" \
    "v_add_f32_dpp v100, v165, v100 wave_ror:1 row_mask:0xf bank_mask:0xf\n\t" \
    "v_cvt_pk_bf16_f32 v160, v96, v100\n\t" \
    "ds_write_b16 %[ya], v160 offset:256\n\t" \
    "ds_write_b16_d16_hi %[ya], v160 offset:384\n\t" \
    "v_cndmask_b32_e64 v163, v107, v103, %[m3]\n\t" \
    "v_cndmask_b32_e64 v162, v106, v102, %[m2]\n\t" \
    "v_cndmask_b32_e64 v161, v105, v101, %[m1]\n\t" \
    "L_cv2_14:\n\t" \
    "s_cbranch_execz L_cv2_15\n\t" \
    "v_add_f32_dpp v162, v163, v162 wave_ror:1 row_mask:0xf bank_mask:0xf\n\t" \
    "s_waitcnt lgkmcnt(4)\n\t" \
    "v_fmac_f32 v104, %[dd], v175\n\t" \
    "v_add_f32_dpp v161, v162, v161 wave_ror:1 row_mask:0xf bank_mask:0xf\n\t" \
    "s_nop 1\n\t" \
    "v_add_f32_dpp v104, v161, v104 wave_ror:1 row_mask:0xf bank_mask:0xf\n\t" \
    "v_cvt_pk_bf16_f32 v160, v104, v104\n\t" \
    "ds_write_b16 %[ya], v160 offset:512\n\t" \
    "ds_read_b128 v[184:187], %[yr]\n\t" \
    "s_waitcnt vmcnt(0)\n\t" \
    "s_mov_b32 exec_hi, 0xffffff\n\t" \
    "buffer_store_dwordx4 v[176:179], %[l16], %[rs], %[yo] offen offset:1408 sc1\n\t" \
    "s_mov_b32 exec_hi, -1\n\t" \
    "s_mov_b32 exec_hi, 0x0\n\t" \
    "buffer_store_dwordx4 v[180:183], %[l16], %[rs], %[yo] offen offset:2304 sc1\n\t" \
    "s_mov_b32 exec_hi, -1\n\t" \
    "s_waitcnt lgkmcnt(0)\n\t" \
    "s_mov_b32 exec_hi, 0xff\n\t" \
    "buffer_store_dwordx4 v[184:187], %[l16], %[rs], %[yo] offen offset:2816 sc1\n\t" \
    "s_mov_b32 exec_hi, -1\n\t" \
    "s_nop 1\n\t" \
    "L_cv2_15:\n\t" \
    "s_cbranch_execz L_back\n\t" \
    "s_branch CVE_%=\n\t" \
    "L_cv3_0:\n\t" \
    "s_cbranch_execz L_cv3_1\n\t" \
    "ds_read_b64 v[0:1], %[ub] offset:0\n\t" \
    "ds_read_b64 v[2:3], %[ub] offset:8\n\t" \
    "ds_read_b64_tr_b16 v[4:5], %[hb] offset:32\n\t" \
    "ds_read_b64_tr_b16 v[6:7], %[hb] offset:0\n\t" \
    "ds_read_b64 v[8:9], %[ub] offset:32\n\t" \
    "ds_read_b64 v[10:11], %[ub] offset:40\n\t" \
    "ds_read_b64_tr_b16 v[12:13], %[hb] offset:1056\n\t" \
    "ds_read_b64_tr_b16 v[14:15], %[hb] offset:1024\n\t" \
    "ds_read_b64_tr_b16 v[16:17], %[hb] offset:1312\n\t" \
    "ds_read_b64_tr_b16 v[18:19], %[hb] offset:1280\n\t" \
    "ds_read_b64_tr_b16 v[20:21], %[hb] offset:1568\n\t" \
    "ds_read_b64_tr_b16 v[22:23], %[hb] offset:1536\n\t" \
    "v_mov_b32 v168, 0\n\t" \
    "v_mov_b32 v169, 0\n\t" \
    "v_mov_b32 v170, 0\n\t" \
    "v_mov_b32 v171, 0\n\t" \
    "v_mov_b32 v172, 0\n\t" \
    "v_mov_b32 v173, 0\n\t" \
    "v_mov_b32 v174, 0\n\t" \
    "v_mov_b32 v175, 0\n\t" \
    "s_waitcnt lgkmcnt(8)\n\t" \
    "v_mfma_f32_16x16x32_bf16 v[24:27], v[0:3], v[4:7], 0\n\t" \
    "ds_read_b64_tr_b16 v[28:29], %[hb] offset:1824\n\t" \
    "ds_read_b64_tr_b16 v[30:31], %[hb] offset:1792\n\t" \
    "ds_read_b64_tr_b16 v[32:33], %[hb] offset:288\n\t" \
    "s_waitcnt lgkmcnt(9)\n\t" \
    "v_mfma_f32_16x16x32_bf16 v[36:39], v[8:11], v[4:7], 0\n\t" \
    "ds_read_b64_tr_b16 v[34:35], %[hb] offset:256\n\t" \
    "ds_read_u16_d16_hi v168, %[ua] offset:0\n\t" \
    "ds_read_u16_d16_hi v169, %[ua] offset:32\n\t" \
    "s_waitcnt lgkmcnt(10)\n\t" \
    "v_mfma_f32_16x16x32_bf16 v[40:43], v[0:3], v[12:15], 0\n\t" \
    "ds_read_b64 v[44:45], %[ub] offset:64\n\t" \
    "L_cv3_1:\n\t" \
    "s_cbranch_execz L_cv3_2\n\t" \
    "v_mfma_f32_16x16x32_bf16 v[48:51], v[8:11], v[12:15], 0\n\t" \
    "ds_read_b64 v[46:47], %[ub] offset:72\n\t" \
    "ds_read_b64 v[52:53], %[ub] offset:96\n\t" \
    "s_waitcnt lgkmcnt(11)\n\t" \
    "v_mfma_f32_16x16x32_bf16 v[56:59], v[0:3], v[16:19], 0\n\t" \
    "ds_read_b64 v[54:55], %[ub] offset:104\n\t" \
    "v_mfma_f32_16x16x32_bf16 v[60:63], v[8:11], v[16:19], 0\n\t" \
    "v_cndmask_b32_e64 v163, v27, 0, %[m3]\n\t" \
    "v_cndmask_b32_e64 v167, v39, v27, %[m3]\n\t" \
    "s_waitcnt lgkmcnt(10)\n\t" \
    "v_mfma_f32_16x16x32_bf16 v[64:67], v[0:3], v[20:23], 0\n\t" \
    "v_cndmask_b32_e64 v162, v26, 0, %[m2]\n\t" \
    "v_mfma_f32_16x16x32_bf16 v[68:71], v[8:11], v[20:23], 0\n\t" \
    "v_cndmask_b32_e64 v166, v38, v26, %[m2]\n\t" \
    "v_cndmask_b32_e64 v161, v25, 0, %[m1]\n\t" \
    "s_waitcnt lgkmcnt(8)\n\t" \
    "v_mfma_f32_16x16x32_bf16 v[72:75], v[0:3], v[28:31], 0\n\t" \
    "ds_read_b64_tr_b16 v[76:77], %[hb] offset:544\n\t" \
    "v_mfma_f32_16x16x32_bf16 v[80:83], v[8:11], v[28:31], 0\n\t" \
    "ds_read_b64_tr_b16 v[78:79], %[hb] offset:512\n\t" \
    "ds_read_u16_d16_hi v170, %[ua] offset:64\n\t" \
    "s_waitcnt lgkmcnt(9)\n\t" \
    "v_mfma_f32_16x16x32_bf16 v[84:87], v[0:3], v[32:35], 0\n\t" \
    "ds_read_u16_d16_hi v171, %[ua] offset:96\n\t" \
    "v_mfma_f32_16x16x32_bf16 v[88:91], v[8:11], v[32:35], 0\n\t" \
    "v_cndmask_b32_e64 v165, v37, v25, %[m1]\n\t" \
    "v_add_f32_dpp v162, v163, v162 wave_ror:1 row_mask:0xf bank_mask:0xf\n\t" \
    "s_waitcnt lgkmcnt(4)\n\t" \
    "v_mfma_f32_16x16x32_bf16 v[84:87], v[44:47], v[4:7], v[84:87]\n\t" \
    "ds_read_b64 v[92:93], %[ub] offset:128\n\t" \
    "v_mfma_f32_16x16x32_bf16 v[88:91], v[52:55], v[4:7], v[88:91]\n\t" \
    "ds_read_b64 v[94:95], %[ub] offset:136\n\t" \
    "ds_read_b64 v[96:97], %[ub] offset:160\n\t" \
    "v_mfma_f32_16x16x32_bf16 v[56:59], v[44:47], v[12:15], v[56:59]\n\t" \
    "L_cv3_2:\n\t" \
    "s_cbranch_execz L_cv3_3\n\t" \
    "ds_read_b64 v[98:99], %[ub] offset:168\n\t" \
    "v_add_f32_dpp v166, v167, v166 wave_ror:1 row_mask:0xf bank_mask:0xf\n\t" \
    "v_mfma_f32_16x16x32_bf16 v[60:63], v[52:55], v[12:15], v[60:63]\n\t" \
    "v_fmac_f32 v24, %[dd], v168\n\t" \
    "v_fmac_f32 v36, %[dd], v169\n\t" \
    "v_mfma_f32_16x16x32_bf16 v[64:67], v[44:47], v[16:19], v[64:67]\n\t" \
    "v_add_f32_dpp v161, v162, v161 wave_ror:1 row_mask:0xf bank_mask:0xf\n\t" \
    "v_add_f32_dpp v165, v166, v165 wave_ror:1 row_mask:0xf bank_mask:0xf\n\t" \
    "v_mfma_f32_16x16x32_bf16 v[68:71], v[52:55], v[16:19], v[68:71]\n\t" \
    "v_add_f32_dpp v24, v161, v24 wave_ror:1 row_mask:0xf bank_mask:0xf\n\t" \
    "v_add_f32_dpp v36, v165, v36 wave_ror:1 row_mask:0xf bank_mask:0xf\n\t" \
    "v_mfma_f32_16x16x32_bf16 v[72:75], v[44:47], v[20:23], v[72:75]\n\t" \
    "ds_read_b64_tr_b16 v[100:101], %[hb] offset:800\n\t" \
    "ds_read_b64_tr_b16 v[102:103], %[hb] offset:768\n\t" \
    "v_mfma_f32_16x16x32_bf16 v[80:83], v[52:55], v[20:23], v[80:83]\n\t" \
    "v_cvt_pk_bf16_f32 v160, v24, v36\n\t" \
    "ds_write_b16 %[ya], v160 offset:0\n\t" \
    "s_waitcnt lgkmcnt(9)\n\t" \
    "v_mfma_f32_16x16x32_bf16 v[104:107], v[0:3], v[76:79], 0\n\t" \
    "ds_write_b16_d16_hi %[ya], v160 offset:128\n\t" \
    "v_mfma_f32_16x16x32_bf16 v[108:111], v[8:11], v[76:79], 0\n\t" \
    "ds_read_u16_d16_hi v172, %[ua] offset:128\n\t" \
    "ds_read_u16_d16_hi v173, %[ua] offset:160\n\t" \
    "v_mfma_f32_16x16x32_bf16 v[104:107], v[44:47], v[32:35], v[104:107]\n\t" \
    "v_cndmask_b32_e64 v163, v87, v39, %[m3]\n\t" \
    "v_cndmask_b32_e64 v167, v91, v87, %[m3]\n\t" \
    "v_mfma_f32_16x16x32_bf16 v[108:111], v[52:55], v[32:35], v[108:111]\n\t" \
    "v_cndmask_b32_e64 v162, v86, v38, %[m2]\n\t" \
    "v_cndmask_b32_e64 v166, v90, v86, %[m2]\n\t" \
    "s_waitcnt lgkmcnt(6)\n\t" \
    "v_mfma_f32_16x16x32_bf16 v[104:107], v[92:95], v[4:7], v[104:107]\n\t" \
    "ds_read_b64 v[112:113], %[ub] offset:192\n\t" \
    "L_cv3_3:\n\t" \
    "s_cbranch_execz L_cv3_4\n\t" \
    "v_mfma_f32_16x16x32_bf16 v[108:111], v[96:99], v[4:7], v[108:111]\n\t" \
    "ds_read_b64 v[114:115], %[ub] offset:200\n\t" \
    "ds_read_b64 v[116:117], %[ub] offset:224\n\t" \
    "v_mfma_f32_16x16x32_bf16 v[64:67], v[92:95], v[12:15], v[64:67]\n\t" \
    "ds_read_b64 v[118:119], %[ub] offset:232\n\t" \
    "v_cndmask_b32_e64 v161, v85, v37, %[m1]\n\t" \
    "v_mfma_f32_16x16x32_bf16 v[68:71], v[96:99], v[12:15], v[68:71]\n\t" \
    "v_cndmask_b32_e64 v165, v89, v85, %[m1]\n\t" \
    "v_add_f32_dpp v162, v163, v162 wave_ror:1 row_mask:0xf bank_mask:0xf\n\t" \
    "v_mfma_f32_16x16x32_bf16 v[72:75], v[92:95], v[16:19], v[72:75]\n\t" \
    "v_add_f32_dpp v166, v167, v166 wave_ror:1 row_mask:0xf bank_mask:0xf\n\t" \
    "v_fmac_f32 v84, %[dd], v170\n\t" \
    "v_mfma_f32_16x16x32_bf16 v[80:83], v[96:99], v[16:19], v[80:83]\n\t" \
    "v_fmac_f32 v88, %[dd], v171\n\t" \
    "v_add_f32_dpp v161, v162, v161 wave_ror:1 row_mask:0xf bank_mask:0xf\n\t" \
    "s_waitcnt lgkmcnt(8)\n\t" \
    "v_mfma_f32_16x16x32_bf16 v[120:123], v[0:3], v[100:103], 0\n\t" \
    "v_add_f32_dpp v165, v166, v165 wave_ror:1 row_mask:0xf bank_mask:0xf\n\t" \
    "v_mfma_f32_16x16x32_bf16 v[124:127], v[8:11], v[100:103], 0\n\t" \
    "v_add_f32_dpp v84, v161, v84 wave_ror:1 row_mask:0xf bank_mask:0xf\n\t" \
    "v_add_f32_dpp v88, v165, v88 wave_ror:1 row_mask:0xf bank_mask:0xf\n\t" \
    "v_mfma_f32_16x16x32_bf16 v[120:123], v[44:47], v[76:79], v[120:123]\n\t" \
    "v_cvt_pk_bf16_f32 v160, v84, v88\n\t" \
    "ds_write_b16 %[ya], v160 offset:256\n\t" \
    "v_mfma_f32_16x16x32_bf16 v[124:127], v[52:55], v[76:79], v[124:127]\n\t" \
    "ds_read_u16_d16_hi v174, %[ua] offset:192\n\t" \
    "ds_read_u16_d16_hi v175, %[ua] offset:224\n\t" \
    "v_mfma_f32_16x16x32_bf16 v[120:123], v[92:95], v[32:35], v[120:123]\n\t" \
    "ds_read_b64 v[128:129], %[ub] offset:256\n\t" \
    "ds_read_b64 v[130:131], %[ub] offset:264\n\t" \
    "v_mfma_f32_16x16x32_bf16 v[124:127], v[96:99], v[32:35], v[124:127]\n\t" \
    "ds_read_b64 v[132:133], %[ub] offset:288\n\t" \
    "L_cv3_4:\n\t" \
    "s_cbranch_execz L_cv3_5\n\t" \
    "ds_read_b64 v[134:135], %[ub] offset:296\n\t" \
    "s_waitcnt lgkmcnt(7)\n\t" \
    "v_mfma_f32_16x16x32_bf16 v[120:123], v[112:115], v[4:7], v[120:123]\n\t" \
    "ds_write_b16_d16_hi %[ya], v160 offset:384\n\t" \
    "v_mfma_f32_16x16x32_bf16 v[124:127], v[116:119], v[4:7], v[124:127]\n\t" \
    "v_cndmask_b32_e64 v163, v107, v91, %[m3]\n\t" \
    "v_cndmask_b32_e64 v167, v111, v107, %[m3]\n\t" \
    "v_mfma_f32_16x16x32_bf16 v[72:75], v[112:115], v[12:15], v[72:75]\n\t" \
    "v_cndmask_b32_e64 v162, v106, v90, %[m2]\n\t" \
    "v_cndmask_b32_e64 v166, v110, v106, %[m2]\n\t" \
    "v_mfma_f32_16x16x32_bf16 v[80:83], v[116:119], v[12:15], v[80:83]\n\t" \
    "v_cndmask_b32_e64 v161, v105, v89, %[m1]\n\t" \
    "v_cndmask_b32_e64 v165, v109, v105, %[m1]\n\t" \
    "v_mfma_f32_16x16x32_bf16 v[40:43], v[44:47], v[100:103], v[40:43]\n\t" \
    "v_add_f32_dpp v162, v163, v162 wave_ror:1 row_mask:0xf bank_mask:0xf\n\t" \
    "v_add_f32_dpp v166, v167, v166 wave_ror:1 row_mask:0xf bank_mask:0xf\n\t" \
    "v_mfma_f32_16x16x32_bf16 v[48:51], v[52:55], v[100:103], v[48:51]\n\t" \
    "v_fmac_f32 v104, %[dd], v172\n\t" \
    "v_fmac_f32 v108, %[dd], v173\n\t" \
    "v_mfma_f32_16x16x32_bf16 v[40:43], v[92:95], v[76:79], v[40:43]\n\t" \
    "ds_read_b64 v[136:137], %[ub] offset:320\n\t" \
    "ds_read_b64 v[138:139], %[ub] offset:328\n\t" \
    "v_mfma_f32_16x16x32_bf16 v[48:51], v[96:99], v[76:79], v[48:51]\n\t" \
    "ds_read_b64 v[140:141], %[ub] offset:352\n\t" \
    "ds_read_b64 v[142:143], %[ub] offset:360\n\t" \
    "v_mfma_f32_16x16x32_bf16 v[40:43], v[112:115], v[32:35], v[40:43]\n\t" \
    "ds_read_u16_d16_hi v168, %[ua] offset:256\n\t" \
    "ds_read_u16_d16_hi v169, %[ua] offset:288\n\t" \
    "v_mfma_f32_16x16x32_bf16 v[48:51], v[116:119], v[32:35], v[48:51]\n\t" \
    "v_add_f32_dpp v161, v162, v161 wave_ror:1 row_mask:0xf bank_mask:0xf\n\t" \
    "v_add_f32_dpp v165, v166, v165 wave_ror:1 row_mask:0xf bank_mask:0xf\n\t" \
    "s_waitcnt lgkmcnt(7)\n\t" \
    "L_cv3_5:\n\t" \
    "s_cbranch_execz L_cv3_6\n\t" \
    "v_mfma_f32_16x16x32_bf16 v[40:43], v[128:131], v[4:7], v[40:43]\n\t" \
    "v_add_f32_dpp v104, v161, v104 wave_ror:1 row_mask:0xf bank_mask:0xf\n\t" \
    "v_mfma_f32_16x16x32_bf16 v[48:51], v[132:135], v[4:7], v[48:51]\n\t" \
    "v_add_f32_dpp v108, v165, v108 wave_ror:1 row_mask:0xf bank_mask:0xf\n\t" \
    "v_cvt_pk_bf16_f32 v160, v104, v108\n\t" \
    "v_mfma_f32_16x16x32_bf16 v[56:59], v[92:95], v[100:103], v[56:59]\n\t" \
    "ds_write_b16 %[ya], v160 offset:512\n\t" \
    "ds_write_b16_d16_hi %[ya], v160 offset:640\n\t" \
    "v_mfma_f32_16x16x32_bf16 v[60:63], v[96:99], v[100:103], v[60:63]\n\t" \
    "v_cndmask_b32_e64 v163, v123, v111, %[m3]\n\t" \
    "v_cndmask_b32_e64 v167, v127, v123, %[m3]\n\t" \
    "v_mfma_f32_16x16x32_bf16 v[56:59], v[112:115], v[76:79], v[56:59]\n\t" \
    "ds_read_b64 v[144:145], %[ub] offset:384\n\t" \
    "ds_read_b64 v[146:147], %[ub] offset:392\n\t" \
    "v_mfma_f32_16x16x32_bf16 v[60:63], v[116:119], v[76:79], v[60:63]\n\t" \
    "ds_read_b64 v[148:149], %[ub] offset:416\n\t" \
    "ds_read_b64 v[150:151], %[ub] offset:424\n\t" \
    "v_mfma_f32_16x16x32_bf16 v[56:59], v[128:131], v[32:35], v[56:59]\n\t" \
    "ds_read_u16_d16_hi v170, %[ua] offset:320\n\t" \
    "ds_read_u16_d16_hi v171, %[ua] offset:352\n\t" \
    "v_mfma_f32_16x16x32_bf16 v[60:63], v[132:135], v[32:35], v[60:63]\n\t" \
    "v_cndmask_b32_e64 v162, v122, v110, %[m2]\n\t" \
    "v_cndmask_b32_e64 v166, v126, v122, %[m2]\n\t" \
    "s_waitcnt lgkmcnt(10)\n\t" \
    "v_mfma_f32_16x16x32_bf16 v[56:59], v[136:139], v[4:7], v[56:59]\n\t" \
    "v_cndmask_b32_e64 v161, v121, v109, %[m1]\n\t" \
    "v_mfma_f32_16x16x32_bf16 v[60:63], v[140:143], v[4:7], v[60:63]\n\t" \
    "v_cndmask_b32_e64 v165, v125, v121, %[m1]\n\t" \
    "v_add_f32_dpp v162, v163, v162 wave_ror:1 row_mask:0xf bank_mask:0xf\n\t" \
    "v_mfma_f32_16x16x32_bf16 v[64:67], v[112:115], v[100:103], v[64:67]\n\t" \
    "v_add_f32_dpp v166, v167, v166 wave_ror:1 row_mask:0xf bank_mask:0xf\n\t" \
    "v_fmac_f32 v120, %[dd], v174\n\t" \
    "L_cv3_6:\n\t" \
    "s_cbranch_execz L_cv3_7\n\t" \
    "v_mfma_f32_16x16x32_bf16 v[68:71], v[116:119], v[100:103], v[68:71]\n\t" \
    "v_fmac_f32 v124, %[dd], v175\n\t" \
    "v_add_f32_dpp v161, v162, v161 wave_ror:1 row_mask:0xf bank_mask:0xf\n\t" \
    "v_mfma_f32_16x16x32_bf16 v[64:67], v[128:131], v[76:79], v[64:67]\n\t" \
    "ds_read_b64 v[152:153], %[ub] offset:448\n\t" \
    "ds_read_b64 v[154:155], %[ub] offset:456\n\t" \
    "v_mfma_f32_16x16x32_bf16 v[68:71], v[132:135], v[76:79], v[68:71]\n\t" \
    "ds_read_b64 v[156:157], %[ub] offset:480\n\t" \
    "ds_read_b64 v[158:159], %[ub] offset:488\n\t" \
    "v_mfma_f32_16x16x32_bf16 v[64:67], v[136:139], v[32:35], v[64:67]\n\t" \
    "ds_read_b64 v[28:29], %[ub2] offset:0\n\t" \
    "ds_read_b64 v[30:31], %[ub2] offset:8\n\t" \
    "v_mfma_f32_16x16x32_bf16 v[68:71], v[140:143], v[32:35], v[68:71]\n\t" \
    "ds_read_b64_tr_b16 v[24:25], %[hb2] offset:3360\n\t" \
    "ds_read_b64_tr_b16 v[26:27], %[hb2] offset:3328\n\t" \
    "s_waitcnt lgkmcnt(10)\n\t" \
    "v_mfma_f32_16x16x32_bf16 v[64:67], v[144:147], v[4:7], v[64:67]\n\t" \
    "ds_read_b64 v[20:21], %[ub2] offset:32\n\t" \
    "v_mfma_f32_16x16x32_bf16 v[68:71], v[148:151], v[4:7], v[68:71]\n\t" \
    "ds_read_b64 v[22:23], %[ub2] offset:40\n\t" \
    "ds_read_b64_tr_b16 v[16:17], %[hb2] offset:3616\n\t" \
    "ds_read_u16_d16_hi v172, %[ua] offset:384\n\t" \
    "ds_read_u16_d16_hi v173, %[ua] offset:416\n\t" \
    "v_mfma_f32_16x16x32_bf16 v[72:75], v[128:131], v[100:103], v[72:75]\n\t" \
    "ds_read_b64_tr_b16 v[18:19], %[hb2] offset:3584\n\t" \
    "ds_read_b64_tr_b16 v[0:1], %[hb2] offset:3872\n\t" \
    "v_mfma_f32_16x16x32_bf16 v[80:83], v[132:135], v[100:103], v[80:83]\n\t" \
    "ds_read_b64_tr_b16 v[2:3], %[hb2] offset:3840\n\t" \
    "v_add_f32_dpp v165, v166, v165 wave_ror:1 row_mask:0xf bank_mask:0xf\n\t" \
    "v_mfma_f32_16x16x32_bf16 v[72:75], v[136:139], v[76:79], v[72:75]\n\t" \
    "ds_read_b64 v[8:9], %[ub2] offset:64\n\t" \
    "ds_read_b64 v[10:11], %[ub2] offset:72\n\t" \
    "L_cv3_7:\n\t" \
    "s_cbranch_execz L_cv3_8\n\t" \
    "v_mfma_f32_16x16x32_bf16 v[80:83], v[140:143], v[76:79], v[80:83]\n\t" \
    "ds_read_b64_tr_b16 v[36:37], %[hb2] offset:3104\n\t" \
    "ds_read_b64_tr_b16 v[38:39], %[hb2] offset:3072\n\t" \
    "v_mfma_f32_16x16x32_bf16 v[72:75], v[144:147], v[32:35], v[72:75]\n\t" \
    "ds_read_b64 v[84:85], %[ub2] offset:96\n\t" \
    "ds_read_b64 v[86:87], %[ub2] offset:104\n\t" \
    "v_mfma_f32_16x16x32_bf16 v[80:83], v[148:151], v[32:35], v[80:83]\n\t" \
    "v_add_f32_dpp v120, v161, v120 wave_ror:1 row_mask:0xf bank_mask:0xf\n\t" \
    "v_add_f32_dpp v124, v165, v124 wave_ror:1 row_mask:0xf bank_mask:0xf\n\t" \
    "s_waitcnt lgkmcnt(14)\n\t" \
    "v_mfma_f32_16x16x32_bf16 v[72:75], v[152:155], v[4:7], v[72:75]\n\t" \
    "v_cvt_pk_bf16_f32 v160, v120, v124\n\t" \
    "v_mfma_f32_16x16x32_bf16 v[80:83], v[156:159], v[4:7], v[80:83]\n\t" \
    "ds_read_u16_d16_hi v174, %[ua] offset:448\n\t" \
    "ds_read_u16_d16_hi v175, %[ua] offset:480\n\t" \
    "v_mfma_f32_16x16x32_bf16 v[12:15], v[28:31], v[24:27], 0\n\t" \
    "ds_read_b64 v[44:45], %[ub2] offset:128\n\t" \
    "ds_read_b64 v[46:47], %[ub2] offset:136\n\t" \
    "s_waitcnt lgkmcnt(14)\n\t" \
    "v_mfma_f32_16x16x32_bf16 v[52:55], v[20:23], v[24:27], 0\n\t" \
    "ds_read_b64_tr_b16 v[88:89], %[hb2] offset:2848\n\t" \
    "s_waitcnt lgkmcnt(13)\n\t" \
    "v_mfma_f32_16x16x32_bf16 v[104:107], v[28:31], v[16:19], 0\n\t" \
    "ds_read_b64_tr_b16 v[90:91], %[hb2] offset:2816\n\t" \
    "v_mfma_f32_16x16x32_bf16 v[92:95], v[20:23], v[16:19], 0\n\t" \
    "ds_read_b64 v[96:97], %[ub2] offset:160\n\t" \
    "ds_read_b64 v[98:99], %[ub2] offset:168\n\t" \
    "s_waitcnt lgkmcnt(14)\n\t" \
    "v_mfma_f32_16x16x32_bf16 v[112:115], v[28:31], v[0:3], 0\n\t" \
    "ds_write_b16 %[ya], v160 offset:768\n\t" \
    "v_mfma_f32_16x16x32_bf16 v[116:119], v[20:23], v[0:3], 0\n\t" \
    "ds_write_b16_d16_hi %[ya], v160 offset:896\n\t" \
    "ds_read_b128 v[176:179], %[yr]\n\t" \
    "s_waitcnt lgkmcnt(11)\n\t" \
    "L_cv3_8:\n\t" \
    "s_cbranch_execz L_cv3_9\n\t" \
    "v_mfma_f32_16x16x32_bf16 v[12:15], v[8:11], v[36:39], v[12:15]\n\t" \
    "ds_read_b64 v[128:129], %[ub2] offset:192\n\t" \
    "v_mfma_f32_16x16x32_bf16 v[52:55], v[84:87], v[36:39], v[52:55]\n\t" \
    "ds_read_b64 v[130:131], %[ub2] offset:200\n\t" \
    "ds_read_b64_tr_b16 v[132:133], %[hb2] offset:2592\n\t" \
    "v_mfma_f32_16x16x32_bf16 v[104:107], v[8:11], v[24:27], v[104:107]\n\t" \
    "ds_read_b64_tr_b16 v[134:135], %[hb2] offset:2560\n\t" \
    "ds_read_b64 v[100:101], %[ub2] offset:224\n\t" \
    "v_mfma_f32_16x16x32_bf16 v[92:95], v[84:87], v[24:27], v[92:95]\n\t" \
    "ds_read_b64 v[102:103], %[ub2] offset:232\n\t" \
    "v_cndmask_b32_e64 v163, v43, v127, %[m3]\n\t" \
    "v_mfma_f32_16x16x32_bf16 v[112:115], v[8:11], v[16:19], v[112:115]\n\t" \
    "v_cndmask_b32_e64 v167, v51, v43, %[m3]\n\t" \
    "v_cndmask_b32_e64 v162, v42, v126, %[m2]\n\t" \
    "v_mfma_f32_16x16x32_bf16 v[116:119], v[84:87], v[16:19], v[116:119]\n\t" \
    "v_cndmask_b32_e64 v166, v50, v42, %[m2]\n\t" \
    "v_cndmask_b32_e64 v161, v41, v125, %[m1]\n\t" \
    "s_waitcnt lgkmcnt(9)\n\t" \
    "v_mfma_f32_16x16x32_bf16 v[12:15], v[44:47], v[88:91], v[12:15]\n\t" \
    "ds_read_b64 v[136:137], %[ub2] offset:256\n\t" \
    "v_mfma_f32_16x16x32_bf16 v[52:55], v[96:99], v[88:91], v[52:55]\n\t" \
    "ds_read_b64 v[138:139], %[ub2] offset:264\n\t" \
    "ds_read_b64_tr_b16 v[140:141], %[hb2] offset:2336\n\t" \
    "v_mfma_f32_16x16x32_bf16 v[104:107], v[44:47], v[36:39], v[104:107]\n\t" \
    "ds_read_b64_tr_b16 v[142:143], %[hb2] offset:2304\n\t" \
    "ds_read_b64 v[76:77], %[ub2] offset:288\n\t" \
    "v_mfma_f32_16x16x32_bf16 v[92:95], v[96:99], v[36:39], v[92:95]\n\t" \
    "ds_read_b64 v[78:79], %[ub2] offset:296\n\t" \
    "v_cndmask_b32_e64 v165, v49, v41, %[m1]\n\t" \
    "v_mfma_f32_16x16x32_bf16 v[112:115], v[44:47], v[24:27], v[112:115]\n\t" \
    "v_add_f32_dpp v162, v163, v162 wave_ror:1 row_mask:0xf bank_mask:0xf\n\t" \
    "v_add_f32_dpp v166, v167, v166 wave_ror:1 row_mask:0xf bank_mask:0xf\n\t" \
    "L_cv3_9:\n\t" \
    "s_cbranch_execz L_cv3_10\n\t" \
    "v_mfma_f32_16x16x32_bf16 v[116:119], v[96:99], v[24:27], v[116:119]\n\t" \
    "v_fmac_f32 v40, %[dd], v168\n\t" \
    "v_fmac_f32 v48, %[dd], v169\n\t" \
    "s_waitcnt lgkmcnt(6)\n\t" \
    "v_mfma_f32_16x16x32_bf16 v[12:15], v[128:131], v[132:135], v[12:15]\n\t" \
    "ds_read_b64 v[144:145], %[ub2] offset:320\n\t" \
    "v_mfma_f32_16x16x32_bf16 v[52:55], v[100:103], v[132:135], v[52:55]\n\t" \
    "ds_read_b64 v[146:147], %[ub2] offset:328\n\t" \
    "ds_read_b64_tr_b16 v[148:149], %[hb2] offset:2080\n\t" \
    "v_mfma_f32_16x16x32_bf16 v[104:107], v[128:131], v[88:91], v[104:107]\n\t" \
    "ds_read_b64_tr_b16 v[150:151], %[hb2] offset:2048\n\t" \
    "ds_read_b64 v[32:33], %[ub2] offset:352\n\t" \
    "v_mfma_f32_16x16x32_bf16 v[92:95], v[100:103], v[88:91], v[92:95]\n\t" \
    "ds_read_b64 v[34:35], %[ub2] offset:360\n\t" \
    "v_add_f32_dpp v161, v162, v161 wave_ror:1 row_mask:0xf bank_mask:0xf\n\t" \
    "v_mfma_f32_16x16x32_bf16 v[112:115], v[128:131], v[36:39], v[112:115]\n\t" \
    "v_add_f32_dpp v165, v166, v165 wave_ror:1 row_mask:0xf bank_mask:0xf\n\t" \
    "v_add_f32_dpp v40, v161, v40 wave_ror:1 row_mask:0xf bank_mask:0xf\n\t" \
    "v_mfma_f32_16x16x32_bf16 v[116:119], v[100:103], v[36:39], v[116:119]\n\t" \
    "v_add_f32_dpp v48, v165, v48 wave_ror:1 row_mask:0xf bank_mask:0xf\n\t" \
    "v_cvt_pk_bf16_f32 v160, v40, v48\n\t" \
    "s_waitcnt lgkmcnt(6)\n\t" \
    "v_mfma_f32_16x16x32_bf16 v[12:15], v[136:139], v[140:143], v[12:15]\n\t" \
    "ds_read_b64 v[108:109], %[ub2] offset:384\n\t" \
    "v_mfma_f32_16x16x32_bf16 v[52:55], v[76:79], v[140:143], v[52:55]\n\t" \
    "ds_read_b64 v[110:111], %[ub2] offset:392\n\t" \
    "ds_read_b64_tr_b16 v[120:121], %[hb2] offset:1824\n\t" \
    "v_mfma_f32_16x16x32_bf16 v[104:107], v[136:139], v[132:135], v[104:107]\n\t" \
    "ds_read_b64_tr_b16 v[122:123], %[hb2] offset:1792\n\t" \
    "ds_read_b64 v[152:153], %[ub2] offset:416\n\t" \
    "v_mfma_f32_16x16x32_bf16 v[92:95], v[76:79], v[132:135], v[92:95]\n\t" \
    "ds_read_b64 v[154:155], %[ub2] offset:424\n\t" \
    "L_cv3_10:\n\t" \
    "s_cbranch_execz L_cv3_11\n\t" \
    "ds_write_b16 %[ya], v160 offset:0\n\t" \
    "v_mfma_f32_16x16x32_bf16 v[112:115], v[136:139], v[88:91], v[112:115]\n\t" \
    "ds_write_b16_d16_hi %[ya], v160 offset:128\n\t" \
    "v_cndmask_b32_e64 v163, v59, v51, %[m3]\n\t" \
    "v_mfma_f32_16x16x32_bf16 v[116:119], v[76:79], v[88:91], v[116:119]\n\t" \
    "v_cndmask_b32_e64 v167, v63, v59, %[m3]\n\t" \
    "v_cndmask_b32_e64 v162, v58, v50, %[m2]\n\t" \
    "s_waitcnt lgkmcnt(8)\n\t" \
    "v_mfma_f32_16x16x32_bf16 v[12:15], v[144:147], v[148:151], v[12:15]\n\t" \
    "ds_read_b64 v[156:157], %[ub2] offset:448\n\t" \
    "v_mfma_f32_16x16x32_bf16 v[52:55], v[32:35], v[148:151], v[52:55]\n\t" \
    "ds_read_b64 v[158:159], %[ub2] offset:456\n\t" \
    "ds_read_b64_tr_b16 v[4:5], %[hb2] offset:1568\n\t" \
    "v_mfma_f32_16x16x32_bf16 v[104:107], v[144:147], v[140:143], v[104:107]\n\t" \
    "ds_read_b64_tr_b16 v[6:7], %[hb2] offset:1536\n\t" \
    "ds_read_b64 v[28:29], %[ub2] offset:480\n\t" \
    "v_mfma_f32_16x16x32_bf16 v[92:95], v[32:35], v[140:143], v[92:95]\n\t" \
    "ds_read_b64 v[30:31], %[ub2] offset:488\n\t" \
    "v_cndmask_b32_e64 v166, v62, v58, %[m2]\n\t" \
    "v_mfma_f32_16x16x32_bf16 v[112:115], v[144:147], v[132:135], v[112:115]\n\t" \
    "v_cndmask_b32_e64 v161, v57, v49, %[m1]\n\t" \
    "v_cndmask_b32_e64 v165, v61, v57, %[m1]\n\t" \
    "v_mfma_f32_16x16x32_bf16 v[116:119], v[32:35], v[132:135], v[116:119]\n\t" \
    "v_add_f32_dpp v162, v163, v162 wave_ror:1 row_mask:0xf bank_mask:0xf\n\t" \
    "v_add_f32_dpp v166, v167, v166 wave_ror:1 row_mask:0xf bank_mask:0xf\n\t" \
    "s_waitcnt lgkmcnt(8)\n\t" \
    "v_mfma_f32_16x16x32_bf16 v[12:15], v[108:111], v[120:123], v[12:15]\n\t" \
    "ds_read_b64 v[20:21], %[ub2] offset:512\n\t" \
    "v_mfma_f32_16x16x32_bf16 v[52:55], v[152:155], v[120:123], v[52:55]\n\t" \
    "ds_read_b64 v[22:23], %[ub2] offset:520\n\t" \
    "ds_read_b64_tr_b16 v[0:1], %[hb2] offset:1312\n\t" \
    "v_mfma_f32_16x16x32_bf16 v[104:107], v[108:111], v[148:151], v[104:107]\n\t" \
    "L_cv3_11:\n\t" \
    "s_cbranch_execz L_cv3_12\n\t" \
    "ds_read_b64_tr_b16 v[2:3], %[hb2] offset:1280\n\t" \
    "ds_read_b64 v[8:9], %[ub2] offset:544\n\t" \
    "v_mfma_f32_16x16x32_bf16 v[92:95], v[152:155], v[148:151], v[92:95]\n\t" \
    "ds_read_b64 v[10:11], %[ub2] offset:552\n\t" \
    "v_fmac_f32 v56, %[dd], v170\n\t" \
    "v_mfma_f32_16x16x32_bf16 v[112:115], v[108:111], v[140:143], v[112:115]\n\t" \
    "v_fmac_f32 v60, %[dd], v171\n\t" \
    "v_add_f32_dpp v161, v162, v161 wave_ror:1 row_mask:0xf bank_mask:0xf\n\t" \
    "v_mfma_f32_16x16x32_bf16 v[116:119], v[152:155], v[140:143], v[116:119]\n\t" \
    "v_add_f32_dpp v165, v166, v165 wave_ror:1 row_mask:0xf bank_mask:0xf\n\t" \
    "v_add_f32_dpp v56, v161, v56 wave_ror:1 row_mask:0xf bank_mask:0xf\n\t" \
    "s_waitcnt lgkmcnt(6)\n\t" \
    "v_mfma_f32_16x16x32_bf16 v[12:15], v[156:159], v[4:7], v[12:15]\n\t" \
    "ds_read_b64 v[84:85], %[ub2] offset:576\n\t" \
    "v_mfma_f32_16x16x32_bf16 v[52:55], v[28:31], v[4:7], v[52:55]\n\t" \
    "ds_read_b64 v[86:87], %[ub2] offset:584\n\t" \
    "ds_read_b64 v[16:17], %[ub2] offset:608\n\t" \
    "v_mfma_f32_16x16x32_bf16 v[104:107], v[156:159], v[120:123], v[104:107]\n\t" \
    "ds_read_b64 v[18:19], %[ub2] offset:616\n\t" \
    "v_add_f32_dpp v60, v165, v60 wave_ror:1 row_mask:0xf bank_mask:0xf\n\t" \
    "v_mfma_f32_16x16x32_bf16 v[92:95], v[28:31], v[120:123], v[92:95]\n\t" \
    "v_cvt_pk_bf16_f32 v160, v56, v60\n\t" \
    "ds_write_b16 %[ya], v160 offset:256\n\t" \
    "v_mfma_f32_16x16x32_bf16 v[112:115], v[156:159], v[148:151], v[112:115]\n\t" \
    "ds_read_b64 v[44:45], %[ub2] offset:640\n\t" \
    "ds_read_b64 v[46:47], %[ub2] offset:648\n\t" \
    "v_mfma_f32_16x16x32_bf16 v[116:119], v[28:31], v[148:151], v[116:119]\n\t" \
    "ds_read_b64 v[96:97], %[ub2] offset:672\n\t" \
    "ds_read_b64 v[98:99], %[ub2] offset:680\n\t" \
    "s_waitcnt lgkmcnt(9)\n\t" \
    "v_mfma_f32_16x16x32_bf16 v[12:15], v[20:23], v[0:3], v[12:15]\n\t" \
    "ds_read_b64_tr_b16 v[24:25], %[hb2] offset:1056\n\t" \
    "L_cv3_12:\n\t" \
    "s_cbranch_execz L_cv3_13\n\t" \
    "v_mfma_f32_16x16x32_bf16 v[52:55], v[8:11], v[0:3], v[52:55]\n\t" \
    "ds_read_b64_tr_b16 v[26:27], %[hb2] offset:1024\n\t" \
    "ds_write_b16_d16_hi %[ya], v160 offset:384\n\t" \
    "v_mfma_f32_16x16x32_bf16 v[104:107], v[20:23], v[4:7], v[104:107]\n\t" \
    "ds_read_b64_tr_b16 v[128:129], %[hb2] offset:800\n\t" \
    "ds_read_b64_tr_b16 v[130:131], %[hb2] offset:768\n\t" \
    "v_mfma_f32_16x16x32_bf16 v[92:95], v[8:11], v[4:7], v[92:95]\n\t" \
    "v_cndmask_b32_e64 v163, v67, v63, %[m3]\n\t" \
    "v_cndmask_b32_e64 v167, v71, v67, %[m3]\n\t" \
    "v_mfma_f32_16x16x32_bf16 v[112:115], v[20:23], v[120:123], v[112:115]\n\t" \
    "ds_read_b64 v[124:125], %[ub2] offset:704\n\t" \
    "ds_read_b64 v[126:127], %[ub2] offset:712\n\t" \
    "v_mfma_f32_16x16x32_bf16 v[116:119], v[8:11], v[120:123], v[116:119]\n\t" \
    "ds_read_b64_tr_b16 v[40:41], %[hb2] offset:544\n\t" \
    "ds_read_b64_tr_b16 v[42:43], %[hb2] offset:512\n\t" \
    "s_waitcnt lgkmcnt(14)\n\t" \
    "v_mfma_f32_16x16x32_bf16 v[104:107], v[84:87], v[0:3], v[104:107]\n\t" \
    "ds_read_b64 v[100:101], %[ub2] offset:736\n\t" \
    "v_mfma_f32_16x16x32_bf16 v[92:95], v[16:19], v[0:3], v[92:95]\n\t" \
    "ds_read_b64 v[102:103], %[ub2] offset:744\n\t" \
    "ds_read_b64 v[36:37], %[ub2] offset:768\n\t" \
    "v_mfma_f32_16x16x32_bf16 v[112:115], v[84:87], v[4:7], v[112:115]\n\t" \
    "ds_read_b64 v[38:39], %[ub2] offset:776\n\t" \
    "ds_read_b64_tr_b16 v[136:137], %[hb2] offset:288\n\t" \
    "v_mfma_f32_16x16x32_bf16 v[116:119], v[16:19], v[4:7], v[116:119]\n\t" \
    "ds_read_b64_tr_b16 v[138:139], %[hb2] offset:256\n\t" \
    "ds_read_b64 v[76:77], %[ub2] offset:800\n\t" \
    "s_waitcnt lgkmcnt(14)\n\t" \
    "v_mfma_f32_16x16x32_bf16 v[112:115], v[44:47], v[0:3], v[112:115]\n\t" \
    "ds_read_b64 v[78:79], %[ub2] offset:808\n\t" \
    "v_mfma_f32_16x16x32_bf16 v[116:119], v[96:99], v[0:3], v[116:119]\n\t" \
    "ds_read_b64 v[88:89], %[ub2] offset:832\n\t" \
    "L_cv3_13:\n\t" \
    "s_cbranch_execz L_cv3_14\n\t" \
    "ds_read_b64 v[90:91], %[ub2] offset:840\n\t" \
    "v_mfma_f32_16x16x32_bf16 v[12:15], v[84:87], v[24:27], v[12:15]\n\t" \
    "ds_read_b64_tr_b16 v[144:145], %[hb2] offset:32\n\t" \
    "ds_read_b64_tr_b16 v[146:147], %[hb2] offset:0\n\t" \
    "ds_read_b64 v[32:33], %[ub2] offset:864\n\t" \
    "v_mfma_f32_16x16x32_bf16 v[52:55], v[16:19], v[24:27], v[52:55]\n\t" \
    "ds_read_b64 v[34:35], %[ub2] offset:872\n\t" \
    "v_cndmask_b32_e64 v162, v66, v62, %[m2]\n\t" \
    "s_waitcnt lgkmcnt(14)\n\t" \
    "v_mfma_f32_16x16x32_bf16 v[12:15], v[44:47], v[128:131], v[12:15]\n\t" \
    "v_cndmask_b32_e64 v166, v70, v66, %[m2]\n\t" \
    "v_mfma_f32_16x16x32_bf16 v[52:55], v[96:99], v[128:131], v[52:55]\n\t" \
    "v_cndmask_b32_e64 v161, v65, v61, %[m1]\n\t" \
    "v_cndmask_b32_e64 v165, v69, v65, %[m1]\n\t" \
    "v_mfma_f32_16x16x32_bf16 v[12:15], v[124:127], v[40:43], v[12:15]\n\t" \
    "v_add_f32_dpp v162, v163, v162 wave_ror:1 row_mask:0xf bank_mask:0xf\n\t" \
    "v_add_f32_dpp v166, v167, v166 wave_ror:1 row_mask:0xf bank_mask:0xf\n\t" \
    "s_waitcnt lgkmcnt(8)\n\t" \
    "v_mfma_f32_16x16x32_bf16 v[52:55], v[100:103], v[40:43], v[52:55]\n\t" \
    "ds_read_u16_d16_hi v168, %[ua2] offset:864\n\t" \
    "v_mfma_f32_16x16x32_bf16 v[12:15], v[36:39], v[136:139], v[12:15]\n\t" \
    "ds_read_b64 v[132:133], %[ub2] offset:896\n\t" \
    "ds_read_b64 v[134:135], %[ub2] offset:904\n\t" \
    "s_waitcnt lgkmcnt(5)\n\t" \
    "v_mfma_f32_16x16x32_bf16 v[52:55], v[76:79], v[136:139], v[52:55]\n\t" \
    "ds_read_b64 v[108:109], %[ub2] offset:928\n\t" \
    "v_mfma_f32_16x16x32_bf16 v[12:15], v[88:91], v[144:147], v[12:15]\n\t" \
    "ds_read_b64 v[110:111], %[ub2] offset:936\n\t" \
    "v_fmac_f32 v64, %[dd], v172\n\t" \
    "s_waitcnt lgkmcnt(5)\n\t" \
    "v_mfma_f32_16x16x32_bf16 v[52:55], v[32:35], v[144:147], v[52:55]\n\t" \
    "v_fmac_f32 v68, %[dd], v173\n\t" \
    "v_mfma_f32_16x16x32_bf16 v[104:107], v[44:47], v[24:27], v[104:107]\n\t" \
    "L_cv3_14:\n\t" \
    "s_cbranch_execz L_cv3_15\n\t" \
    "v_add_f32_dpp v161, v162, v161 wave_ror:1 row_mask:0xf bank_mask:0xf\n\t" \
    "v_add_f32_dpp v165, v166, v165 wave_ror:1 row_mask:0xf bank_mask:0xf\n\t" \
    "v_mfma_f32_16x16x32_bf16 v[92:95], v[96:99], v[24:27], v[92:95]\n\t" \
    "v_add_f32_dpp v64, v161, v64 wave_ror:1 row_mask:0xf bank_mask:0xf\n\t" \
    "v_add_f32_dpp v68, v165, v68 wave_ror:1 row_mask:0xf bank_mask:0xf\n\t" \
    "v_mfma_f32_16x16x32_bf16 v[104:107], v[124:127], v[128:131], v[104:107]\n\t" \
    "v_cvt_pk_bf16_f32 v160, v64, v68\n\t" \
    "ds_write_b16 %[ya], v160 offset:512\n\t" \
    "v_mfma_f32_16x16x32_bf16 v[92:95], v[100:103], v[128:131], v[92:95]\n\t" \
    "ds_write_b16_d16_hi %[ya], v160 offset:640\n\t" \
    "v_cndmask_b32_e64 v163, v75, v71, %[m3]\n\t" \
    "v_mfma_f32_16x16x32_bf16 v[104:107], v[36:39], v[40:43], v[104:107]\n\t" \
    "v_cndmask_b32_e64 v167, v83, v75, %[m3]\n\t" \
    "v_cndmask_b32_e64 v162, v74, v70, %[m2]\n\t" \
    "v_mfma_f32_16x16x32_bf16 v[92:95], v[76:79], v[40:43], v[92:95]\n\t" \
    "ds_read_u16_d16_hi v169, %[ua2] offset:896\n\t" \
    "ds_read_u16_d16_hi v170, %[ua2] offset:928\n\t" \
    "v_mfma_f32_16x16x32_bf16 v[104:107], v[88:91], v[136:139], v[104:107]\n\t" \
    "ds_read_b64 v[152:153], %[ub2] offset:960\n\t" \
    "ds_read_b64 v[154:155], %[ub2] offset:968\n\t" \
    "v_mfma_f32_16x16x32_bf16 v[92:95], v[32:35], v[136:139], v[92:95]\n\t" \
    "ds_read_b64 v[140:141], %[ub2] offset:992\n\t" \
    "ds_read_b64 v[142:143], %[ub2] offset:1000\n\t" \
    "s_waitcnt lgkmcnt(8)\n\t" \
    "v_mfma_f32_16x16x32_bf16 v[104:107], v[132:135], v[144:147], v[104:107]\n\t" \
    "v_cndmask_b32_e64 v166, v82, v74, %[m2]\n\t" \
    "v_mfma_f32_16x16x32_bf16 v[92:95], v[108:111], v[144:147], v[92:95]\n\t" \
    "v_cndmask_b32_e64 v161, v73, v69, %[m1]\n\t" \
    "v_cndmask_b32_e64 v165, v81, v73, %[m1]\n\t" \
    "v_mfma_f32_16x16x32_bf16 v[112:115], v[124:127], v[24:27], v[112:115]\n\t" \
    "v_add_f32_dpp v162, v163, v162 wave_ror:1 row_mask:0xf bank_mask:0xf\n\t" \
    "v_add_f32_dpp v166, v167, v166 wave_ror:1 row_mask:0xf bank_mask:0xf\n\t" \
    "L_cv3_15:\n\t" \
    "s_cbranch_execz L_cv3_16\n\t" \
    "v_mfma_f32_16x16x32_bf16 v[116:119], v[100:103], v[24:27], v[116:119]\n\t" \
    "v_fmac_f32 v72, %[dd], v174\n\t" \
    "v_fmac_f32 v80, %[dd], v175\n\t" \
    "v_mfma_f32_16x16x32_bf16 v[112:115], v[36:39], v[128:131], v[112:115]\n\t" \
    "v_add_f32_dpp v161, v162, v161 wave_ror:1 row_mask:0xf bank_mask:0xf\n\t" \
    "v_add_f32_dpp v165, v166, v165 wave_ror:1 row_mask:0xf bank_mask:0xf\n\t" \
    "v_mfma_f32_16x16x32_bf16 v[116:119], v[76:79], v[128:131], v[116:119]\n\t" \
    "v_add_f32_dpp v72, v161, v72 wave_ror:1 row_mask:0xf bank_mask:0xf\n\t" \
    "v_add_f32_dpp v80, v165, v80 wave_ror:1 row_mask:0xf bank_mask:0xf\n\t" \
    "v_mfma_f32_16x16x32_bf16 v[112:115], v[88:91], v[40:43], v[112:115]\n\t" \
    "v_cvt_pk_bf16_f32 v160, v72, v80\n\t" \
    "ds_write_b16 %[ya], v160 offset:768\n\t" \
    "v_mfma_f32_16x16x32_bf16 v[116:119], v[32:35], v[40:43], v[116:119]\n\t" \
    "ds_read_u16_d16_hi v171, %[ua2] offset:960\n\t" \
    "ds_read_u16_d16_hi v172, %[ua2] offset:992\n\t" \
    "v_mfma_f32_16x16x32_bf16 v[112:115], v[132:135], v[136:139], v[112:115]\n\t" \
    "ds_write_b16_d16_hi %[ya], v160 offset:896\n\t" \
    "ds_write_b32 %[fc], %[fc] offset:4\n\t" \
    "v_mfma_f32_16x16x32_bf16 v[116:119], v[108:111], v[136:139], v[116:119]\n\t" \
    "ds_read_b128 v[180:183], %[yr]\n\t" \
    "v_cndmask_b32_e64 v163, v55, v15, %[m3]\n\t" \
    "s_waitcnt lgkmcnt(6)\n\t" \
    "v_mfma_f32_16x16x32_bf16 v[112:115], v[152:155], v[144:147], v[112:115]\n\t" \
    "v_cndmask_b32_e64 v162, v54, v14, %[m2]\n\t" \
    "v_mfma_f32_16x16x32_bf16 v[116:119], v[140:143], v[144:147], v[116:119]\n\t" \
    "v_cndmask_b32_e64 v161, v53, v13, %[m1]\n\t" \
    "v_add_f32_dpp v162, v163, v162 wave_ror:1 row_mask:0xf bank_mask:0xf\n\t" \
    "v_fmac_f32 v52, %[dd2], v168\n\t" \
    "s_nop 0\n\t" \
    "v_add_f32_dpp v161, v162, v161 wave_ror:1 row_mask:0xf bank_mask:0xf\n\t" \
    "s_nop 1\n\t" \
    "v_add_f32_dpp v52, v161, v52 wave_ror:1 row_mask:0xf bank_mask:0xf\n\t" \
    "v_cvt_pk_bf16_f32 v160, v52, v52\n\t" \
    "L_cv3_16:\n\t" \
    "s_cbranch_execz L_cv3_17\n\t" \
    "ds_write_b16 %[ya], v160 offset:0\n\t" \
    "v_cndmask_b32_e64 v163, v107, v55, %[m3]\n\t" \
    "v_cndmask_b32_e64 v167, v95, v107, %[m3]\n\t" \
    "v_cndmask_b32_e64 v162, v106, v54, %[m2]\n\t" \
    "v_cndmask_b32_e64 v166, v94, v106, %[m2]\n\t" \
    "v_cndmask_b32_e64 v161, v105, v53, %[m1]\n\t" \
    "v_cndmask_b32_e64 v165, v93, v105, %[m1]\n\t" \
    "v_add_f32_dpp v162, v163, v162 wave_ror:1 row_mask:0xf bank_mask:0xf\n\t" \
    "v_add_f32_dpp v166, v167, v166 wave_ror:1 row_mask:0xf bank_mask:0xf\n\t" \
    "v_fmac_f32 v104, %[dd2], v169\n\t" \
    "v_fmac_f32 v92, %[dd2], v170\n\t" \
    "v_add_f32_dpp v161, v162, v161 wave_ror:1 row_mask:0xf bank_mask:0xf\n\t" \
    "v_add_f32_dpp v165, v166, v165 wave_ror:1 row_mask:0xf bank_mask:0xf\n\t" \
    "s_nop 0\n\t" \
    "v_add_f32_dpp v104, v161, v104 wave_ror:1 row_mask:0xf bank_mask:0xf\n\t" \
    "v_add_f32_dpp v92, v165, v92 wave_ror:1 row_mask:0xf bank_mask:0xf\n\t" \
    "v_cvt_pk_bf16_f32 v160, v104, v92\n\t" \
    "ds_write_b16 %[ya], v160 offset:128\n\t" \
    "ds_write_b16_d16_hi %[ya], v160 offset:256\n\t" \
    "v_cndmask_b32_e64 v163, v115, v95, %[m3]\n\t" \
    "v_cndmask_b32_e64 v167, v119, v115, %[m3]\n\t" \
    "v_cndmask_b32_e64 v162, v114, v94, %[m2]\n\t" \
    "v_cndmask_b32_e64 v166, v118, v114, %[m2]\n\t" \
    "v_cndmask_b32_e64 v161, v113, v93, %[m1]\n\t" \
    "v_cndmask_b32_e64 v165, v117, v113, %[m1]\n\t" \
    "v_add_f32_dpp v162, v163, v162 wave_ror:1 row_mask:0xf bank_mask:0xf\n\t" \
    "v_add_f32_dpp v166, v167, v166 wave_ror:1 row_mask:0xf bank_mask:0xf\n\t" \
    "s_waitcnt lgkmcnt(6)\n\t" \
    "v_fmac_f32 v112, %[dd2], v171\n\t" \
    "v_fmac_f32 v116, %[dd2], v172\n\t" \
    "v_add_f32_dpp v161, v162, v161 wave_ror:1 row_mask:0xf bank_mask:0xf\n\t" \
    "v_add_f32_dpp v165, v166, v165 wave_ror:1 row_mask:0xf bank_mask:0xf\n\t" \
    "s_nop 0\n\t" \
    "L_cv3_17:\n\t" \
    "s_cbranch_execz L_cv3_18\n\t" \
    "v_add_f32_dpp v112, v161, v112 wave_ror:1 row_mask:0xf bank_mask:0xf\n\t" \
    "v_add_f32_dpp v116, v165, v116 wave_ror:1 row_mask:0xf bank_mask:0xf\n\t" \
    "v_cvt_pk_bf16_f32 v160, v112, v116\n\t" \
    "ds_write_b16 %[ya], v160 offset:384\n\t" \
    "ds_write_b16_d16_hi %[ya], v160 offset:512\n\t" \
    "ds_read_b128 v[184:187], %[yr]\n\t" \
    "s_waitcnt vmcnt(0)\n\t" \
    "buffer_store_dwordx4 v[176:179], %[l16], %[rs], %[yo] offen offset:0 sc1\n\t" \
    "s_waitcnt lgkmcnt(6)\n\t" \
    "buffer_store_dwordx4 v[180:183], %[l16], %[rs], %[yo] offen offset:1024 sc1\n\t" \
    "s_waitcnt lgkmcnt(0)\n\t" \
    "s_mov_b32 exec_hi, 0xff\n\t" \
    "buffer_store_dwordx4 v[184:187], %[l16], %[rs], %[yo2] offen offset:3456 sc1\n\t" \
    "s_mov_b32 exec_hi, -1\n\t" \
    "s_nop 1\n\t" \
    "L_cv3_18:\n\t" \
    "s_cbranch_execz L_back\n\t" \
    "s_branch CVE_%=\n\t" \
    "L_cv4_0:\n\t" \
    "s_cbranch_execz L_cv4_1\n\t" \
    "ds_read_b64 v[0:1], %[ub] offset:32\n\t" \
    "ds_read_b64 v[2:3], %[ub] offset:40\n\t" \
    "ds_read_b64_tr_b16 v[4:5], %[hb] offset:1824\n\t" \
    "ds_read_b64_tr_b16 v[6:7], %[hb] offset:1792\n\t" \
    "ds_read_b64 v[8:9], %[ub] offset:0\n\t" \
    "ds_read_b64 v[10:11], %[ub] offset:8\n\t" \
    "ds_read_b64_tr_b16 v[12:13], %[hb] offset:2080\n\t" \
    "ds_read_b64_tr_b16 v[14:15], %[hb] offset:2048\n\t" \
    "ds_read_b64_tr_b16 v[16:17], %[hb] offset:2336\n\t" \
    "ds_read_b64_tr_b16 v[18:19], %[hb] offset:2304\n\t" \
    "ds_read_b64_tr_b16 v[20:21], %[hb] offset:2592\n\t" \
    "ds_read_b64_tr_b16 v[22:23], %[hb] offset:2560\n\t" \
    "v_mov_b32 v168, 0\n\t" \
    "v_mov_b32 v169, 0\n\t" \
    "v_mov_b32 v170, 0\n\t" \
    "v_mov_b32 v171, 0\n\t" \
    "v_mov_b32 v172, 0\n\t" \
    "v_mov_b32 v173, 0\n\t" \
    "v_mov_b32 v174, 0\n\t" \
    "v_mov_b32 v175, 0\n\t" \
    "s_waitcnt lgkmcnt(8)\n\t" \
    "v_mfma_f32_16x16x32_bf16 v[24:27], v[0:3], v[4:7], 0\n\t" \
    "ds_read_b64_tr_b16 v[28:29], %[hb] offset:2848\n\t" \
    "ds_read_b64_tr_b16 v[30:31], %[hb] offset:2816\n\t" \
    "ds_read_b64 v[32:33], %[ub] offset:96\n\t" \
    "s_waitcnt lgkmcnt(7)\n\t" \
    "v_mfma_f32_16x16x32_bf16 v[36:39], v[8:11], v[12:15], 0\n\t" \
    "ds_read_b64 v[34:35], %[ub] offset:104\n\t" \
    "ds_read_b64_tr_b16 v[40:41], %[hb] offset:1568\n\t" \
    "ds_read_b64_tr_b16 v[42:43], %[hb] offset:1536\n\t" \
    "v_mfma_f32_16x16x32_bf16 v[44:47], v[0:3], v[12:15], 0\n\t" \
    "ds_read_b64 v[48:49], %[ub] offset:64\n\t" \
    "L_cv4_1:\n\t" \
    "s_cbranch_execz L_cv4_2\n\t" \
    "ds_read_b64 v[50:51], %[ub] offset:72\n\t" \
    "s_waitcnt lgkmcnt(10)\n\t" \
    "v_mfma_f32_16x16x32_bf16 v[52:55], v[8:11], v[16:19], 0\n\t" \
    "v_mfma_f32_16x16x32_bf16 v[56:59], v[0:3], v[16:19], 0\n\t" \
    "ds_read_b64 v[60:61], %[ub] offset:160\n\t" \
    "ds_read_b64 v[62:63], %[ub] offset:168\n\t" \
    "s_waitcnt lgkmcnt(10)\n\t" \
    "v_mfma_f32_16x16x32_bf16 v[64:67], v[8:11], v[20:23], 0\n\t" \
    "ds_read_b64_tr_b16 v[68:69], %[hb] offset:1312\n\t" \
    "v_mfma_f32_16x16x32_bf16 v[72:75], v[0:3], v[20:23], 0\n\t" \
    "ds_read_b64_tr_b16 v[70:71], %[hb] offset:1280\n\t" \
    "ds_read_b64 v[76:77], %[ub] offset:128\n\t" \
    "s_waitcnt lgkmcnt(7)\n\t" \
    "v_mfma_f32_16x16x32_bf16 v[80:83], v[8:11], v[28:31], 0\n\t" \
    "ds_read_b64 v[78:79], %[ub] offset:136\n\t" \
    "v_mfma_f32_16x16x32_bf16 v[24:27], v[32:35], v[40:43], v[24:27]\n\t" \
    "s_waitcnt lgkmcnt(6)\n\t" \
    "v_mfma_f32_16x16x32_bf16 v[36:39], v[48:51], v[4:7], v[36:39]\n\t" \
    "v_mfma_f32_16x16x32_bf16 v[44:47], v[32:35], v[4:7], v[44:47]\n\t" \
    "v_mfma_f32_16x16x32_bf16 v[52:55], v[48:51], v[12:15], v[52:55]\n\t" \
    "v_mfma_f32_16x16x32_bf16 v[56:59], v[32:35], v[12:15], v[56:59]\n\t" \
    "ds_read_b64 v[84:85], %[ub] offset:192\n\t" \
    "ds_read_b64 v[86:87], %[ub] offset:200\n\t" \
    "v_mfma_f32_16x16x32_bf16 v[64:67], v[48:51], v[16:19], v[64:67]\n\t" \
    "ds_read_b64 v[88:89], %[ub] offset:224\n\t" \
    "ds_read_b64 v[90:91], %[ub] offset:232\n\t" \
    "v_mfma_f32_16x16x32_bf16 v[72:75], v[32:35], v[16:19], v[72:75]\n\t" \
    "L_cv4_2:\n\t" \
    "s_cbranch_execz L_cv4_3\n\t" \
    "v_mfma_f32_16x16x32_bf16 v[80:83], v[48:51], v[20:23], v[80:83]\n\t" \
    "s_waitcnt lgkmcnt(4)\n\t" \
    "v_mfma_f32_16x16x32_bf16 v[24:27], v[60:63], v[68:71], v[24:27]\n\t" \
    "v_mfma_f32_16x16x32_bf16 v[36:39], v[76:79], v[40:43], v[36:39]\n\t" \
    "v_mfma_f32_16x16x32_bf16 v[44:47], v[60:63], v[40:43], v[44:47]\n\t" \
    "v_mfma_f32_16x16x32_bf16 v[52:55], v[76:79], v[4:7], v[52:55]\n\t" \
    "ds_read_b64 v[92:93], %[ub] offset:256\n\t" \
    "ds_read_b64 v[94:95], %[ub] offset:264\n\t" \
    "v_mfma_f32_16x16x32_bf16 v[56:59], v[60:63], v[4:7], v[56:59]\n\t" \
    "ds_read_b64 v[96:97], %[ub] offset:288\n\t" \
    "ds_read_b64 v[98:99], %[ub] offset:296\n\t" \
    "v_mfma_f32_16x16x32_bf16 v[64:67], v[76:79], v[12:15], v[64:67]\n\t" \
    "v_mfma_f32_16x16x32_bf16 v[72:75], v[60:63], v[12:15], v[72:75]\n\t" \
    "v_mfma_f32_16x16x32_bf16 v[80:83], v[76:79], v[16:19], v[80:83]\n\t" \
    "s_waitcnt lgkmcnt(4)\n\t" \
    "v_mfma_f32_16x16x32_bf16 v[36:39], v[84:87], v[68:71], v[36:39]\n\t" \
    "ds_read_b64 v[100:101], %[ub] offset:320\n\t" \
    "v_mfma_f32_16x16x32_bf16 v[44:47], v[88:91], v[68:71], v[44:47]\n\t" \
    "ds_read_b64 v[102:103], %[ub] offset:328\n\t" \
    "ds_read_b64 v[104:105], %[ub] offset:352\n\t" \
    "v_mfma_f32_16x16x32_bf16 v[52:55], v[84:87], v[40:43], v[52:55]\n\t" \
    "ds_read_b64 v[106:107], %[ub] offset:360\n\t" \
    "v_mfma_f32_16x16x32_bf16 v[56:59], v[88:91], v[40:43], v[56:59]\n\t" \
    "ds_read_b64 v[108:109], %[ub] offset:384\n\t" \
    "ds_read_b64 v[110:111], %[ub] offset:392\n\t" \
    "v_mfma_f32_16x16x32_bf16 v[64:67], v[84:87], v[4:7], v[64:67]\n\t" \
    "ds_read_b64_tr_b16 v[112:113], %[hb] offset:1056\n\t" \
    "ds_read_b64_tr_b16 v[114:115], %[hb] offset:1024\n\t" \
    "v_mfma_f32_16x16x32_bf16 v[72:75], v[88:91], v[4:7], v[72:75]\n\t" \
    "ds_read_b64_tr_b16 v[116:117], %[hb] offset:800\n\t" \
    "ds_read_b64_tr_b16 v[118:119], %[hb] offset:768\n\t" \
    "v_mfma_f32_16x16x32_bf16 v[80:83], v[84:87], v[12:15], v[80:83]\n\t" \
    "L_cv4_3:\n\t" \
    "s_cbranch_execz L_cv4_4\n\t" \
    "ds_read_b64_tr_b16 v[120:121], %[hb] offset:544\n\t" \
    "ds_read_b64_tr_b16 v[122:123], %[hb] offset:512\n\t" \
    "s_waitcnt lgkmcnt(12)\n\t" \
    "v_mfma_f32_16x16x32_bf16 v[52:55], v[92:95], v[68:71], v[52:55]\n\t" \
    "ds_read_b64 v[124:125], %[ub] offset:416\n\t" \
    "v_mfma_f32_16x16x32_bf16 v[56:59], v[96:99], v[68:71], v[56:59]\n\t" \
    "ds_read_b64 v[126:127], %[ub] offset:424\n\t" \
    "ds_read_b64_tr_b16 v[128:129], %[hb] offset:288\n\t" \
    "v_mfma_f32_16x16x32_bf16 v[64:67], v[92:95], v[40:43], v[64:67]\n\t" \
    "ds_read_b64_tr_b16 v[130:131], %[hb] offset:256\n\t" \
    "ds_read_b64 v[132:133], %[ub] offset:480\n\t" \
    "v_mfma_f32_16x16x32_bf16 v[72:75], v[96:99], v[40:43], v[72:75]\n\t" \
    "ds_read_b64 v[134:135], %[ub] offset:488\n\t" \
    "ds_read_b64_tr_b16 v[136:137], %[hb] offset:32\n\t" \
    "v_mfma_f32_16x16x32_bf16 v[80:83], v[92:95], v[4:7], v[80:83]\n\t" \
    "ds_read_b64_tr_b16 v[138:139], %[hb] offset:0\n\t" \
    "s_waitcnt lgkmcnt(14)\n\t" \
    "v_mfma_f32_16x16x32_bf16 v[64:67], v[100:103], v[68:71], v[64:67]\n\t" \
    "v_mfma_f32_16x16x32_bf16 v[72:75], v[104:107], v[68:71], v[72:75]\n\t" \
    "v_mfma_f32_16x16x32_bf16 v[80:83], v[100:103], v[40:43], v[80:83]\n\t" \
    "v_mfma_f32_16x16x32_bf16 v[80:83], v[108:111], v[68:71], v[80:83]\n\t" \
    "ds_read_b64 v[140:141], %[ub] offset:448\n\t" \
    "ds_read_b64 v[142:143], %[ub] offset:456\n\t" \
    "s_waitcnt lgkmcnt(12)\n\t" \
    "v_mfma_f32_16x16x32_bf16 v[24:27], v[88:91], v[112:115], v[24:27]\n\t" \
    "v_mfma_f32_16x16x32_bf16 v[24:27], v[96:99], v[116:119], v[24:27]\n\t" \
    "ds_read_b64 v[144:145], %[ub] offset:512\n\t" \
    "ds_read_b64 v[146:147], %[ub] offset:520\n\t" \
    "s_waitcnt lgkmcnt(8)\n\t" \
    "v_mfma_f32_16x16x32_bf16 v[24:27], v[104:107], v[120:123], v[24:27]\n\t" \
    "ds_read_b64 v[148:149], %[ub] offset:544\n\t" \
    "v_mfma_f32_16x16x32_bf16 v[24:27], v[124:127], v[128:131], v[24:27]\n\t" \
    "ds_read_b64 v[150:151], %[ub] offset:552\n\t" \
    "s_waitcnt lgkmcnt(6)\n\t" \
    "L_cv4_4:\n\t" \
    "s_cbranch_execz L_cv4_5\n\t" \
    "v_mfma_f32_16x16x32_bf16 v[24:27], v[132:135], v[136:139], v[24:27]\n\t" \
    "v_mfma_f32_16x16x32_bf16 v[36:39], v[92:95], v[112:115], v[36:39]\n\t" \
    "v_mfma_f32_16x16x32_bf16 v[44:47], v[96:99], v[112:115], v[44:47]\n\t" \
    "v_mfma_f32_16x16x32_bf16 v[36:39], v[100:103], v[116:119], v[36:39]\n\t" \
    "v_mfma_f32_16x16x32_bf16 v[44:47], v[104:107], v[116:119], v[44:47]\n\t" \
    "v_mfma_f32_16x16x32_bf16 v[36:39], v[108:111], v[120:123], v[36:39]\n\t" \
    "v_mfma_f32_16x16x32_bf16 v[44:47], v[124:127], v[120:123], v[44:47]\n\t" \
    "ds_read_u16_d16_hi v168, %[ua] offset:512\n\t" \
    "ds_read_u16_d16_hi v169, %[ua] offset:544\n\t" \
    "s_waitcnt lgkmcnt(6)\n\t" \
    "v_mfma_f32_16x16x32_bf16 v[36:39], v[140:143], v[128:131], v[36:39]\n\t" \
    "ds_read_b64 v[152:153], %[ub] offset:576\n\t" \
    "v_mfma_f32_16x16x32_bf16 v[44:47], v[132:135], v[128:131], v[44:47]\n\t" \
    "ds_read_b64 v[154:155], %[ub] offset:584\n\t" \
    "ds_read_b64 v[156:157], %[ub] offset:608\n\t" \
    "s_waitcnt lgkmcnt(5)\n\t" \
    "v_mfma_f32_16x16x32_bf16 v[36:39], v[144:147], v[136:139], v[36:39]\n\t" \
    "ds_read_b64 v[158:159], %[ub] offset:616\n\t" \
    "v_mfma_f32_16x16x32_bf16 v[44:47], v[148:151], v[136:139], v[44:47]\n\t" \
    "v_mfma_f32_16x16x32_bf16 v[52:55], v[100:103], v[112:115], v[52:55]\n\t" \
    "v_mfma_f32_16x16x32_bf16 v[56:59], v[104:107], v[112:115], v[56:59]\n\t" \
    "v_mfma_f32_16x16x32_bf16 v[52:55], v[108:111], v[116:119], v[52:55]\n\t" \
    "v_mfma_f32_16x16x32_bf16 v[56:59], v[124:127], v[116:119], v[56:59]\n\t" \
    "s_nop 1\n\t" \
    "v_cndmask_b32_e64 v163, v39, v27, %[m3]\n\t" \
    "s_nop 0\n\t" \
    "v_cndmask_b32_e64 v167, v47, v39, %[m3]\n\t" \
    "v_mfma_f32_16x16x32_bf16 v[52:55], v[140:143], v[120:123], v[52:55]\n\t" \
    "v_cndmask_b32_e64 v162, v38, v26, %[m2]\n\t" \
    "v_cndmask_b32_e64 v166, v46, v38, %[m2]\n\t" \
    "v_mfma_f32_16x16x32_bf16 v[56:59], v[132:135], v[120:123], v[56:59]\n\t" \
    "ds_read_u16_d16_hi v170, %[ua] offset:576\n\t" \
    "ds_read_u16_d16_hi v171, %[ua] offset:608\n\t" \
    "L_cv4_5:\n\t" \
    "s_cbranch_execz L_cv4_6\n\t" \
    "v_mfma_f32_16x16x32_bf16 v[52:55], v[144:147], v[128:131], v[52:55]\n\t" \
    "ds_read_b64 v[0:1], %[ub] offset:640\n\t" \
    "ds_read_b64 v[2:3], %[ub] offset:648\n\t" \
    "v_mfma_f32_16x16x32_bf16 v[56:59], v[148:151], v[128:131], v[56:59]\n\t" \
    "ds_read_b64 v[8:9], %[ub] offset:672\n\t" \
    "ds_read_b64 v[10:11], %[ub] offset:680\n\t" \
    "s_waitcnt lgkmcnt(6)\n\t" \
    "v_mfma_f32_16x16x32_bf16 v[52:55], v[152:155], v[136:139], v[52:55]\n\t" \
    "v_cndmask_b32_e64 v161, v37, v25, %[m1]\n\t" \
    "v_mfma_f32_16x16x32_bf16 v[56:59], v[156:159], v[136:139], v[56:59]\n\t" \
    "v_cndmask_b32_e64 v165, v45, v37, %[m1]\n\t" \
    "v_add_f32_dpp v162, v163, v162 wave_ror:1 row_mask:0xf bank_mask:0xf\n\t" \
    "v_mfma_f32_16x16x32_bf16 v[64:67], v[108:111], v[112:115], v[64:67]\n\t" \
    "v_add_f32_dpp v166, v167, v166 wave_ror:1 row_mask:0xf bank_mask:0xf\n\t" \
    "v_fmac_f32 v36, %[dd], v168\n\t" \
    "v_mfma_f32_16x16x32_bf16 v[72:75], v[124:127], v[112:115], v[72:75]\n\t" \
    "v_fmac_f32 v44, %[dd], v169\n\t" \
    "v_add_f32_dpp v161, v162, v161 wave_ror:1 row_mask:0xf bank_mask:0xf\n\t" \
    "v_mfma_f32_16x16x32_bf16 v[64:67], v[140:143], v[116:119], v[64:67]\n\t" \
    "ds_read_b64 v[28:29], %[ub] offset:704\n\t" \
    "ds_read_b64 v[30:31], %[ub] offset:712\n\t" \
    "v_mfma_f32_16x16x32_bf16 v[72:75], v[132:135], v[116:119], v[72:75]\n\t" \
    "ds_read_b64 v[32:33], %[ub2] offset:0\n\t" \
    "ds_read_b64 v[34:35], %[ub2] offset:8\n\t" \
    "v_mfma_f32_16x16x32_bf16 v[64:67], v[144:147], v[120:123], v[64:67]\n\t" \
    "ds_read_b64_tr_b16 v[48:49], %[hb2] offset:3360\n\t" \
    "ds_read_b64_tr_b16 v[50:51], %[hb2] offset:3328\n\t" \
    "v_mfma_f32_16x16x32_bf16 v[72:75], v[148:151], v[120:123], v[72:75]\n\t" \
    "ds_read_b64 v[20:21], %[ub2] offset:32\n\t" \
    "ds_read_b64 v[22:23], %[ub2] offset:40\n\t" \
    "v_mfma_f32_16x16x32_bf16 v[64:67], v[152:155], v[128:131], v[64:67]\n\t" \
    "ds_read_b64_tr_b16 v[60:61], %[hb2] offset:3616\n\t" \
    "L_cv4_6:\n\t" \
    "s_cbranch_execz L_cv4_7\n\t" \
    "ds_read_b64_tr_b16 v[62:63], %[hb2] offset:3584\n\t" \
    "v_mfma_f32_16x16x32_bf16 v[72:75], v[156:159], v[128:131], v[72:75]\n\t" \
    "ds_read_b64_tr_b16 v[76:77], %[hb2] offset:3872\n\t" \
    "ds_read_b64_tr_b16 v[78:79], %[hb2] offset:3840\n\t" \
    "s_waitcnt lgkmcnt(12)\n\t" \
    "v_mfma_f32_16x16x32_bf16 v[64:67], v[0:3], v[136:139], v[64:67]\n\t" \
    "ds_read_u16_d16_hi v172, %[ua] offset:640\n\t" \
    "v_mfma_f32_16x16x32_bf16 v[72:75], v[8:11], v[136:139], v[72:75]\n\t" \
    "ds_read_b64 v[16:17], %[ub2] offset:64\n\t" \
    "ds_read_b64 v[18:19], %[ub2] offset:72\n\t" \
    "ds_read_u16_d16_hi v173, %[ua] offset:672\n\t" \
    "v_mfma_f32_16x16x32_bf16 v[80:83], v[140:143], v[112:115], v[80:83]\n\t" \
    "ds_read_b64_tr_b16 v[84:85], %[hb2] offset:3104\n\t" \
    "ds_read_b64_tr_b16 v[86:87], %[hb2] offset:3072\n\t" \
    "v_mfma_f32_16x16x32_bf16 v[80:83], v[144:147], v[116:119], v[80:83]\n\t" \
    "ds_read_b64 v[12:13], %[ub2] offset:96\n\t" \
    "ds_read_b64 v[14:15], %[ub2] offset:104\n\t" \
    "v_mfma_f32_16x16x32_bf16 v[80:83], v[152:155], v[120:123], v[80:83]\n\t" \
    "ds_read_u16_d16_hi v174, %[ua] offset:704\n\t" \
    "v_add_f32_dpp v165, v166, v165 wave_ror:1 row_mask:0xf bank_mask:0xf\n\t" \
    "v_mfma_f32_16x16x32_bf16 v[80:83], v[0:3], v[128:131], v[80:83]\n\t" \
    "v_add_f32_dpp v36, v161, v36 wave_ror:1 row_mask:0xf bank_mask:0xf\n\t" \
    "v_add_f32_dpp v44, v165, v44 wave_ror:1 row_mask:0xf bank_mask:0xf\n\t" \
    "s_waitcnt lgkmcnt(14)\n\t" \
    "v_mfma_f32_16x16x32_bf16 v[80:83], v[28:31], v[136:139], v[80:83]\n\t" \
    "v_cvt_pk_bf16_f32 v160, v36, v44\n\t" \
    "v_mfma_f32_16x16x32_bf16 v[4:7], v[32:35], v[48:51], 0\n\t" \
    "ds_read_b64 v[40:41], %[ub2] offset:128\n\t" \
    "ds_read_b64 v[42:43], %[ub2] offset:136\n\t" \
    "s_waitcnt lgkmcnt(13)\n\t" \
    "v_mfma_f32_16x16x32_bf16 v[68:71], v[20:23], v[48:51], 0\n\t" \
    "ds_read_b64_tr_b16 v[88:89], %[hb2] offset:2848\n\t" \
    "v_mfma_f32_16x16x32_bf16 v[92:95], v[32:35], v[60:63], 0\n\t" \
    "L_cv4_7:\n\t" \
    "s_cbranch_execz L_cv4_8\n\t" \
    "ds_read_b64_tr_b16 v[90:91], %[hb2] offset:2816\n\t" \
    "ds_read_b64 v[96:97], %[ub2] offset:160\n\t" \
    "v_mfma_f32_16x16x32_bf16 v[100:103], v[20:23], v[60:63], 0\n\t" \
    "ds_read_b64 v[98:99], %[ub2] offset:168\n\t" \
    "ds_write_b16 %[ya], v160 offset:0\n\t" \
    "s_waitcnt lgkmcnt(14)\n\t" \
    "v_mfma_f32_16x16x32_bf16 v[104:107], v[32:35], v[76:79], 0\n\t" \
    "ds_write_b16_d16_hi %[ya], v160 offset:128\n\t" \
    "v_mfma_f32_16x16x32_bf16 v[108:111], v[20:23], v[76:79], 0\n\t" \
    "v_cndmask_b32_e64 v163, v55, v47, %[m3]\n\t" \
    "v_cndmask_b32_e64 v167, v59, v55, %[m3]\n\t" \
    "s_waitcnt lgkmcnt(9)\n\t" \
    "v_mfma_f32_16x16x32_bf16 v[4:7], v[16:19], v[84:87], v[4:7]\n\t" \
    "ds_read_b64 v[124:125], %[ub2] offset:192\n\t" \
    "v_mfma_f32_16x16x32_bf16 v[68:71], v[12:15], v[84:87], v[68:71]\n\t" \
    "ds_read_b64 v[126:127], %[ub2] offset:200\n\t" \
    "ds_read_b64_tr_b16 v[132:133], %[hb2] offset:2592\n\t" \
    "v_mfma_f32_16x16x32_bf16 v[92:95], v[16:19], v[48:51], v[92:95]\n\t" \
    "ds_read_b64_tr_b16 v[134:135], %[hb2] offset:2560\n\t" \
    "ds_read_b64 v[148:149], %[ub2] offset:224\n\t" \
    "v_mfma_f32_16x16x32_bf16 v[100:103], v[12:15], v[48:51], v[100:103]\n\t" \
    "ds_read_b64 v[150:151], %[ub2] offset:232\n\t" \
    "v_cndmask_b32_e64 v162, v54, v46, %[m2]\n\t" \
    "v_mfma_f32_16x16x32_bf16 v[104:107], v[16:19], v[60:63], v[104:107]\n\t" \
    "v_cndmask_b32_e64 v166, v58, v54, %[m2]\n\t" \
    "v_cndmask_b32_e64 v161, v53, v45, %[m1]\n\t" \
    "v_mfma_f32_16x16x32_bf16 v[108:111], v[12:15], v[60:63], v[108:111]\n\t" \
    "v_cndmask_b32_e64 v165, v57, v53, %[m1]\n\t" \
    "v_add_f32_dpp v162, v163, v162 wave_ror:1 row_mask:0xf bank_mask:0xf\n\t" \
    "s_waitcnt lgkmcnt(8)\n\t" \
    "v_mfma_f32_16x16x32_bf16 v[4:7], v[40:43], v[88:91], v[4:7]\n\t" \
    "ds_read_b64 v[156:157], %[ub2] offset:256\n\t" \
    "v_mfma_f32_16x16x32_bf16 v[68:71], v[96:99], v[88:91], v[68:71]\n\t" \
    "L_cv4_8:\n\t" \
    "s_cbranch_execz L_cv4_9\n\t" \
    "ds_read_b64 v[158:159], %[ub2] offset:264\n\t" \
    "ds_read_b64_tr_b16 v[8:9], %[hb2] offset:2336\n\t" \
    "v_mfma_f32_16x16x32_bf16 v[92:95], v[40:43], v[84:87], v[92:95]\n\t" \
    "ds_read_b64_tr_b16 v[10:11], %[hb2] offset:2304\n\t" \
    "ds_read_b64 v[140:141], %[ub2] offset:288\n\t" \
    "v_mfma_f32_16x16x32_bf16 v[100:103], v[96:99], v[84:87], v[100:103]\n\t" \
    "ds_read_b64 v[142:143], %[ub2] offset:296\n\t" \
    "v_add_f32_dpp v166, v167, v166 wave_ror:1 row_mask:0xf bank_mask:0xf\n\t" \
    "v_mfma_f32_16x16x32_bf16 v[104:107], v[40:43], v[48:51], v[104:107]\n\t" \
    "v_fmac_f32 v52, %[dd], v170\n\t" \
    "v_fmac_f32 v56, %[dd], v171\n\t" \
    "v_mfma_f32_16x16x32_bf16 v[108:111], v[96:99], v[48:51], v[108:111]\n\t" \
    "v_add_f32_dpp v161, v162, v161 wave_ror:1 row_mask:0xf bank_mask:0xf\n\t" \
    "v_add_f32_dpp v165, v166, v165 wave_ror:1 row_mask:0xf bank_mask:0xf\n\t" \
    "s_waitcnt lgkmcnt(6)\n\t" \
    "v_mfma_f32_16x16x32_bf16 v[4:7], v[124:127], v[132:135], v[4:7]\n\t" \
    "ds_read_b64 v[112:113], %[ub2] offset:320\n\t" \
    "v_mfma_f32_16x16x32_bf16 v[68:71], v[148:151], v[132:135], v[68:71]\n\t" \
    "ds_read_b64 v[114:115], %[ub2] offset:328\n\t" \
    "ds_read_b64_tr_b16 v[144:145], %[hb2] offset:2080\n\t" \
    "v_mfma_f32_16x16x32_bf16 v[92:95], v[124:127], v[88:91], v[92:95]\n\t" \
    "ds_read_b64_tr_b16 v[146:147], %[hb2] offset:2048\n\t" \
    "ds_read_b64 v[116:117], %[ub2] offset:352\n\t" \
    "v_mfma_f32_16x16x32_bf16 v[100:103], v[148:151], v[88:91], v[100:103]\n\t" \
    "ds_read_b64 v[118:119], %[ub2] offset:360\n\t" \
    "v_add_f32_dpp v52, v161, v52 wave_ror:1 row_mask:0xf bank_mask:0xf\n\t" \
    "v_mfma_f32_16x16x32_bf16 v[104:107], v[124:127], v[84:87], v[104:107]\n\t" \
    "v_add_f32_dpp v56, v165, v56 wave_ror:1 row_mask:0xf bank_mask:0xf\n\t" \
    "v_cvt_pk_bf16_f32 v160, v52, v56\n\t" \
    "v_mfma_f32_16x16x32_bf16 v[108:111], v[148:151], v[84:87], v[108:111]\n\t" \
    "ds_write_b16 %[ya], v160 offset:256\n\t" \
    "ds_write_b16_d16_hi %[ya], v160 offset:384\n\t" \
    "L_cv4_9:\n\t" \
    "s_cbranch_execz L_cv4_10\n\t" \
    "s_waitcnt lgkmcnt(8)\n\t" \
    "v_mfma_f32_16x16x32_bf16 v[4:7], v[156:159], v[8:11], v[4:7]\n\t" \
    "ds_read_b64 v[152:153], %[ub2] offset:384\n\t" \
    "v_mfma_f32_16x16x32_bf16 v[68:71], v[140:143], v[8:11], v[68:71]\n\t" \
    "ds_read_b64 v[154:155], %[ub2] offset:392\n\t" \
    "ds_read_b64_tr_b16 v[120:121], %[hb2] offset:1824\n\t" \
    "v_mfma_f32_16x16x32_bf16 v[92:95], v[156:159], v[132:135], v[92:95]\n\t" \
    "ds_read_b64_tr_b16 v[122:123], %[hb2] offset:1792\n\t" \
    "ds_read_b64 v[0:1], %[ub2] offset:416\n\t" \
    "v_mfma_f32_16x16x32_bf16 v[100:103], v[140:143], v[132:135], v[100:103]\n\t" \
    "ds_read_b64 v[2:3], %[ub2] offset:424\n\t" \
    "v_cndmask_b32_e64 v163, v67, v59, %[m3]\n\t" \
    "v_mfma_f32_16x16x32_bf16 v[104:107], v[156:159], v[88:91], v[104:107]\n\t" \
    "v_cndmask_b32_e64 v167, v75, v67, %[m3]\n\t" \
    "v_cndmask_b32_e64 v162, v66, v58, %[m2]\n\t" \
    "v_mfma_f32_16x16x32_bf16 v[108:111], v[140:143], v[88:91], v[108:111]\n\t" \
    "v_cndmask_b32_e64 v166, v74, v66, %[m2]\n\t" \
    "v_cndmask_b32_e64 v161, v65, v57, %[m1]\n\t" \
    "s_waitcnt lgkmcnt(8)\n\t" \
    "v_mfma_f32_16x16x32_bf16 v[4:7], v[112:115], v[144:147], v[4:7]\n\t" \
    "ds_read_b64 v[128:129], %[ub2] offset:448\n\t" \
    "v_mfma_f32_16x16x32_bf16 v[68:71], v[116:119], v[144:147], v[68:71]\n\t" \
    "ds_read_b64 v[130:131], %[ub2] offset:456\n\t" \
    "ds_read_b64_tr_b16 v[24:25], %[hb2] offset:1568\n\t" \
    "v_mfma_f32_16x16x32_bf16 v[92:95], v[112:115], v[8:11], v[92:95]\n\t" \
    "ds_read_b64_tr_b16 v[26:27], %[hb2] offset:1536\n\t" \
    "ds_read_b64 v[36:37], %[ub2] offset:480\n\t" \
    "v_mfma_f32_16x16x32_bf16 v[100:103], v[116:119], v[8:11], v[100:103]\n\t" \
    "ds_read_b64 v[38:39], %[ub2] offset:488\n\t" \
    "v_cndmask_b32_e64 v165, v73, v65, %[m1]\n\t" \
    "v_mfma_f32_16x16x32_bf16 v[104:107], v[112:115], v[132:135], v[104:107]\n\t" \
    "v_add_f32_dpp v162, v163, v162 wave_ror:1 row_mask:0xf bank_mask:0xf\n\t" \
    "L_cv4_10:\n\t" \
    "s_cbranch_execz L_cv4_11\n\t" \
    "v_add_f32_dpp v166, v167, v166 wave_ror:1 row_mask:0xf bank_mask:0xf\n\t" \
    "v_mfma_f32_16x16x32_bf16 v[108:111], v[116:119], v[132:135], v[108:111]\n\t" \
    "v_fmac_f32 v64, %[dd], v172\n\t" \
    "v_fmac_f32 v72, %[dd], v173\n\t" \
    "s_waitcnt lgkmcnt(6)\n\t" \
    "v_mfma_f32_16x16x32_bf16 v[4:7], v[152:155], v[120:123], v[4:7]\n\t" \
    "ds_read_b64 v[28:29], %[ub2] offset:512\n\t" \
    "v_mfma_f32_16x16x32_bf16 v[68:71], v[0:3], v[120:123], v[68:71]\n\t" \
    "ds_read_b64 v[30:31], %[ub2] offset:520\n\t" \
    "ds_read_b64_tr_b16 v[136:137], %[hb2] offset:1312\n\t" \
    "v_mfma_f32_16x16x32_bf16 v[92:95], v[152:155], v[144:147], v[92:95]\n\t" \
    "ds_read_b64_tr_b16 v[138:139], %[hb2] offset:1280\n\t" \
    "ds_read_b64 v[32:33], %[ub2] offset:544\n\t" \
    "v_mfma_f32_16x16x32_bf16 v[100:103], v[0:3], v[144:147], v[100:103]\n\t" \
    "ds_read_b64 v[34:35], %[ub2] offset:552\n\t" \
    "v_add_f32_dpp v161, v162, v161 wave_ror:1 row_mask:0xf bank_mask:0xf\n\t" \
    "v_mfma_f32_16x16x32_bf16 v[104:107], v[152:155], v[8:11], v[104:107]\n\t" \
    "v_add_f32_dpp v165, v166, v165 wave_ror:1 row_mask:0xf bank_mask:0xf\n\t" \
    "v_add_f32_dpp v64, v161, v64 wave_ror:1 row_mask:0xf bank_mask:0xf\n\t" \
    "v_mfma_f32_16x16x32_bf16 v[108:111], v[0:3], v[8:11], v[108:111]\n\t" \
    "v_add_f32_dpp v72, v165, v72 wave_ror:1 row_mask:0xf bank_mask:0xf\n\t" \
    "v_cvt_pk_bf16_f32 v160, v64, v72\n\t" \
    "s_waitcnt lgkmcnt(6)\n\t" \
    "v_mfma_f32_16x16x32_bf16 v[4:7], v[128:131], v[24:27], v[4:7]\n\t" \
    "ds_read_b64 v[20:21], %[ub2] offset:576\n\t" \
    "v_mfma_f32_16x16x32_bf16 v[68:71], v[36:39], v[24:27], v[68:71]\n\t" \
    "ds_read_b64 v[22:23], %[ub2] offset:584\n\t" \
    "ds_read_b64 v[76:77], %[ub2] offset:608\n\t" \
    "v_mfma_f32_16x16x32_bf16 v[92:95], v[128:131], v[120:123], v[92:95]\n\t" \
    "ds_read_b64 v[78:79], %[ub2] offset:616\n\t" \
    "ds_write_b16 %[ya], v160 offset:512\n\t" \
    "v_mfma_f32_16x16x32_bf16 v[100:103], v[36:39], v[120:123], v[100:103]\n\t" \
    "L_cv4_11:\n\t" \
    "s_cbranch_execz L_cv4_12\n\t" \
    "ds_write_b16_d16_hi %[ya], v160 offset:640\n\t" \
    "v_cndmask_b32_e64 v163, v83, v75, %[m3]\n\t" \
    "v_mfma_f32_16x16x32_bf16 v[104:107], v[128:131], v[144:147], v[104:107]\n\t" \
    "ds_read_b64 v[16:17], %[ub2] offset:640\n\t" \
    "ds_read_b64 v[18:19], %[ub2] offset:648\n\t" \
    "v_mfma_f32_16x16x32_bf16 v[108:111], v[36:39], v[144:147], v[108:111]\n\t" \
    "ds_read_b64 v[12:13], %[ub2] offset:672\n\t" \
    "ds_read_b64 v[14:15], %[ub2] offset:680\n\t" \
    "s_waitcnt lgkmcnt(10)\n\t" \
    "v_mfma_f32_16x16x32_bf16 v[4:7], v[28:31], v[136:139], v[4:7]\n\t" \
    "ds_read_b64_tr_b16 v[60:61], %[hb2] offset:1056\n\t" \
    "v_mfma_f32_16x16x32_bf16 v[68:71], v[32:35], v[136:139], v[68:71]\n\t" \
    "ds_read_b64_tr_b16 v[62:63], %[hb2] offset:1024\n\t" \
    "v_cndmask_b32_e64 v162, v82, v74, %[m2]\n\t" \
    "v_mfma_f32_16x16x32_bf16 v[92:95], v[28:31], v[24:27], v[92:95]\n\t" \
    "ds_read_b64_tr_b16 v[40:41], %[hb2] offset:800\n\t" \
    "ds_read_b64_tr_b16 v[42:43], %[hb2] offset:768\n\t" \
    "v_mfma_f32_16x16x32_bf16 v[100:103], v[32:35], v[24:27], v[100:103]\n\t" \
    "v_cndmask_b32_e64 v161, v81, v73, %[m1]\n\t" \
    "v_add_f32_dpp v162, v163, v162 wave_ror:1 row_mask:0xf bank_mask:0xf\n\t" \
    "v_mfma_f32_16x16x32_bf16 v[104:107], v[28:31], v[120:123], v[104:107]\n\t" \
    "ds_read_b64 v[96:97], %[ub2] offset:704\n\t" \
    "ds_read_b64 v[98:99], %[ub2] offset:712\n\t" \
    "v_mfma_f32_16x16x32_bf16 v[108:111], v[32:35], v[120:123], v[108:111]\n\t" \
    "ds_read_b64_tr_b16 v[48:49], %[hb2] offset:544\n\t" \
    "ds_read_b64_tr_b16 v[50:51], %[hb2] offset:512\n\t" \
    "s_waitcnt lgkmcnt(14)\n\t" \
    "v_mfma_f32_16x16x32_bf16 v[92:95], v[20:23], v[136:139], v[92:95]\n\t" \
    "ds_read_b64 v[44:45], %[ub2] offset:736\n\t" \
    "v_mfma_f32_16x16x32_bf16 v[100:103], v[76:79], v[136:139], v[100:103]\n\t" \
    "ds_read_b64 v[46:47], %[ub2] offset:744\n\t" \
    "ds_read_b64 v[52:53], %[ub2] offset:768\n\t" \
    "L_cv4_12:\n\t" \
    "s_cbranch_execz L_cv4_13\n\t" \
    "v_mfma_f32_16x16x32_bf16 v[104:107], v[20:23], v[24:27], v[104:107]\n\t" \
    "ds_read_b64 v[54:55], %[ub2] offset:776\n\t" \
    "ds_read_b64_tr_b16 v[124:125], %[hb2] offset:288\n\t" \
    "v_mfma_f32_16x16x32_bf16 v[108:111], v[76:79], v[24:27], v[108:111]\n\t" \
    "ds_read_b64_tr_b16 v[126:127], %[hb2] offset:256\n\t" \
    "ds_read_b64 v[148:149], %[ub2] offset:800\n\t" \
    "s_waitcnt lgkmcnt(14)\n\t" \
    "v_mfma_f32_16x16x32_bf16 v[104:107], v[16:19], v[136:139], v[104:107]\n\t" \
    "ds_read_b64 v[150:151], %[ub2] offset:808\n\t" \
    "v_mfma_f32_16x16x32_bf16 v[108:111], v[12:15], v[136:139], v[108:111]\n\t" \
    "ds_read_b64 v[84:85], %[ub2] offset:832\n\t" \
    "ds_read_b64 v[86:87], %[ub2] offset:840\n\t" \
    "s_waitcnt lgkmcnt(14)\n\t" \
    "v_mfma_f32_16x16x32_bf16 v[4:7], v[20:23], v[60:63], v[4:7]\n\t" \
    "ds_read_b64_tr_b16 v[156:157], %[hb2] offset:32\n\t" \
    "ds_read_b64_tr_b16 v[158:159], %[hb2] offset:0\n\t" \
    "ds_read_b64 v[140:141], %[ub2] offset:864\n\t" \
    "v_mfma_f32_16x16x32_bf16 v[68:71], v[76:79], v[60:63], v[68:71]\n\t" \
    "ds_read_b64 v[142:143], %[ub2] offset:872\n\t" \
    "v_fmac_f32 v80, %[dd], v174\n\t" \
    "v_mfma_f32_16x16x32_bf16 v[4:7], v[16:19], v[40:43], v[4:7]\n\t" \
    "v_add_f32_dpp v161, v162, v161 wave_ror:1 row_mask:0xf bank_mask:0xf\n\t" \
    "s_nop 1\n\t" \
    "v_add_f32_dpp v80, v161, v80 wave_ror:1 row_mask:0xf bank_mask:0xf\n\t" \
    "v_mfma_f32_16x16x32_bf16 v[68:71], v[12:15], v[40:43], v[68:71]\n\t" \
    "v_cvt_pk_bf16_f32 v160, v80, v80\n\t" \
    "ds_write_b16 %[ya], v160 offset:768\n\t" \
    "s_waitcnt lgkmcnt(13)\n\t" \
    "v_mfma_f32_16x16x32_bf16 v[4:7], v[96:99], v[48:51], v[4:7]\n\t" \
    "ds_write_b32 %[fc], %[fc] offset:4\n\t" \
    "v_mfma_f32_16x16x32_bf16 v[68:71], v[44:47], v[48:51], v[68:71]\n\t" \
    "ds_read_u16_d16_hi v175, %[ua2] offset:864\n\t" \
    "ds_read_b128 v[176:179], %[yr]\n\t" \
    "s_waitcnt lgkmcnt(10)\n\t" \
    "L_cv4_13:\n\t" \
    "s_cbranch_execz L_cv4_14\n\t" \
    "v_mfma_f32_16x16x32_bf16 v[4:7], v[52:55], v[124:127], v[4:7]\n\t" \
    "ds_read_b64 v[88:89], %[ub2] offset:896\n\t" \
    "v_mfma_f32_16x16x32_bf16 v[68:71], v[148:151], v[124:127], v[68:71]\n\t" \
    "ds_read_b64 v[90:91], %[ub2] offset:904\n\t" \
    "ds_read_b64 v[112:113], %[ub2] offset:928\n\t" \
    "s_waitcnt lgkmcnt(7)\n\t" \
    "v_mfma_f32_16x16x32_bf16 v[4:7], v[84:87], v[156:159], v[4:7]\n\t" \
    "ds_read_b64 v[114:115], %[ub2] offset:936\n\t" \
    "v_mfma_f32_16x16x32_bf16 v[68:71], v[140:143], v[156:159], v[68:71]\n\t" \
    "v_mfma_f32_16x16x32_bf16 v[92:95], v[16:19], v[60:63], v[92:95]\n\t" \
    "v_mfma_f32_16x16x32_bf16 v[100:103], v[12:15], v[60:63], v[100:103]\n\t" \
    "v_mfma_f32_16x16x32_bf16 v[92:95], v[96:99], v[40:43], v[92:95]\n\t" \
    "v_mfma_f32_16x16x32_bf16 v[100:103], v[44:47], v[40:43], v[100:103]\n\t" \
    "s_nop 3\n\t" \
    "v_cndmask_b32_e64 v163, v71, v7, %[m3]\n\t" \
    "v_cndmask_b32_e64 v162, v70, v6, %[m2]\n\t" \
    "v_mfma_f32_16x16x32_bf16 v[92:95], v[52:55], v[48:51], v[92:95]\n\t" \
    "v_cndmask_b32_e64 v161, v69, v5, %[m1]\n\t" \
    "v_add_f32_dpp v162, v163, v162 wave_ror:1 row_mask:0xf bank_mask:0xf\n\t" \
    "v_mfma_f32_16x16x32_bf16 v[100:103], v[148:151], v[48:51], v[100:103]\n\t" \
    "ds_read_u16_d16_hi v168, %[ua2] offset:896\n\t" \
    "ds_read_u16_d16_hi v169, %[ua2] offset:928\n\t" \
    "v_mfma_f32_16x16x32_bf16 v[92:95], v[84:87], v[124:127], v[92:95]\n\t" \
    "ds_read_b64 v[116:117], %[ub2] offset:960\n\t" \
    "ds_read_b64 v[118:119], %[ub2] offset:968\n\t" \
    "v_mfma_f32_16x16x32_bf16 v[100:103], v[140:143], v[124:127], v[100:103]\n\t" \
    "ds_read_b64 v[132:133], %[ub2] offset:992\n\t" \
    "ds_read_b64 v[134:135], %[ub2] offset:1000\n\t" \
    "s_waitcnt lgkmcnt(6)\n\t" \
    "v_mfma_f32_16x16x32_bf16 v[92:95], v[88:91], v[156:159], v[92:95]\n\t" \
    "v_fmac_f32 v68, %[dd2], v175\n\t" \
    "v_mfma_f32_16x16x32_bf16 v[100:103], v[112:115], v[156:159], v[100:103]\n\t" \
    "v_add_f32_dpp v161, v162, v161 wave_ror:1 row_mask:0xf bank_mask:0xf\n\t" \
    "L_cv4_14:\n\t" \
    "s_cbranch_execz L_cv4_15\n\t" \
    "s_nop 1\n\t" \
    "v_add_f32_dpp v68, v161, v68 wave_ror:1 row_mask:0xf bank_mask:0xf\n\t" \
    "v_mfma_f32_16x16x32_bf16 v[104:107], v[96:99], v[60:63], v[104:107]\n\t" \
    "v_cvt_pk_bf16_f32 v160, v68, v68\n\t" \
    "ds_write_b16 %[ya], v160 offset:0\n\t" \
    "v_mfma_f32_16x16x32_bf16 v[108:111], v[44:47], v[60:63], v[108:111]\n\t" \
    "v_mfma_f32_16x16x32_bf16 v[104:107], v[52:55], v[40:43], v[104:107]\n\t" \
    "v_mfma_f32_16x16x32_bf16 v[108:111], v[148:151], v[40:43], v[108:111]\n\t" \
    "v_cndmask_b32_e64 v163, v95, v71, %[m3]\n\t" \
    "v_cndmask_b32_e64 v167, v103, v95, %[m3]\n\t" \
    "v_mfma_f32_16x16x32_bf16 v[104:107], v[84:87], v[48:51], v[104:107]\n\t" \
    "v_cndmask_b32_e64 v162, v94, v70, %[m2]\n\t" \
    "v_cndmask_b32_e64 v166, v102, v94, %[m2]\n\t" \
    "v_mfma_f32_16x16x32_bf16 v[108:111], v[140:143], v[48:51], v[108:111]\n\t" \
    "ds_read_u16_d16_hi v170, %[ua2] offset:960\n\t" \
    "ds_read_u16_d16_hi v171, %[ua2] offset:992\n\t" \
    "v_mfma_f32_16x16x32_bf16 v[104:107], v[88:91], v[124:127], v[104:107]\n\t" \
    "v_cndmask_b32_e64 v161, v93, v69, %[m1]\n\t" \
    "v_cndmask_b32_e64 v165, v101, v93, %[m1]\n\t" \
    "v_mfma_f32_16x16x32_bf16 v[108:111], v[112:115], v[124:127], v[108:111]\n\t" \
    "v_add_f32_dpp v162, v163, v162 wave_ror:1 row_mask:0xf bank_mask:0xf\n\t" \
    "v_add_f32_dpp v166, v167, v166 wave_ror:1 row_mask:0xf bank_mask:0xf\n\t" \
    "s_waitcnt lgkmcnt(3)\n\t" \
    "v_mfma_f32_16x16x32_bf16 v[104:107], v[116:119], v[156:159], v[104:107]\n\t" \
    "v_fmac_f32 v92, %[dd2], v168\n\t" \
    "v_mfma_f32_16x16x32_bf16 v[108:111], v[132:135], v[156:159], v[108:111]\n\t" \
    "v_fmac_f32 v100, %[dd2], v169\n\t" \
    "v_add_f32_dpp v161, v162, v161 wave_ror:1 row_mask:0xf bank_mask:0xf\n\t" \
    "v_add_f32_dpp v165, v166, v165 wave_ror:1 row_mask:0xf bank_mask:0xf\n\t" \
    "s_nop 0\n\t" \
    "v_add_f32_dpp v92, v161, v92 wave_ror:1 row_mask:0xf bank_mask:0xf\n\t" \
    "v_add_f32_dpp v100, v165, v100 wave_ror:1 row_mask:0xf bank_mask:0xf\n\t" \
    "v_cvt_pk_bf16_f32 v160, v92, v100\n\t" \
    "L_cv4_15:\n\t" \
    "s_cbranch_execz L_cv4_16\n\t" \
    "ds_write_b16 %[ya], v160 offset:128\n\t" \
    "ds_write_b16_d16_hi %[ya], v160 offset:256\n\t" \
    "v_cndmask_b32_e64 v163, v107, v103, %[m3]\n\t" \
    "v_cndmask_b32_e64 v167, v111, v107, %[m3]\n\t" \
    "v_cndmask_b32_e64 v162, v106, v102, %[m2]\n\t" \
    "v_cndmask_b32_e64 v166, v110, v106, %[m2]\n\t" \
    "v_cndmask_b32_e64 v161, v105, v101, %[m1]\n\t" \
    "v_cndmask_b32_e64 v165, v109, v105, %[m1]\n\t" \
    "v_add_f32_dpp v162, v163, v162 wave_ror:1 row_mask:0xf bank_mask:0xf\n\t" \
    "v_add_f32_dpp v166, v167, v166 wave_ror:1 row_mask:0xf bank_mask:0xf\n\t" \
    "s_waitcnt lgkmcnt(2)\n\t" \
    "v_fmac_f32 v104, %[dd2], v170\n\t" \
    "v_fmac_f32 v108, %[dd2], v171\n\t" \
    "v_add_f32_dpp v161, v162, v161 wave_ror:1 row_mask:0xf bank_mask:0xf\n\t" \
    "v_add_f32_dpp v165, v166, v165 wave_ror:1 row_mask:0xf bank_mask:0xf\n\t" \
    "s_nop 0\n\t" \
    "v_add_f32_dpp v104, v161, v104 wave_ror:1 row_mask:0xf bank_mask:0xf\n\t" \
    "v_add_f32_dpp v108, v165, v108 wave_ror:1 row_mask:0xf bank_mask:0xf\n\t" \
    "v_cvt_pk_bf16_f32 v160, v104, v108\n\t" \
    "ds_write_b16 %[ya], v160 offset:384\n\t" \
    "ds_write_b16_d16_hi %[ya], v160 offset:512\n\t" \
    "ds_read_b128 v[180:183], %[yr]\n\t" \
    "s_waitcnt vmcnt(0)\n\t" \
    "s_mov_b32 exec_hi, 0xffffff\n\t" \
    "buffer_store_dwordx4 v[176:179], %[l16], %[rs], %[yo] offen offset:2048 sc1\n\t" \
    "s_mov_b32 exec_hi, -1\n\t" \
    "s_waitcnt lgkmcnt(0)\n\t" \
    "s_mov_b32 exec_hi, 0xff\n\t" \
    "buffer_store_dwordx4 v[180:183], %[l16], %[rs], %[yo2] offen offset:3456 sc1\n\t" \
    "s_mov_b32 exec_hi, -1\n\t" \
    "s_nop 1\n\t" \
    "L_cv4_16:\n\t" \
    "s_cbranch_execz L_back\n\t" \
    "s_branch CVE_%=\n\t" \
    "L_cv5_0:\n\t" \
    "s_cbranch_execz L_cv5_1\n\t" \
    "ds_read_b64 v[0:1], %[ub] offset:0\n\t" \
    "ds_read_b64 v[2:3], %[ub] offset:8\n\t" \
    "ds_read_b64_tr_b16 v[4:5], %[hb] offset:2848\n\t" \
    "ds_read_b64_tr_b16 v[6:7], %[hb] offset:2816\n\t" \
    "ds_read_b64 v[8:9], %[ub] offset:32\n\t" \
    "ds_read_b64 v[10:11], %[ub] offset:40\n\t" \
    "ds_read_b64_tr_b16 v[12:13], %[hb] offset:3104\n\t" \
    "ds_read_b64_tr_b16 v[14:15], %[hb] offset:3072\n\t" \
    "ds_read_b64_tr_b16 v[16:17], %[hb] offset:3360\n\t" \
    "ds_read_b64_tr_b16 v[18:19], %[hb] offset:3328\n\t" \
    "ds_read_b64 v[20:21], %[ub] offset:64\n\t" \
    "ds_read_b64 v[22:23], %[ub] offset:72\n\t" \
    "v_mov_b32 v168, 0\n\t" \
    "v_mov_b32 v169, 0\n\t" \
    "v_mov_b32 v170, 0\n\t" \
    "v_mov_b32 v171, 0\n\t" \
    "v_mov_b32 v172, 0\n\t" \
    "v_mov_b32 v173, 0\n\t" \
    "v_mov_b32 v174, 0\n\t" \
    "v_mov_b32 v175, 0\n\t" \
    "s_waitcnt lgkmcnt(8)\n\t" \
    "v_mfma_f32_16x16x32_bf16 v[24:27], v[0:3], v[4:7], 0\n\t" \
    "ds_read_b64_tr_b16 v[28:29], %[hb] offset:2592\n\t" \
    "ds_read_b64_tr_b16 v[30:31], %[hb] offset:2560\n\t" \
    "ds_read_b64 v[32:33], %[ub] offset:96\n\t" \
    "s_waitcnt lgkmcnt(9)\n\t" \
    "v_mfma_f32_16x16x32_bf16 v[36:39], v[8:11], v[4:7], 0\n\t" \
    "ds_read_b64 v[34:35], %[ub] offset:104\n\t" \
    "ds_read_b64 v[40:41], %[ub] offset:128\n\t" \
    "ds_read_b64 v[42:43], %[ub] offset:136\n\t" \
    "s_waitcnt lgkmcnt(10)\n\t" \
    "v_mfma_f32_16x16x32_bf16 v[44:47], v[0:3], v[12:15], 0\n\t" \
    "ds_read_b64_tr_b16 v[48:49], %[hb] offset:2336\n\t" \
    "L_cv5_1:\n\t" \
    "s_cbranch_execz L_cv5_2\n\t" \
    "v_mfma_f32_16x16x32_bf16 v[52:55], v[8:11], v[12:15], 0\n\t" \
    "ds_read_b64_tr_b16 v[50:51], %[hb] offset:2304\n\t" \
    "ds_read_b64 v[56:57], %[ub] offset:160\n\t" \
    "s_waitcnt lgkmcnt(11)\n\t" \
    "v_mfma_f32_16x16x32_bf16 v[60:63], v[0:3], v[16:19], 0\n\t" \
    "ds_read_b64 v[58:59], %[ub] offset:168\n\t" \
    "v_mfma_f32_16x16x32_bf16 v[64:67], v[8:11], v[16:19], 0\n\t" \
    "s_waitcnt lgkmcnt(8)\n\t" \
    "v_mfma_f32_16x16x32_bf16 v[24:27], v[20:23], v[28:31], v[24:27]\n\t" \
    "ds_read_b64 v[68:69], %[ub] offset:192\n\t" \
    "s_waitcnt lgkmcnt(7)\n\t" \
    "v_mfma_f32_16x16x32_bf16 v[36:39], v[32:35], v[28:31], v[36:39]\n\t" \
    "ds_read_b64 v[70:71], %[ub] offset:200\n\t" \
    "v_mfma_f32_16x16x32_bf16 v[44:47], v[20:23], v[4:7], v[44:47]\n\t" \
    "ds_read_b64_tr_b16 v[72:73], %[hb] offset:2080\n\t" \
    "ds_read_b64_tr_b16 v[74:75], %[hb] offset:2048\n\t" \
    "v_mfma_f32_16x16x32_bf16 v[52:55], v[32:35], v[4:7], v[52:55]\n\t" \
    "ds_read_b64 v[76:77], %[ub] offset:224\n\t" \
    "ds_read_b64 v[78:79], %[ub] offset:232\n\t" \
    "v_mfma_f32_16x16x32_bf16 v[60:63], v[20:23], v[12:15], v[60:63]\n\t" \
    "v_mfma_f32_16x16x32_bf16 v[64:67], v[32:35], v[12:15], v[64:67]\n\t" \
    "s_waitcnt lgkmcnt(6)\n\t" \
    "v_mfma_f32_16x16x32_bf16 v[24:27], v[40:43], v[48:51], v[24:27]\n\t" \
    "ds_read_b64 v[80:81], %[ub] offset:256\n\t" \
    "v_mfma_f32_16x16x32_bf16 v[36:39], v[56:59], v[48:51], v[36:39]\n\t" \
    "ds_read_b64 v[82:83], %[ub] offset:264\n\t" \
    "ds_read_b64_tr_b16 v[84:85], %[hb] offset:1824\n\t" \
    "v_mfma_f32_16x16x32_bf16 v[44:47], v[40:43], v[28:31], v[44:47]\n\t" \
    "ds_read_b64_tr_b16 v[86:87], %[hb] offset:1792\n\t" \
    "ds_read_b64 v[88:89], %[ub] offset:288\n\t" \
    "v_mfma_f32_16x16x32_bf16 v[52:55], v[56:59], v[28:31], v[52:55]\n\t" \
    "ds_read_b64 v[90:91], %[ub] offset:296\n\t" \
    "v_mfma_f32_16x16x32_bf16 v[60:63], v[40:43], v[4:7], v[60:63]\n\t" \
    "L_cv5_2:\n\t" \
    "s_cbranch_execz L_cv5_3\n\t" \
    "v_mfma_f32_16x16x32_bf16 v[64:67], v[56:59], v[4:7], v[64:67]\n\t" \
    "s_waitcnt lgkmcnt(6)\n\t" \
    "v_mfma_f32_16x16x32_bf16 v[24:27], v[68:71], v[72:75], v[24:27]\n\t" \
    "ds_read_b64 v[92:93], %[ub] offset:320\n\t" \
    "v_mfma_f32_16x16x32_bf16 v[36:39], v[76:79], v[72:75], v[36:39]\n\t" \
    "ds_read_b64 v[94:95], %[ub] offset:328\n\t" \
    "ds_read_b64_tr_b16 v[96:97], %[hb] offset:1568\n\t" \
    "v_mfma_f32_16x16x32_bf16 v[44:47], v[68:71], v[48:51], v[44:47]\n\t" \
    "ds_read_b64_tr_b16 v[98:99], %[hb] offset:1536\n\t" \
    "ds_read_b64 v[100:101], %[ub] offset:352\n\t" \
    "v_mfma_f32_16x16x32_bf16 v[52:55], v[76:79], v[48:51], v[52:55]\n\t" \
    "ds_read_b64 v[102:103], %[ub] offset:360\n\t" \
    "v_mfma_f32_16x16x32_bf16 v[60:63], v[68:71], v[28:31], v[60:63]\n\t" \
    "v_mfma_f32_16x16x32_bf16 v[64:67], v[76:79], v[28:31], v[64:67]\n\t" \
    "s_waitcnt lgkmcnt(6)\n\t" \
    "v_mfma_f32_16x16x32_bf16 v[24:27], v[80:83], v[84:87], v[24:27]\n\t" \
    "ds_read_b64 v[104:105], %[ub] offset:384\n\t" \
    "v_mfma_f32_16x16x32_bf16 v[36:39], v[88:91], v[84:87], v[36:39]\n\t" \
    "ds_read_b64 v[106:107], %[ub] offset:392\n\t" \
    "ds_read_b64_tr_b16 v[108:109], %[hb] offset:1312\n\t" \
    "v_mfma_f32_16x16x32_bf16 v[44:47], v[80:83], v[72:75], v[44:47]\n\t" \
    "ds_read_b64_tr_b16 v[110:111], %[hb] offset:1280\n\t" \
    "ds_read_b64 v[112:113], %[ub] offset:416\n\t" \
    "v_mfma_f32_16x16x32_bf16 v[52:55], v[88:91], v[72:75], v[52:55]\n\t" \
    "ds_read_b64 v[114:115], %[ub] offset:424\n\t" \
    "v_mfma_f32_16x16x32_bf16 v[60:63], v[80:83], v[48:51], v[60:63]\n\t" \
    "L_cv5_3:\n\t" \
    "s_cbranch_execz L_cv5_4\n\t" \
    "v_mfma_f32_16x16x32_bf16 v[64:67], v[88:91], v[48:51], v[64:67]\n\t" \
    "s_waitcnt lgkmcnt(6)\n\t" \
    "v_mfma_f32_16x16x32_bf16 v[24:27], v[92:95], v[96:99], v[24:27]\n\t" \
    "ds_read_b64 v[116:117], %[ub] offset:448\n\t" \
    "v_mfma_f32_16x16x32_bf16 v[36:39], v[100:103], v[96:99], v[36:39]\n\t" \
    "ds_read_b64 v[118:119], %[ub] offset:456\n\t" \
    "ds_read_b64 v[120:121], %[ub] offset:480\n\t" \
    "v_mfma_f32_16x16x32_bf16 v[44:47], v[92:95], v[84:87], v[44:47]\n\t" \
    "ds_read_b64 v[122:123], %[ub] offset:488\n\t" \
    "v_mfma_f32_16x16x32_bf16 v[52:55], v[100:103], v[84:87], v[52:55]\n\t" \
    "v_mfma_f32_16x16x32_bf16 v[60:63], v[92:95], v[72:75], v[60:63]\n\t" \
    "ds_read_b64 v[124:125], %[ub] offset:512\n\t" \
    "ds_read_b64 v[126:127], %[ub] offset:520\n\t" \
    "v_mfma_f32_16x16x32_bf16 v[64:67], v[100:103], v[72:75], v[64:67]\n\t" \
    "ds_read_b64 v[128:129], %[ub] offset:544\n\t" \
    "ds_read_b64 v[130:131], %[ub] offset:552\n\t" \
    "s_waitcnt lgkmcnt(8)\n\t" \
    "v_mfma_f32_16x16x32_bf16 v[24:27], v[104:107], v[108:111], v[24:27]\n\t" \
    "ds_read_b64_tr_b16 v[132:133], %[hb] offset:1056\n\t" \
    "v_mfma_f32_16x16x32_bf16 v[36:39], v[112:115], v[108:111], v[36:39]\n\t" \
    "ds_read_b64_tr_b16 v[134:135], %[hb] offset:1024\n\t" \
    "v_mfma_f32_16x16x32_bf16 v[44:47], v[104:107], v[96:99], v[44:47]\n\t" \
    "ds_read_b64_tr_b16 v[136:137], %[hb] offset:800\n\t" \
    "ds_read_b64_tr_b16 v[138:139], %[hb] offset:768\n\t" \
    "v_mfma_f32_16x16x32_bf16 v[52:55], v[112:115], v[96:99], v[52:55]\n\t" \
    "v_mfma_f32_16x16x32_bf16 v[60:63], v[104:107], v[84:87], v[60:63]\n\t" \
    "ds_read_b64 v[140:141], %[ub] offset:576\n\t" \
    "ds_read_b64 v[142:143], %[ub] offset:584\n\t" \
    "v_mfma_f32_16x16x32_bf16 v[64:67], v[112:115], v[84:87], v[64:67]\n\t" \
    "ds_read_b64_tr_b16 v[144:145], %[hb] offset:544\n\t" \
    "ds_read_b64_tr_b16 v[146:147], %[hb] offset:512\n\t" \
    "s_waitcnt lgkmcnt(12)\n\t" \
    "v_mfma_f32_16x16x32_bf16 v[44:47], v[116:119], v[108:111], v[44:47]\n\t" \
    "L_cv5_4:\n\t" \
    "s_cbranch_execz L_cv5_5\n\t" \
    "ds_read_b64 v[148:149], %[ub] offset:608\n\t" \
    "v_mfma_f32_16x16x32_bf16 v[52:55], v[120:123], v[108:111], v[52:55]\n\t" \
    "ds_read_b64 v[150:151], %[ub] offset:616\n\t" \
    "ds_read_b64 v[152:153], %[ub] offset:640\n\t" \
    "v_mfma_f32_16x16x32_bf16 v[60:63], v[116:119], v[96:99], v[60:63]\n\t" \
    "ds_read_b64 v[154:155], %[ub] offset:648\n\t" \
    "ds_read_b64_tr_b16 v[156:157], %[hb] offset:288\n\t" \
    "v_mfma_f32_16x16x32_bf16 v[64:67], v[120:123], v[96:99], v[64:67]\n\t" \
    "ds_read_b64_tr_b16 v[158:159], %[hb] offset:256\n\t" \
    "ds_read_b64 v[0:1], %[ub] offset:672\n\t" \
    "s_waitcnt lgkmcnt(14)\n\t" \
    "v_mfma_f32_16x16x32_bf16 v[60:63], v[124:127], v[108:111], v[60:63]\n\t" \
    "ds_read_b64 v[2:3], %[ub] offset:680\n\t" \
    "v_mfma_f32_16x16x32_bf16 v[64:67], v[128:131], v[108:111], v[64:67]\n\t" \
    "ds_read_b64 v[8:9], %[ub] offset:704\n\t" \
    "ds_read_b64 v[10:11], %[ub] offset:712\n\t" \
    "s_waitcnt lgkmcnt(14)\n\t" \
    "v_mfma_f32_16x16x32_bf16 v[24:27], v[116:119], v[132:135], v[24:27]\n\t" \
    "ds_read_b64_tr_b16 v[16:17], %[hb] offset:32\n\t" \
    "ds_read_b64_tr_b16 v[18:19], %[hb] offset:0\n\t" \
    "ds_read_b64 v[20:21], %[ub] offset:736\n\t" \
    "v_mfma_f32_16x16x32_bf16 v[36:39], v[120:123], v[132:135], v[36:39]\n\t" \
    "ds_read_b64 v[22:23], %[ub] offset:744\n\t" \
    "v_mfma_f32_16x16x32_bf16 v[24:27], v[124:127], v[136:139], v[24:27]\n\t" \
    "v_mfma_f32_16x16x32_bf16 v[36:39], v[128:131], v[136:139], v[36:39]\n\t" \
    "s_waitcnt lgkmcnt(12)\n\t" \
    "v_mfma_f32_16x16x32_bf16 v[24:27], v[140:143], v[144:147], v[24:27]\n\t" \
    "L_cv5_5:\n\t" \
    "s_cbranch_execz L_cv5_6\n\t" \
    "v_mfma_f32_16x16x32_bf16 v[36:39], v[148:151], v[144:147], v[36:39]\n\t" \
    "ds_read_u16_d16_hi v168, %[ua] offset:736\n\t" \
    "s_waitcnt lgkmcnt(7)\n\t" \
    "v_mfma_f32_16x16x32_bf16 v[24:27], v[152:155], v[156:159], v[24:27]\n\t" \
    "ds_read_b64 v[32:33], %[ub] offset:768\n\t" \
    "v_mfma_f32_16x16x32_bf16 v[36:39], v[0:3], v[156:159], v[36:39]\n\t" \
    "ds_read_b64 v[34:35], %[ub] offset:776\n\t" \
    "ds_read_b64 v[12:13], %[ub] offset:800\n\t" \
    "s_waitcnt lgkmcnt(4)\n\t" \
    "v_mfma_f32_16x16x32_bf16 v[24:27], v[8:11], v[16:19], v[24:27]\n\t" \
    "ds_read_b64 v[14:15], %[ub] offset:808\n\t" \
    "v_mfma_f32_16x16x32_bf16 v[36:39], v[20:23], v[16:19], v[36:39]\n\t" \
    "v_mfma_f32_16x16x32_bf16 v[44:47], v[124:127], v[132:135], v[44:47]\n\t" \
    "v_mfma_f32_16x16x32_bf16 v[52:55], v[128:131], v[132:135], v[52:55]\n\t" \
    "v_mfma_f32_16x16x32_bf16 v[44:47], v[140:143], v[136:139], v[44:47]\n\t" \
    "v_mfma_f32_16x16x32_bf16 v[52:55], v[148:151], v[136:139], v[52:55]\n\t" \
    "s_nop 3\n\t" \
    "v_cndmask_b32_e64 v163, v39, v27, %[m3]\n\t" \
    "v_cndmask_b32_e64 v162, v38, v26, %[m2]\n\t" \
    "v_mfma_f32_16x16x32_bf16 v[44:47], v[152:155], v[144:147], v[44:47]\n\t" \
    "v_cndmask_b32_e64 v161, v37, v25, %[m1]\n\t" \
    "v_add_f32_dpp v162, v163, v162 wave_ror:1 row_mask:0xf bank_mask:0xf\n\t" \
    "v_mfma_f32_16x16x32_bf16 v[52:55], v[0:3], v[144:147], v[52:55]\n\t" \
    "ds_read_u16_d16_hi v169, %[ua] offset:768\n\t" \
    "ds_read_u16_d16_hi v170, %[ua] offset:800\n\t" \
    "v_mfma_f32_16x16x32_bf16 v[44:47], v[8:11], v[156:159], v[44:47]\n\t" \
    "ds_read_b64 v[40:41], %[ub] offset:832\n\t" \
    "ds_read_b64 v[42:43], %[ub] offset:840\n\t" \
    "v_mfma_f32_16x16x32_bf16 v[52:55], v[20:23], v[156:159], v[52:55]\n\t" \
    "ds_read_b64 v[56:57], %[ub] offset:864\n\t" \
    "ds_read_b64 v[58:59], %[ub] offset:872\n\t" \
    "s_waitcnt lgkmcnt(6)\n\t" \
    "v_mfma_f32_16x16x32_bf16 v[44:47], v[32:35], v[16:19], v[44:47]\n\t" \
    "L_cv5_6:\n\t" \
    "s_cbranch_execz L_cv5_7\n\t" \
    "ds_read_b64 v[4:5], %[ub2] offset:0\n\t" \
    "v_mfma_f32_16x16x32_bf16 v[52:55], v[12:15], v[16:19], v[52:55]\n\t" \
    "ds_read_b64 v[6:7], %[ub2] offset:8\n\t" \
    "ds_read_b64_tr_b16 v[68:69], %[hb2] offset:3360\n\t" \
    "v_mfma_f32_16x16x32_bf16 v[60:63], v[140:143], v[132:135], v[60:63]\n\t" \
    "ds_read_b64_tr_b16 v[70:71], %[hb2] offset:3328\n\t" \
    "ds_read_b64 v[76:77], %[ub2] offset:32\n\t" \
    "v_mfma_f32_16x16x32_bf16 v[64:67], v[148:151], v[132:135], v[64:67]\n\t" \
    "ds_read_b64 v[78:79], %[ub2] offset:40\n\t" \
    "ds_read_b64_tr_b16 v[28:29], %[hb2] offset:3616\n\t" \
    "v_mfma_f32_16x16x32_bf16 v[60:63], v[152:155], v[136:139], v[60:63]\n\t" \
    "ds_read_b64_tr_b16 v[30:31], %[hb2] offset:3584\n\t" \
    "ds_read_b64_tr_b16 v[80:81], %[hb2] offset:3872\n\t" \
    "v_mfma_f32_16x16x32_bf16 v[64:67], v[0:3], v[136:139], v[64:67]\n\t" \
    "ds_read_b64_tr_b16 v[82:83], %[hb2] offset:3840\n\t" \
    "v_fmac_f32 v36, %[dd], v168\n\t" \
    "v_mfma_f32_16x16x32_bf16 v[60:63], v[8:11], v[144:147], v[60:63]\n\t" \
    "ds_read_b64 v[88:89], %[ub2] offset:64\n\t" \
    "ds_read_b64 v[90:91], %[ub2] offset:72\n\t" \
    "v_mfma_f32_16x16x32_bf16 v[64:67], v[20:23], v[144:147], v[64:67]\n\t" \
    "ds_read_b64_tr_b16 v[48:49], %[hb2] offset:3104\n\t" \
    "ds_read_b64_tr_b16 v[50:51], %[hb2] offset:3072\n\t" \
    "v_mfma_f32_16x16x32_bf16 v[60:63], v[32:35], v[156:159], v[60:63]\n\t" \
    "ds_read_b64 v[92:93], %[ub2] offset:96\n\t" \
    "ds_read_b64 v[94:95], %[ub2] offset:104\n\t" \
    "v_mfma_f32_16x16x32_bf16 v[64:67], v[12:15], v[156:159], v[64:67]\n\t" \
    "ds_read_u16_d16_hi v171, %[ua] offset:832\n\t" \
    "ds_read_u16_d16_hi v172, %[ua] offset:864\n\t" \
    "s_waitcnt lgkmcnt(14)\n\t" \
    "v_mfma_f32_16x16x32_bf16 v[60:63], v[40:43], v[16:19], v[60:63]\n\t" \
    "v_add_f32_dpp v161, v162, v161 wave_ror:1 row_mask:0xf bank_mask:0xf\n\t" \
    "v_mfma_f32_16x16x32_bf16 v[64:67], v[56:59], v[16:19], v[64:67]\n\t" \
    "L_cv5_7:\n\t" \
    "s_cbranch_execz L_cv5_8\n\t" \
    "s_nop 0\n\t" \
    "v_add_f32_dpp v36, v161, v36 wave_ror:1 row_mask:0xf bank_mask:0xf\n\t" \
    "v_cvt_pk_bf16_f32 v160, v36, v36\n\t" \
    "v_mfma_f32_16x16x32_bf16 v[100:103], v[4:7], v[68:71], 0\n\t" \
    "ds_read_b64 v[72:73], %[ub2] offset:128\n\t" \
    "ds_read_b64 v[74:75], %[ub2] offset:136\n\t" \
    "s_waitcnt lgkmcnt(12)\n\t" \
    "v_mfma_f32_16x16x32_bf16 v[104:107], v[76:79], v[68:71], 0\n\t" \
    "ds_read_b64_tr_b16 v[112:113], %[hb2] offset:2848\n\t" \
    "v_mfma_f32_16x16x32_bf16 v[84:87], v[4:7], v[28:31], 0\n\t" \
    "ds_read_b64_tr_b16 v[114:115], %[hb2] offset:2816\n\t" \
    "ds_read_b64 v[96:97], %[ub2] offset:160\n\t" \
    "v_mfma_f32_16x16x32_bf16 v[108:111], v[76:79], v[28:31], 0\n\t" \
    "ds_read_b64 v[98:99], %[ub2] offset:168\n\t" \
    "ds_write_b16 %[ya], v160 offset:0\n\t" \
    "s_waitcnt lgkmcnt(14)\n\t" \
    "v_mfma_f32_16x16x32_bf16 v[116:119], v[4:7], v[80:83], 0\n\t" \
    "v_cndmask_b32_e64 v163, v47, v39, %[m3]\n\t" \
    "v_mfma_f32_16x16x32_bf16 v[120:123], v[76:79], v[80:83], 0\n\t" \
    "v_cndmask_b32_e64 v167, v55, v47, %[m3]\n\t" \
    "v_cndmask_b32_e64 v162, v46, v38, %[m2]\n\t" \
    "s_waitcnt lgkmcnt(9)\n\t" \
    "v_mfma_f32_16x16x32_bf16 v[100:103], v[88:91], v[48:51], v[100:103]\n\t" \
    "ds_read_b64 v[124:125], %[ub2] offset:192\n\t" \
    "v_mfma_f32_16x16x32_bf16 v[104:107], v[92:95], v[48:51], v[104:107]\n\t" \
    "ds_read_b64 v[126:127], %[ub2] offset:200\n\t" \
    "ds_read_b64_tr_b16 v[128:129], %[hb2] offset:2592\n\t" \
    "v_mfma_f32_16x16x32_bf16 v[84:87], v[88:91], v[68:71], v[84:87]\n\t" \
    "ds_read_b64_tr_b16 v[130:131], %[hb2] offset:2560\n\t" \
    "ds_read_b64 v[140:141], %[ub2] offset:224\n\t" \
    "v_mfma_f32_16x16x32_bf16 v[108:111], v[92:95], v[68:71], v[108:111]\n\t" \
    "ds_read_b64 v[142:143], %[ub2] offset:232\n\t" \
    "v_cndmask_b32_e64 v166, v54, v46, %[m2]\n\t" \
    "L_cv5_8:\n\t" \
    "s_cbranch_execz L_cv5_9\n\t" \
    "v_mfma_f32_16x16x32_bf16 v[116:119], v[88:91], v[28:31], v[116:119]\n\t" \
    "v_cndmask_b32_e64 v161, v45, v37, %[m1]\n\t" \
    "v_cndmask_b32_e64 v165, v53, v45, %[m1]\n\t" \
    "v_mfma_f32_16x16x32_bf16 v[120:123], v[92:95], v[28:31], v[120:123]\n\t" \
    "v_add_f32_dpp v162, v163, v162 wave_ror:1 row_mask:0xf bank_mask:0xf\n\t" \
    "v_add_f32_dpp v166, v167, v166 wave_ror:1 row_mask:0xf bank_mask:0xf\n\t" \
    "s_waitcnt lgkmcnt(7)\n\t" \
    "v_mfma_f32_16x16x32_bf16 v[100:103], v[72:75], v[112:115], v[100:103]\n\t" \
    "ds_read_b64 v[148:149], %[ub2] offset:256\n\t" \
    "v_mfma_f32_16x16x32_bf16 v[104:107], v[96:99], v[112:115], v[104:107]\n\t" \
    "ds_read_b64 v[150:151], %[ub2] offset:264\n\t" \
    "ds_read_b64_tr_b16 v[132:133], %[hb2] offset:2336\n\t" \
    "v_mfma_f32_16x16x32_bf16 v[84:87], v[72:75], v[48:51], v[84:87]\n\t" \
    "ds_read_b64_tr_b16 v[134:135], %[hb2] offset:2304\n\t" \
    "ds_read_b64 v[152:153], %[ub2] offset:288\n\t" \
    "v_mfma_f32_16x16x32_bf16 v[108:111], v[96:99], v[48:51], v[108:111]\n\t" \
    "ds_read_b64 v[154:155], %[ub2] offset:296\n\t" \
    "v_fmac_f32 v44, %[dd], v169\n\t" \
    "v_mfma_f32_16x16x32_bf16 v[116:119], v[72:75], v[68:71], v[116:119]\n\t" \
    "v_fmac_f32 v52, %[dd], v170\n\t" \
    "v_add_f32_dpp v161, v162, v161 wave_ror:1 row_mask:0xf bank_mask:0xf\n\t" \
    "v_mfma_f32_16x16x32_bf16 v[120:123], v[96:99], v[68:71], v[120:123]\n\t" \
    "v_add_f32_dpp v165, v166, v165 wave_ror:1 row_mask:0xf bank_mask:0xf\n\t" \
    "v_add_f32_dpp v44, v161, v44 wave_ror:1 row_mask:0xf bank_mask:0xf\n\t" \
    "s_waitcnt lgkmcnt(6)\n\t" \
    "v_mfma_f32_16x16x32_bf16 v[100:103], v[124:127], v[128:131], v[100:103]\n\t" \
    "ds_read_b64 v[0:1], %[ub2] offset:320\n\t" \
    "v_mfma_f32_16x16x32_bf16 v[104:107], v[140:143], v[128:131], v[104:107]\n\t" \
    "ds_read_b64 v[2:3], %[ub2] offset:328\n\t" \
    "ds_read_b64_tr_b16 v[136:137], %[hb2] offset:2080\n\t" \
    "v_mfma_f32_16x16x32_bf16 v[84:87], v[124:127], v[112:115], v[84:87]\n\t" \
    "ds_read_b64_tr_b16 v[138:139], %[hb2] offset:2048\n\t" \
    "L_cv5_9:\n\t" \
    "s_cbranch_execz L_cv5_10\n\t" \
    "ds_read_b64 v[8:9], %[ub2] offset:352\n\t" \
    "v_mfma_f32_16x16x32_bf16 v[108:111], v[140:143], v[112:115], v[108:111]\n\t" \
    "ds_read_b64 v[10:11], %[ub2] offset:360\n\t" \
    "v_add_f32_dpp v52, v165, v52 wave_ror:1 row_mask:0xf bank_mask:0xf\n\t" \
    "v_mfma_f32_16x16x32_bf16 v[116:119], v[124:127], v[48:51], v[116:119]\n\t" \
    "v_cvt_pk_bf16_f32 v160, v44, v52\n\t" \
    "ds_write_b16 %[ya], v160 offset:128\n\t" \
    "v_mfma_f32_16x16x32_bf16 v[120:123], v[140:143], v[48:51], v[120:123]\n\t" \
    "ds_write_b16_d16_hi %[ya], v160 offset:256\n\t" \
    "v_cndmask_b32_e64 v163, v63, v55, %[m3]\n\t" \
    "s_waitcnt lgkmcnt(8)\n\t" \
    "v_mfma_f32_16x16x32_bf16 v[100:103], v[148:151], v[132:135], v[100:103]\n\t" \
    "ds_read_b64 v[20:21], %[ub2] offset:384\n\t" \
    "v_mfma_f32_16x16x32_bf16 v[104:107], v[152:155], v[132:135], v[104:107]\n\t" \
    "ds_read_b64 v[22:23], %[ub2] offset:392\n\t" \
    "ds_read_b64_tr_b16 v[144:145], %[hb2] offset:1824\n\t" \
    "v_mfma_f32_16x16x32_bf16 v[84:87], v[148:151], v[128:131], v[84:87]\n\t" \
    "ds_read_b64_tr_b16 v[146:147], %[hb2] offset:1792\n\t" \
    "ds_read_b64 v[32:33], %[ub2] offset:416\n\t" \
    "v_mfma_f32_16x16x32_bf16 v[108:111], v[152:155], v[128:131], v[108:111]\n\t" \
    "ds_read_b64 v[34:35], %[ub2] offset:424\n\t" \
    "v_cndmask_b32_e64 v167, v67, v63, %[m3]\n\t" \
    "v_mfma_f32_16x16x32_bf16 v[116:119], v[148:151], v[112:115], v[116:119]\n\t" \
    "v_cndmask_b32_e64 v162, v62, v54, %[m2]\n\t" \
    "v_cndmask_b32_e64 v166, v66, v62, %[m2]\n\t" \
    "v_mfma_f32_16x16x32_bf16 v[120:123], v[152:155], v[112:115], v[120:123]\n\t" \
    "v_cndmask_b32_e64 v161, v61, v53, %[m1]\n\t" \
    "v_cndmask_b32_e64 v165, v65, v61, %[m1]\n\t" \
    "s_waitcnt lgkmcnt(8)\n\t" \
    "v_mfma_f32_16x16x32_bf16 v[100:103], v[0:3], v[136:139], v[100:103]\n\t" \
    "ds_read_b64 v[12:13], %[ub2] offset:448\n\t" \
    "v_mfma_f32_16x16x32_bf16 v[104:107], v[8:11], v[136:139], v[104:107]\n\t" \
    "L_cv5_10:\n\t" \
    "s_cbranch_execz L_cv5_11\n\t" \
    "ds_read_b64 v[14:15], %[ub2] offset:456\n\t" \
    "ds_read_b64_tr_b16 v[156:157], %[hb2] offset:1568\n\t" \
    "v_mfma_f32_16x16x32_bf16 v[84:87], v[0:3], v[132:135], v[84:87]\n\t" \
    "ds_read_b64_tr_b16 v[158:159], %[hb2] offset:1536\n\t" \
    "ds_read_b64 v[40:41], %[ub2] offset:480\n\t" \
    "v_mfma_f32_16x16x32_bf16 v[108:111], v[8:11], v[132:135], v[108:111]\n\t" \
    "ds_read_b64 v[42:43], %[ub2] offset:488\n\t" \
    "v_add_f32_dpp v162, v163, v162 wave_ror:1 row_mask:0xf bank_mask:0xf\n\t" \
    "v_mfma_f32_16x16x32_bf16 v[116:119], v[0:3], v[128:131], v[116:119]\n\t" \
    "v_add_f32_dpp v166, v167, v166 wave_ror:1 row_mask:0xf bank_mask:0xf\n\t" \
    "v_fmac_f32 v60, %[dd], v171\n\t" \
    "v_mfma_f32_16x16x32_bf16 v[120:123], v[8:11], v[128:131], v[120:123]\n\t" \
    "v_fmac_f32 v64, %[dd], v172\n\t" \
    "v_add_f32_dpp v161, v162, v161 wave_ror:1 row_mask:0xf bank_mask:0xf\n\t" \
    "s_waitcnt lgkmcnt(6)\n\t" \
    "v_mfma_f32_16x16x32_bf16 v[100:103], v[20:23], v[144:147], v[100:103]\n\t" \
    "ds_read_b64 v[24:25], %[ub2] offset:512\n\t" \
    "v_mfma_f32_16x16x32_bf16 v[104:107], v[32:35], v[144:147], v[104:107]\n\t" \
    "ds_read_b64 v[26:27], %[ub2] offset:520\n\t" \
    "ds_read_b64_tr_b16 v[56:57], %[hb2] offset:1312\n\t" \
    "v_mfma_f32_16x16x32_bf16 v[84:87], v[20:23], v[136:139], v[84:87]\n\t" \
    "ds_read_b64_tr_b16 v[58:59], %[hb2] offset:1280\n\t" \
    "ds_read_b64 v[16:17], %[ub2] offset:544\n\t" \
    "v_mfma_f32_16x16x32_bf16 v[108:111], v[32:35], v[136:139], v[108:111]\n\t" \
    "ds_read_b64 v[18:19], %[ub2] offset:552\n\t" \
    "v_add_f32_dpp v165, v166, v165 wave_ror:1 row_mask:0xf bank_mask:0xf\n\t" \
    "v_mfma_f32_16x16x32_bf16 v[116:119], v[20:23], v[132:135], v[116:119]\n\t" \
    "v_add_f32_dpp v60, v161, v60 wave_ror:1 row_mask:0xf bank_mask:0xf\n\t" \
    "v_add_f32_dpp v64, v165, v64 wave_ror:1 row_mask:0xf bank_mask:0xf\n\t" \
    "v_mfma_f32_16x16x32_bf16 v[120:123], v[32:35], v[132:135], v[120:123]\n\t" \
    "v_cvt_pk_bf16_f32 v160, v60, v64\n\t" \
    "ds_write_b16 %[ya], v160 offset:384\n\t" \
    "L_cv5_11:\n\t" \
    "s_cbranch_execz L_cv5_12\n\t" \
    "s_waitcnt lgkmcnt(7)\n\t" \
    "v_mfma_f32_16x16x32_bf16 v[100:103], v[12:15], v[156:159], v[100:103]\n\t" \
    "ds_read_b64 v[4:5], %[ub2] offset:576\n\t" \
    "v_mfma_f32_16x16x32_bf16 v[104:107], v[40:43], v[156:159], v[104:107]\n\t" \
    "ds_read_b64 v[6:7], %[ub2] offset:584\n\t" \
    "ds_read_b64 v[76:77], %[ub2] offset:608\n\t" \
    "v_mfma_f32_16x16x32_bf16 v[84:87], v[12:15], v[144:147], v[84:87]\n\t" \
    "ds_read_b64 v[78:79], %[ub2] offset:616\n\t" \
    "ds_write_b16_d16_hi %[ya], v160 offset:512\n\t" \
    "v_mfma_f32_16x16x32_bf16 v[108:111], v[40:43], v[144:147], v[108:111]\n\t" \
    "ds_write_b32 %[fc], %[fc] offset:4\n\t" \
    "ds_read_b128 v[176:179], %[yr]\n\t" \
    "v_mfma_f32_16x16x32_bf16 v[116:119], v[12:15], v[136:139], v[116:119]\n\t" \
    "ds_read_b64 v[80:81], %[ub2] offset:640\n\t" \
    "ds_read_b64 v[82:83], %[ub2] offset:648\n\t" \
    "v_mfma_f32_16x16x32_bf16 v[120:123], v[40:43], v[136:139], v[120:123]\n\t" \
    "ds_read_b64 v[88:89], %[ub2] offset:672\n\t" \
    "ds_read_b64 v[90:91], %[ub2] offset:680\n\t" \
    "s_waitcnt lgkmcnt(12)\n\t" \
    "v_mfma_f32_16x16x32_bf16 v[100:103], v[24:27], v[56:59], v[100:103]\n\t" \
    "ds_read_b64_tr_b16 v[92:93], %[hb2] offset:1056\n\t" \
    "v_mfma_f32_16x16x32_bf16 v[104:107], v[16:19], v[56:59], v[104:107]\n\t" \
    "ds_read_b64_tr_b16 v[94:95], %[hb2] offset:1024\n\t" \
    "v_mfma_f32_16x16x32_bf16 v[84:87], v[24:27], v[156:159], v[84:87]\n\t" \
    "ds_read_b64_tr_b16 v[28:29], %[hb2] offset:800\n\t" \
    "ds_read_b64_tr_b16 v[30:31], %[hb2] offset:768\n\t" \
    "v_mfma_f32_16x16x32_bf16 v[108:111], v[16:19], v[156:159], v[108:111]\n\t" \
    "L_cv5_12:\n\t" \
    "s_cbranch_execz L_cv5_13\n\t" \
    "v_mfma_f32_16x16x32_bf16 v[116:119], v[24:27], v[144:147], v[116:119]\n\t" \
    "ds_read_b64 v[72:73], %[ub2] offset:704\n\t" \
    "ds_read_b64 v[74:75], %[ub2] offset:712\n\t" \
    "v_mfma_f32_16x16x32_bf16 v[120:123], v[16:19], v[144:147], v[120:123]\n\t" \
    "ds_read_b64_tr_b16 v[96:97], %[hb2] offset:544\n\t" \
    "ds_read_b64_tr_b16 v[98:99], %[hb2] offset:512\n\t" \
    "s_waitcnt lgkmcnt(14)\n\t" \
    "v_mfma_f32_16x16x32_bf16 v[84:87], v[4:7], v[56:59], v[84:87]\n\t" \
    "ds_read_b64 v[68:69], %[ub2] offset:736\n\t" \
    "v_mfma_f32_16x16x32_bf16 v[108:111], v[76:79], v[56:59], v[108:111]\n\t" \
    "ds_read_b64 v[70:71], %[ub2] offset:744\n\t" \
    "ds_read_b64 v[36:37], %[ub2] offset:768\n\t" \
    "v_mfma_f32_16x16x32_bf16 v[116:119], v[4:7], v[156:159], v[116:119]\n\t" \
    "ds_read_b64 v[38:39], %[ub2] offset:776\n\t" \
    "ds_read_b64_tr_b16 v[44:45], %[hb2] offset:288\n\t" \
    "v_mfma_f32_16x16x32_bf16 v[120:123], v[76:79], v[156:159], v[120:123]\n\t" \
    "ds_read_b64_tr_b16 v[46:47], %[hb2] offset:256\n\t" \
    "ds_read_b64 v[124:125], %[ub2] offset:800\n\t" \
    "s_waitcnt lgkmcnt(14)\n\t" \
    "v_mfma_f32_16x16x32_bf16 v[116:119], v[80:83], v[56:59], v[116:119]\n\t" \
    "ds_read_b64 v[126:127], %[ub2] offset:808\n\t" \
    "v_mfma_f32_16x16x32_bf16 v[120:123], v[88:91], v[56:59], v[120:123]\n\t" \
    "ds_read_b64 v[140:141], %[ub2] offset:832\n\t" \
    "ds_read_b64 v[142:143], %[ub2] offset:840\n\t" \
    "s_waitcnt lgkmcnt(14)\n\t" \
    "v_mfma_f32_16x16x32_bf16 v[100:103], v[4:7], v[92:95], v[100:103]\n\t" \
    "ds_read_b64_tr_b16 v[48:49], %[hb2] offset:32\n\t" \
    "ds_read_b64_tr_b16 v[50:51], %[hb2] offset:0\n\t" \
    "ds_read_b64 v[148:149], %[ub2] offset:864\n\t" \
    "v_mfma_f32_16x16x32_bf16 v[104:107], v[76:79], v[92:95], v[104:107]\n\t" \
    "ds_read_b64 v[150:151], %[ub2] offset:872\n\t" \
    "v_mfma_f32_16x16x32_bf16 v[100:103], v[80:83], v[28:31], v[100:103]\n\t" \
    "L_cv5_13:\n\t" \
    "s_cbranch_execz L_cv5_14\n\t" \
    "v_mfma_f32_16x16x32_bf16 v[104:107], v[88:91], v[28:31], v[104:107]\n\t" \
    "s_waitcnt lgkmcnt(12)\n\t" \
    "v_mfma_f32_16x16x32_bf16 v[100:103], v[72:75], v[96:99], v[100:103]\n\t" \
    "v_mfma_f32_16x16x32_bf16 v[104:107], v[68:71], v[96:99], v[104:107]\n\t" \
    "ds_read_u16_d16_hi v173, %[ua2] offset:864\n\t" \
    "s_waitcnt lgkmcnt(7)\n\t" \
    "v_mfma_f32_16x16x32_bf16 v[100:103], v[36:39], v[44:47], v[100:103]\n\t" \
    "ds_read_b64 v[152:153], %[ub2] offset:896\n\t" \
    "v_mfma_f32_16x16x32_bf16 v[104:107], v[124:127], v[44:47], v[104:107]\n\t" \
    "ds_read_b64 v[154:155], %[ub2] offset:904\n\t" \
    "ds_read_b64 v[112:113], %[ub2] offset:928\n\t" \
    "s_waitcnt lgkmcnt(4)\n\t" \
    "v_mfma_f32_16x16x32_bf16 v[100:103], v[140:143], v[48:51], v[100:103]\n\t" \
    "ds_read_b64 v[114:115], %[ub2] offset:936\n\t" \
    "v_mfma_f32_16x16x32_bf16 v[104:107], v[148:151], v[48:51], v[104:107]\n\t" \
    "v_mfma_f32_16x16x32_bf16 v[84:87], v[80:83], v[92:95], v[84:87]\n\t" \
    "v_mfma_f32_16x16x32_bf16 v[108:111], v[88:91], v[92:95], v[108:111]\n\t" \
    "v_mfma_f32_16x16x32_bf16 v[84:87], v[72:75], v[28:31], v[84:87]\n\t" \
    "v_mfma_f32_16x16x32_bf16 v[108:111], v[68:71], v[28:31], v[108:111]\n\t" \
    "s_nop 3\n\t" \
    "v_cndmask_b32_e64 v163, v107, v103, %[m3]\n\t" \
    "v_cndmask_b32_e64 v162, v106, v102, %[m2]\n\t" \
    "v_mfma_f32_16x16x32_bf16 v[84:87], v[36:39], v[96:99], v[84:87]\n\t" \
    "v_cndmask_b32_e64 v161, v105, v101, %[m1]\n\t" \
    "v_add_f32_dpp v162, v163, v162 wave_ror:1 row_mask:0xf bank_mask:0xf\n\t" \
    "v_mfma_f32_16x16x32_bf16 v[108:111], v[124:127], v[96:99], v[108:111]\n\t" \
    "ds_read_u16_d16_hi v174, %[ua2] offset:896\n\t" \
    "ds_read_u16_d16_hi v175, %[ua2] offset:928\n\t" \
    "v_mfma_f32_16x16x32_bf16 v[84:87], v[140:143], v[44:47], v[84:87]\n\t" \
    "ds_read_b64 v[0:1], %[ub2] offset:960\n\t" \
    "ds_read_b64 v[2:3], %[ub2] offset:968\n\t" \
    "v_mfma_f32_16x16x32_bf16 v[108:111], v[148:151], v[44:47], v[108:111]\n\t" \
    "ds_read_b64 v[8:9], %[ub2] offset:992\n\t" \
    "L_cv5_14:\n\t" \
    "s_cbranch_execz L_cv5_15\n\t" \
    "ds_read_b64 v[10:11], %[ub2] offset:1000\n\t" \
    "s_waitcnt lgkmcnt(6)\n\t" \
    "v_mfma_f32_16x16x32_bf16 v[84:87], v[152:155], v[48:51], v[84:87]\n\t" \
    "v_fmac_f32 v104, %[dd2], v173\n\t" \
    "v_mfma_f32_16x16x32_bf16 v[108:111], v[112:115], v[48:51], v[108:111]\n\t" \
    "v_add_f32_dpp v161, v162, v161 wave_ror:1 row_mask:0xf bank_mask:0xf\n\t" \
    "s_nop 1\n\t" \
    "v_add_f32_dpp v104, v161, v104 wave_ror:1 row_mask:0xf bank_mask:0xf\n\t" \
    "v_mfma_f32_16x16x32_bf16 v[116:119], v[72:75], v[92:95], v[116:119]\n\t" \
    "v_cvt_pk_bf16_f32 v160, v104, v104\n\t" \
    "ds_write_b16 %[ya], v160 offset:0\n\t" \
    "v_mfma_f32_16x16x32_bf16 v[120:123], v[68:71], v[92:95], v[120:123]\n\t" \
    "v_mfma_f32_16x16x32_bf16 v[116:119], v[36:39], v[28:31], v[116:119]\n\t" \
    "v_mfma_f32_16x16x32_bf16 v[120:123], v[124:127], v[28:31], v[120:123]\n\t" \
    "v_cndmask_b32_e64 v163, v87, v107, %[m3]\n\t" \
    "v_cndmask_b32_e64 v167, v111, v87, %[m3]\n\t" \
    "v_mfma_f32_16x16x32_bf16 v[116:119], v[140:143], v[96:99], v[116:119]\n\t" \
    "v_cndmask_b32_e64 v162, v86, v106, %[m2]\n\t" \
    "v_cndmask_b32_e64 v166, v110, v86, %[m2]\n\t" \
    "v_mfma_f32_16x16x32_bf16 v[120:123], v[148:151], v[96:99], v[120:123]\n\t" \
    "ds_read_u16_d16_hi v168, %[ua2] offset:960\n\t" \
    "ds_read_u16_d16_hi v169, %[ua2] offset:992\n\t" \
    "v_mfma_f32_16x16x32_bf16 v[116:119], v[152:155], v[44:47], v[116:119]\n\t" \
    "v_cndmask_b32_e64 v161, v85, v105, %[m1]\n\t" \
    "v_cndmask_b32_e64 v165, v109, v85, %[m1]\n\t" \
    "v_mfma_f32_16x16x32_bf16 v[120:123], v[112:115], v[44:47], v[120:123]\n\t" \
    "v_add_f32_dpp v162, v163, v162 wave_ror:1 row_mask:0xf bank_mask:0xf\n\t" \
    "v_add_f32_dpp v166, v167, v166 wave_ror:1 row_mask:0xf bank_mask:0xf\n\t" \
    "s_waitcnt lgkmcnt(3)\n\t" \
    "v_mfma_f32_16x16x32_bf16 v[116:119], v[0:3], v[48:51], v[116:119]\n\t" \
    "v_fmac_f32 v84, %[dd2], v174\n\t" \
    "v_mfma_f32_16x16x32_bf16 v[120:123], v[8:11], v[48:51], v[120:123]\n\t" \
    "v_fmac_f32 v108, %[dd2], v175\n\t" \
    "L_cv5_15:\n\t" \
    "s_cbranch_execz L_cv5_16\n\t" \
    "v_add_f32_dpp v161, v162, v161 wave_ror:1 row_mask:0xf bank_mask:0xf\n\t" \
    "v_add_f32_dpp v165, v166, v165 wave_ror:1 row_mask:0xf bank_mask:0xf\n\t" \
    "s_nop 0\n\t" \
    "v_add_f32_dpp v84, v161, v84 wave_ror:1 row_mask:0xf bank_mask:0xf\n\t" \
    "v_add_f32_dpp v108, v165, v108 wave_ror:1 row_mask:0xf bank_mask:0xf\n\t" \
    "v_cvt_pk_bf16_f32 v160, v84, v108\n\t" \
    "ds_write_b16 %[ya], v160 offset:128\n\t" \
    "ds_write_b16_d16_hi %[ya], v160 offset:256\n\t" \
    "v_cndmask_b32_e64 v163, v119, v111, %[m3]\n\t" \
    "v_cndmask_b32_e64 v167, v123, v119, %[m3]\n\t" \
    "v_cndmask_b32_e64 v162, v118, v110, %[m2]\n\t" \
    "v_cndmask_b32_e64 v166, v122, v118, %[m2]\n\t" \
    "v_cndmask_b32_e64 v161, v117, v109, %[m1]\n\t" \
    "v_cndmask_b32_e64 v165, v121, v117, %[m1]\n\t" \
    "v_add_f32_dpp v162, v163, v162 wave_ror:1 row_mask:0xf bank_mask:0xf\n\t" \
    "v_add_f32_dpp v166, v167, v166 wave_ror:1 row_mask:0xf bank_mask:0xf\n\t" \
    "s_waitcnt lgkmcnt(2)\n\t" \
    "v_fmac_f32 v116, %[dd2], v168\n\t" \
    "v_fmac_f32 v120, %[dd2], v169\n\t" \
    "v_add_f32_dpp v161, v162, v161 wave_ror:1 row_mask:0xf bank_mask:0xf\n\t" \
    "v_add_f32_dpp v165, v166, v165 wave_ror:1 row_mask:0xf bank_mask:0xf\n\t" \
    "s_nop 0\n\t" \
    "v_add_f32_dpp v116, v161, v116 wave_ror:1 row_mask:0xf bank_mask:0xf\n\t" \
    "v_add_f32_dpp v120, v165, v120 wave_ror:1 row_mask:0xf bank_mask:0xf\n\t" \
    "v_cvt_pk_bf16_f32 v160, v116, v120\n\t" \
    "ds_write_b16 %[ya], v160 offset:384\n\t" \
    "ds_write_b16_d16_hi %[ya], v160 offset:512\n\t" \
    "ds_read_b128 v[180:183], %[yr]\n\t" \
    "s_waitcnt vmcnt(0)\n\t" \
    "s_mov_b32 exec_hi, 0xff\n\t" \
    "buffer_store_dwordx4 v[176:179], %[l16], %[rs], %[yo] offen offset:2944 sc1\n\t" \
    "s_mov_b32 exec_hi, -1\n\t" \
    "s_waitcnt lgkmcnt(0)\n\t" \
    "s_mov_b32 exec_hi, 0xff\n\t" \
    "L_cv5_16:\n\t" \
    "s_cbranch_execz L_cv5_17\n\t" \
    "buffer_store_dwordx4 v[180:183], %[l16], %[rs], %[yo2] offen offset:3456 sc1\n\t" \
    "s_mov_b32 exec_hi, -1\n\t" \
    "s_nop 1\n\t" \
    "L_cv5_17:\n\t" \
    "s_cbranch_execz L_back\n\t" \
    "s_branch CVE_%=\n\t" \
    "L_cv6_0:\n\t" \
    "s_cbranch_execz L_cv6_1\n\t" \
    "ds_read_b64 v[0:1], %[ub] offset:32\n\t" \
    "ds_read_b64 v[2:3], %[ub] offset:40\n\t" \
    "ds_read_b64_tr_b16 v[4:5], %[hb] offset:3360\n\t" \
    "ds_read_b64_tr_b16 v[6:7], %[hb] offset:3328\n\t" \
    "ds_read_b64 v[8:9], %[ub] offset:0\n\t" \
    "ds_read_b64 v[10:11], %[ub] offset:8\n\t" \
    "ds_read_b64_tr_b16 v[12:13], %[hb] offset:3616\n\t" \
    "ds_read_b64_tr_b16 v[14:15], %[hb] offset:3584\n\t" \
    "ds_read_b64_tr_b16 v[16:17], %[hb] offset:3872\n\t" \
    "ds_read_b64_tr_b16 v[18:19], %[hb] offset:3840\n\t" \
    "ds_read_b64 v[20:21], %[ub] offset:96\n\t" \
    "ds_read_b64 v[22:23], %[ub] offset:104\n\t" \
    "v_mov_b32 v168, 0\n\t" \
    "v_mov_b32 v169, 0\n\t" \
    "v_mov_b32 v170, 0\n\t" \
    "v_mov_b32 v171, 0\n\t" \
    "v_mov_b32 v172, 0\n\t" \
    "v_mov_b32 v173, 0\n\t" \
    "v_mov_b32 v174, 0\n\t" \
    "v_mov_b32 v175, 0\n\t" \
    "s_waitcnt lgkmcnt(8)\n\t" \
    "v_mfma_f32_16x16x32_bf16 v[24:27], v[0:3], v[4:7], 0\n\t" \
    "ds_read_b64_tr_b16 v[28:29], %[hb] offset:3104\n\t" \
    "ds_read_b64_tr_b16 v[30:31], %[hb] offset:3072\n\t" \
    "ds_read_b64 v[32:33], %[ub] offset:64\n\t" \
    "s_waitcnt lgkmcnt(7)\n\t" \
    "v_mfma_f32_16x16x32_bf16 v[36:39], v[8:11], v[12:15], 0\n\t" \
    "ds_read_b64 v[34:35], %[ub] offset:72\n\t" \
    "ds_read_b64 v[40:41], %[ub] offset:160\n\t" \
    "ds_read_b64 v[42:43], %[ub] offset:168\n\t" \
    "v_mfma_f32_16x16x32_bf16 v[44:47], v[0:3], v[12:15], 0\n\t" \
    "ds_read_b64_tr_b16 v[48:49], %[hb] offset:2848\n\t" \
    "L_cv6_1:\n\t" \
    "s_cbranch_execz L_cv6_2\n\t" \
    "ds_read_b64_tr_b16 v[50:51], %[hb] offset:2816\n\t" \
    "ds_read_b64 v[52:53], %[ub] offset:128\n\t" \
    "s_waitcnt lgkmcnt(11)\n\t" \
    "v_mfma_f32_16x16x32_bf16 v[56:59], v[8:11], v[16:19], 0\n\t" \
    "ds_read_b64 v[54:55], %[ub] offset:136\n\t" \
    "ds_read_b64 v[60:61], %[ub] offset:224\n\t" \
    "ds_read_b64 v[62:63], %[ub] offset:232\n\t" \
    "v_mfma_f32_16x16x32_bf16 v[64:67], v[0:3], v[16:19], 0\n\t" \
    "ds_read_b64_tr_b16 v[68:69], %[hb] offset:2592\n\t" \
    "ds_read_b64_tr_b16 v[70:71], %[hb] offset:2560\n\t" \
    "s_waitcnt lgkmcnt(12)\n\t" \
    "v_mfma_f32_16x16x32_bf16 v[24:27], v[20:23], v[28:31], v[24:27]\n\t" \
    "ds_read_b64 v[72:73], %[ub] offset:192\n\t" \
    "s_waitcnt lgkmcnt(11)\n\t" \
    "v_mfma_f32_16x16x32_bf16 v[36:39], v[32:35], v[4:7], v[36:39]\n\t" \
    "ds_read_b64 v[74:75], %[ub] offset:200\n\t" \
    "v_mfma_f32_16x16x32_bf16 v[44:47], v[20:23], v[4:7], v[44:47]\n\t" \
    "v_mfma_f32_16x16x32_bf16 v[56:59], v[32:35], v[12:15], v[56:59]\n\t" \
    "ds_read_b64 v[76:77], %[ub] offset:288\n\t" \
    "ds_read_b64 v[78:79], %[ub] offset:296\n\t" \
    "v_mfma_f32_16x16x32_bf16 v[64:67], v[20:23], v[12:15], v[64:67]\n\t" \
    "ds_read_b64_tr_b16 v[80:81], %[hb] offset:2336\n\t" \
    "ds_read_b64_tr_b16 v[82:83], %[hb] offset:2304\n\t" \
    "s_waitcnt lgkmcnt(10)\n\t" \
    "v_mfma_f32_16x16x32_bf16 v[24:27], v[40:43], v[48:51], v[24:27]\n\t" \
    "ds_read_b64 v[84:85], %[ub] offset:256\n\t" \
    "v_mfma_f32_16x16x32_bf16 v[36:39], v[52:55], v[28:31], v[36:39]\n\t" \
    "ds_read_b64 v[86:87], %[ub] offset:264\n\t" \
    "v_mfma_f32_16x16x32_bf16 v[44:47], v[40:43], v[28:31], v[44:47]\n\t" \
    "L_cv6_2:\n\t" \
    "s_cbranch_execz L_cv6_3\n\t" \
    "v_mfma_f32_16x16x32_bf16 v[56:59], v[52:55], v[4:7], v[56:59]\n\t" \
    "ds_read_b64 v[88:89], %[ub] offset:352\n\t" \
    "ds_read_b64 v[90:91], %[ub] offset:360\n\t" \
    "v_mfma_f32_16x16x32_bf16 v[64:67], v[40:43], v[4:7], v[64:67]\n\t" \
    "ds_read_b64_tr_b16 v[92:93], %[hb] offset:2080\n\t" \
    "ds_read_b64_tr_b16 v[94:95], %[hb] offset:2048\n\t" \
    "s_waitcnt lgkmcnt(10)\n\t" \
    "v_mfma_f32_16x16x32_bf16 v[24:27], v[60:63], v[68:71], v[24:27]\n\t" \
    "ds_read_b64 v[96:97], %[ub] offset:320\n\t" \
    "v_mfma_f32_16x16x32_bf16 v[36:39], v[72:75], v[48:51], v[36:39]\n\t" \
    "ds_read_b64 v[98:99], %[ub] offset:328\n\t" \
    "v_mfma_f32_16x16x32_bf16 v[44:47], v[60:63], v[48:51], v[44:47]\n\t" \
    "v_mfma_f32_16x16x32_bf16 v[56:59], v[72:75], v[28:31], v[56:59]\n\t" \
    "ds_read_b64 v[100:101], %[ub] offset:416\n\t" \
    "ds_read_b64 v[102:103], %[ub] offset:424\n\t" \
    "v_mfma_f32_16x16x32_bf16 v[64:67], v[60:63], v[28:31], v[64:67]\n\t" \
    "ds_read_b64_tr_b16 v[104:105], %[hb] offset:1824\n\t" \
    "ds_read_b64_tr_b16 v[106:107], %[hb] offset:1792\n\t" \
    "s_waitcnt lgkmcnt(10)\n\t" \
    "v_mfma_f32_16x16x32_bf16 v[24:27], v[76:79], v[80:83], v[24:27]\n\t" \
    "ds_read_b64 v[108:109], %[ub] offset:384\n\t" \
    "v_mfma_f32_16x16x32_bf16 v[36:39], v[84:87], v[68:71], v[36:39]\n\t" \
    "ds_read_b64 v[110:111], %[ub] offset:392\n\t" \
    "v_mfma_f32_16x16x32_bf16 v[44:47], v[76:79], v[68:71], v[44:47]\n\t" \
    "v_mfma_f32_16x16x32_bf16 v[56:59], v[84:87], v[48:51], v[56:59]\n\t" \
    "ds_read_b64 v[112:113], %[ub] offset:480\n\t" \
    "ds_read_b64 v[114:115], %[ub] offset:488\n\t" \
    "v_mfma_f32_16x16x32_bf16 v[64:67], v[76:79], v[48:51], v[64:67]\n\t" \
    "ds_read_b64_tr_b16 v[116:117], %[hb] offset:1568\n\t" \
    "ds_read_b64_tr_b16 v[118:119], %[hb] offset:1536\n\t" \
    "s_waitcnt lgkmcnt(10)\n\t" \
    "v_mfma_f32_16x16x32_bf16 v[24:27], v[88:91], v[92:95], v[24:27]\n\t" \
    "ds_read_b64 v[120:121], %[ub] offset:448\n\t" \
    "L_cv6_3:\n\t" \
    "s_cbranch_execz L_cv6_4\n\t" \
    "v_mfma_f32_16x16x32_bf16 v[36:39], v[96:99], v[80:83], v[36:39]\n\t" \
    "ds_read_b64 v[122:123], %[ub] offset:456\n\t" \
    "v_mfma_f32_16x16x32_bf16 v[44:47], v[88:91], v[80:83], v[44:47]\n\t" \
    "v_mfma_f32_16x16x32_bf16 v[56:59], v[96:99], v[68:71], v[56:59]\n\t" \
    "ds_read_b64 v[124:125], %[ub] offset:544\n\t" \
    "ds_read_b64 v[126:127], %[ub] offset:552\n\t" \
    "v_mfma_f32_16x16x32_bf16 v[64:67], v[88:91], v[68:71], v[64:67]\n\t" \
    "ds_read_b64_tr_b16 v[128:129], %[hb] offset:1312\n\t" \
    "ds_read_b64_tr_b16 v[130:131], %[hb] offset:1280\n\t" \
    "s_waitcnt lgkmcnt(10)\n\t" \
    "v_mfma_f32_16x16x32_bf16 v[24:27], v[100:103], v[104:107], v[24:27]\n\t" \
    "ds_read_b64 v[132:133], %[ub] offset:512\n\t" \
    "v_mfma_f32_16x16x32_bf16 v[36:39], v[108:111], v[92:95], v[36:39]\n\t" \
    "ds_read_b64 v[134:135], %[ub] offset:520\n\t" \
    "v_mfma_f32_16x16x32_bf16 v[44:47], v[100:103], v[92:95], v[44:47]\n\t" \
    "v_mfma_f32_16x16x32_bf16 v[56:59], v[108:111], v[80:83], v[56:59]\n\t" \
    "ds_read_b64 v[136:137], %[ub] offset:576\n\t" \
    "ds_read_b64 v[138:139], %[ub] offset:584\n\t" \
    "v_mfma_f32_16x16x32_bf16 v[64:67], v[100:103], v[80:83], v[64:67]\n\t" \
    "ds_read_b64 v[140:141], %[ub] offset:608\n\t" \
    "ds_read_b64 v[142:143], %[ub] offset:616\n\t" \
    "s_waitcnt lgkmcnt(10)\n\t" \
    "v_mfma_f32_16x16x32_bf16 v[24:27], v[112:115], v[116:119], v[24:27]\n\t" \
    "v_mfma_f32_16x16x32_bf16 v[36:39], v[120:123], v[104:107], v[36:39]\n\t" \
    "v_mfma_f32_16x16x32_bf16 v[44:47], v[112:115], v[104:107], v[44:47]\n\t" \
    "ds_read_b64 v[144:145], %[ub] offset:640\n\t" \
    "ds_read_b64 v[146:147], %[ub] offset:648\n\t" \
    "v_mfma_f32_16x16x32_bf16 v[56:59], v[120:123], v[92:95], v[56:59]\n\t" \
    "ds_read_b64 v[148:149], %[ub] offset:672\n\t" \
    "ds_read_b64 v[150:151], %[ub] offset:680\n\t" \
    "v_mfma_f32_16x16x32_bf16 v[64:67], v[112:115], v[92:95], v[64:67]\n\t" \
    "ds_read_b64_tr_b16 v[152:153], %[hb] offset:1056\n\t" \
    "L_cv6_4:\n\t" \
    "s_cbranch_execz L_cv6_5\n\t" \
    "ds_read_b64_tr_b16 v[154:155], %[hb] offset:1024\n\t" \
    "s_waitcnt lgkmcnt(10)\n\t" \
    "v_mfma_f32_16x16x32_bf16 v[24:27], v[124:127], v[128:131], v[24:27]\n\t" \
    "ds_read_b64_tr_b16 v[156:157], %[hb] offset:800\n\t" \
    "v_mfma_f32_16x16x32_bf16 v[36:39], v[132:135], v[116:119], v[36:39]\n\t" \
    "ds_read_b64_tr_b16 v[158:159], %[hb] offset:768\n\t" \
    "ds_read_b64 v[8:9], %[ub] offset:736\n\t" \
    "v_mfma_f32_16x16x32_bf16 v[44:47], v[124:127], v[116:119], v[44:47]\n\t" \
    "ds_read_b64 v[10:11], %[ub] offset:744\n\t" \
    "ds_read_b64_tr_b16 v[0:1], %[hb] offset:544\n\t" \
    "v_mfma_f32_16x16x32_bf16 v[56:59], v[132:135], v[104:107], v[56:59]\n\t" \
    "ds_read_b64_tr_b16 v[2:3], %[hb] offset:512\n\t" \
    "ds_read_b64 v[16:17], %[ub] offset:800\n\t" \
    "v_mfma_f32_16x16x32_bf16 v[64:67], v[124:127], v[104:107], v[64:67]\n\t" \
    "ds_read_b64 v[18:19], %[ub] offset:808\n\t" \
    "ds_read_b64_tr_b16 v[32:33], %[hb] offset:288\n\t" \
    "s_waitcnt lgkmcnt(14)\n\t" \
    "v_mfma_f32_16x16x32_bf16 v[36:39], v[136:139], v[128:131], v[36:39]\n\t" \
    "ds_read_b64_tr_b16 v[34:35], %[hb] offset:256\n\t" \
    "v_mfma_f32_16x16x32_bf16 v[44:47], v[140:143], v[128:131], v[44:47]\n\t" \
    "ds_read_b64 v[20:21], %[ub] offset:864\n\t" \
    "ds_read_b64 v[22:23], %[ub] offset:872\n\t" \
    "v_mfma_f32_16x16x32_bf16 v[56:59], v[136:139], v[116:119], v[56:59]\n\t" \
    "ds_read_b64_tr_b16 v[12:13], %[hb] offset:32\n\t" \
    "ds_read_b64_tr_b16 v[14:15], %[hb] offset:0\n\t" \
    "ds_read_b64 v[52:53], %[ub] offset:704\n\t" \
    "v_mfma_f32_16x16x32_bf16 v[64:67], v[140:143], v[116:119], v[64:67]\n\t" \
    "ds_read_b64 v[54:55], %[ub] offset:712\n\t" \
    "ds_read_b64 v[40:41], %[ub] offset:768\n\t" \
    "s_waitcnt lgkmcnt(14)\n\t" \
    "v_mfma_f32_16x16x32_bf16 v[56:59], v[144:147], v[128:131], v[56:59]\n\t" \
    "ds_read_b64 v[42:43], %[ub] offset:776\n\t" \
    "v_mfma_f32_16x16x32_bf16 v[64:67], v[148:151], v[128:131], v[64:67]\n\t" \
    "L_cv6_5:\n\t" \
    "s_cbranch_execz L_cv6_6\n\t" \
    "ds_read_b64 v[4:5], %[ub] offset:832\n\t" \
    "ds_read_b64 v[6:7], %[ub] offset:840\n\t" \
    "v_mfma_f32_16x16x32_bf16 v[24:27], v[140:143], v[152:155], v[24:27]\n\t" \
    "v_mfma_f32_16x16x32_bf16 v[24:27], v[148:151], v[156:159], v[24:27]\n\t" \
    "ds_read_b64 v[72:73], %[ub] offset:896\n\t" \
    "ds_read_b64 v[74:75], %[ub] offset:904\n\t" \
    "s_waitcnt lgkmcnt(12)\n\t" \
    "v_mfma_f32_16x16x32_bf16 v[24:27], v[8:11], v[0:3], v[24:27]\n\t" \
    "ds_read_b64 v[60:61], %[ub] offset:928\n\t" \
    "v_mfma_f32_16x16x32_bf16 v[24:27], v[16:19], v[32:35], v[24:27]\n\t" \
    "ds_read_b64 v[62:63], %[ub] offset:936\n\t" \
    "s_waitcnt lgkmcnt(10)\n\t" \
    "v_mfma_f32_16x16x32_bf16 v[24:27], v[20:23], v[12:15], v[24:27]\n\t" \
    "v_mfma_f32_16x16x32_bf16 v[36:39], v[144:147], v[152:155], v[36:39]\n\t" \
    "v_mfma_f32_16x16x32_bf16 v[44:47], v[148:151], v[152:155], v[44:47]\n\t" \
    "s_waitcnt lgkmcnt(8)\n\t" \
    "v_mfma_f32_16x16x32_bf16 v[36:39], v[52:55], v[156:159], v[36:39]\n\t" \
    "v_mfma_f32_16x16x32_bf16 v[44:47], v[8:11], v[156:159], v[44:47]\n\t" \
    "s_waitcnt lgkmcnt(6)\n\t" \
    "v_mfma_f32_16x16x32_bf16 v[36:39], v[40:43], v[0:3], v[36:39]\n\t" \
    "v_mfma_f32_16x16x32_bf16 v[44:47], v[16:19], v[0:3], v[44:47]\n\t" \
    "ds_read_u16_d16_hi v168, %[ua] offset:896\n\t" \
    "ds_read_u16_d16_hi v169, %[ua] offset:928\n\t" \
    "s_waitcnt lgkmcnt(6)\n\t" \
    "v_mfma_f32_16x16x32_bf16 v[36:39], v[4:7], v[32:35], v[36:39]\n\t" \
    "ds_read_b64 v[28:29], %[ub] offset:960\n\t" \
    "v_mfma_f32_16x16x32_bf16 v[44:47], v[20:23], v[32:35], v[44:47]\n\t" \
    "ds_read_b64 v[30:31], %[ub] offset:968\n\t" \
    "ds_read_b64 v[84:85], %[ub] offset:992\n\t" \
    "s_waitcnt lgkmcnt(5)\n\t" \
    "v_mfma_f32_16x16x32_bf16 v[36:39], v[72:75], v[12:15], v[36:39]\n\t" \
    "ds_read_b64 v[86:87], %[ub] offset:1000\n\t" \
    "v_mfma_f32_16x16x32_bf16 v[44:47], v[60:63], v[12:15], v[44:47]\n\t" \
    "ds_read_b64 v[76:77], %[ub2] offset:0\n\t" \
    "L_cv6_6:\n\t" \
    "s_cbranch_execz L_cv6_7\n\t" \
    "ds_read_b64 v[78:79], %[ub2] offset:8\n\t" \
    "v_mfma_f32_16x16x32_bf16 v[56:59], v[52:55], v[152:155], v[56:59]\n\t" \
    "ds_read_b64_tr_b16 v[48:49], %[hb2] offset:3360\n\t" \
    "ds_read_b64_tr_b16 v[50:51], %[hb2] offset:3328\n\t" \
    "v_mfma_f32_16x16x32_bf16 v[64:67], v[8:11], v[152:155], v[64:67]\n\t" \
    "ds_read_b64 v[96:97], %[ub2] offset:32\n\t" \
    "ds_read_b64 v[98:99], %[ub2] offset:40\n\t" \
    "v_mfma_f32_16x16x32_bf16 v[56:59], v[40:43], v[156:159], v[56:59]\n\t" \
    "ds_read_b64_tr_b16 v[88:89], %[hb2] offset:3616\n\t" \
    "ds_read_b64_tr_b16 v[90:91], %[hb2] offset:3584\n\t" \
    "v_mfma_f32_16x16x32_bf16 v[64:67], v[16:19], v[156:159], v[64:67]\n\t" \
    "ds_read_b64_tr_b16 v[68:69], %[hb2] offset:3872\n\t" \
    "ds_read_b64_tr_b16 v[70:71], %[hb2] offset:3840\n\t" \
    "v_mfma_f32_16x16x32_bf16 v[56:59], v[4:7], v[0:3], v[56:59]\n\t" \
    "ds_read_b64 v[108:109], %[ub2] offset:64\n\t" \
    "ds_read_b64 v[110:111], %[ub2] offset:72\n\t" \
    "v_mfma_f32_16x16x32_bf16 v[64:67], v[20:23], v[0:3], v[64:67]\n\t" \
    "ds_read_b64_tr_b16 v[100:101], %[hb2] offset:3104\n\t" \
    "ds_read_b64_tr_b16 v[102:103], %[hb2] offset:3072\n\t" \
    "v_mfma_f32_16x16x32_bf16 v[56:59], v[72:75], v[32:35], v[56:59]\n\t" \
    "ds_read_b64 v[80:81], %[ub2] offset:96\n\t" \
    "ds_read_b64 v[82:83], %[ub2] offset:104\n\t" \
    "v_mfma_f32_16x16x32_bf16 v[64:67], v[60:63], v[32:35], v[64:67]\n\t" \
    "ds_read_u16_d16_hi v170, %[ua] offset:960\n\t" \
    "ds_read_u16_d16_hi v171, %[ua] offset:992\n\t" \
    "s_waitcnt lgkmcnt(14)\n\t" \
    "v_mfma_f32_16x16x32_bf16 v[56:59], v[28:31], v[12:15], v[56:59]\n\t" \
    "v_cndmask_b32_e64 v163, v39, v27, %[m3]\n\t" \
    "v_mfma_f32_16x16x32_bf16 v[64:67], v[84:87], v[12:15], v[64:67]\n\t" \
    "v_cndmask_b32_e64 v167, v47, v39, %[m3]\n\t" \
    "v_cndmask_b32_e64 v162, v38, v26, %[m2]\n\t" \
    "v_mfma_f32_16x16x32_bf16 v[120:123], v[76:79], v[48:51], 0\n\t" \
    "L_cv6_7:\n\t" \
    "s_cbranch_execz L_cv6_8\n\t" \
    "ds_read_b64 v[112:113], %[ub2] offset:128\n\t" \
    "ds_read_b64 v[114:115], %[ub2] offset:136\n\t" \
    "s_waitcnt lgkmcnt(12)\n\t" \
    "v_mfma_f32_16x16x32_bf16 v[92:95], v[96:99], v[48:51], 0\n\t" \
    "ds_read_b64_tr_b16 v[132:133], %[hb2] offset:2848\n\t" \
    "v_mfma_f32_16x16x32_bf16 v[124:127], v[76:79], v[88:91], 0\n\t" \
    "ds_read_b64_tr_b16 v[134:135], %[hb2] offset:2816\n\t" \
    "ds_read_b64 v[104:105], %[ub2] offset:160\n\t" \
    "v_mfma_f32_16x16x32_bf16 v[136:139], v[96:99], v[88:91], 0\n\t" \
    "ds_read_b64 v[106:107], %[ub2] offset:168\n\t" \
    "v_cndmask_b32_e64 v166, v46, v38, %[m2]\n\t" \
    "s_waitcnt lgkmcnt(14)\n\t" \
    "v_mfma_f32_16x16x32_bf16 v[116:119], v[76:79], v[68:71], 0\n\t" \
    "v_cndmask_b32_e64 v161, v37, v25, %[m1]\n\t" \
    "v_mfma_f32_16x16x32_bf16 v[128:131], v[96:99], v[68:71], 0\n\t" \
    "v_cndmask_b32_e64 v165, v45, v37, %[m1]\n\t" \
    "v_add_f32_dpp v162, v163, v162 wave_ror:1 row_mask:0xf bank_mask:0xf\n\t" \
    "s_waitcnt lgkmcnt(8)\n\t" \
    "v_mfma_f32_16x16x32_bf16 v[120:123], v[108:111], v[100:103], v[120:123]\n\t" \
    "ds_read_b64 v[140:141], %[ub2] offset:192\n\t" \
    "v_mfma_f32_16x16x32_bf16 v[92:95], v[80:83], v[100:103], v[92:95]\n\t" \
    "ds_read_b64 v[142:143], %[ub2] offset:200\n\t" \
    "ds_read_b64_tr_b16 v[144:145], %[hb2] offset:2592\n\t" \
    "v_mfma_f32_16x16x32_bf16 v[124:127], v[108:111], v[48:51], v[124:127]\n\t" \
    "ds_read_b64_tr_b16 v[146:147], %[hb2] offset:2560\n\t" \
    "ds_read_b64 v[148:149], %[ub2] offset:224\n\t" \
    "v_mfma_f32_16x16x32_bf16 v[136:139], v[80:83], v[48:51], v[136:139]\n\t" \
    "ds_read_b64 v[150:151], %[ub2] offset:232\n\t" \
    "v_add_f32_dpp v166, v167, v166 wave_ror:1 row_mask:0xf bank_mask:0xf\n\t" \
    "v_mfma_f32_16x16x32_bf16 v[116:119], v[108:111], v[88:91], v[116:119]\n\t" \
    "v_fmac_f32 v36, %[dd], v168\n\t" \
    "v_fmac_f32 v44, %[dd], v169\n\t" \
    "v_mfma_f32_16x16x32_bf16 v[128:131], v[80:83], v[88:91], v[128:131]\n\t" \
    "L_cv6_8:\n\t" \
    "s_cbranch_execz L_cv6_9\n\t" \
    "v_add_f32_dpp v161, v162, v161 wave_ror:1 row_mask:0xf bank_mask:0xf\n\t" \
    "v_add_f32_dpp v165, v166, v165 wave_ror:1 row_mask:0xf bank_mask:0xf\n\t" \
    "s_waitcnt lgkmcnt(6)\n\t" \
    "v_mfma_f32_16x16x32_bf16 v[120:123], v[112:115], v[132:135], v[120:123]\n\t" \
    "ds_read_b64 v[52:53], %[ub2] offset:256\n\t" \
    "v_mfma_f32_16x16x32_bf16 v[92:95], v[104:107], v[132:135], v[92:95]\n\t" \
    "ds_read_b64 v[54:55], %[ub2] offset:264\n\t" \
    "ds_read_b64_tr_b16 v[8:9], %[hb2] offset:2336\n\t" \
    "v_mfma_f32_16x16x32_bf16 v[124:127], v[112:115], v[100:103], v[124:127]\n\t" \
    "ds_read_b64_tr_b16 v[10:11], %[hb2] offset:2304\n\t" \
    "ds_read_b64 v[152:153], %[ub2] offset:288\n\t" \
    "v_mfma_f32_16x16x32_bf16 v[136:139], v[104:107], v[100:103], v[136:139]\n\t" \
    "ds_read_b64 v[154:155], %[ub2] offset:296\n\t" \
    "v_add_f32_dpp v36, v161, v36 wave_ror:1 row_mask:0xf bank_mask:0xf\n\t" \
    "v_mfma_f32_16x16x32_bf16 v[116:119], v[112:115], v[48:51], v[116:119]\n\t" \
    "v_add_f32_dpp v44, v165, v44 wave_ror:1 row_mask:0xf bank_mask:0xf\n\t" \
    "v_cvt_pk_bf16_f32 v160, v36, v44\n\t" \
    "v_mfma_f32_16x16x32_bf16 v[128:131], v[104:107], v[48:51], v[128:131]\n\t" \
    "ds_write_b16 %[ya], v160 offset:0\n\t" \
    "ds_write_b16_d16_hi %[ya], v160 offset:128\n\t" \
    "s_waitcnt lgkmcnt(8)\n\t" \
    "v_mfma_f32_16x16x32_bf16 v[120:123], v[140:143], v[144:147], v[120:123]\n\t" \
    "ds_read_b64 v[40:41], %[ub2] offset:320\n\t" \
    "v_mfma_f32_16x16x32_bf16 v[92:95], v[148:151], v[144:147], v[92:95]\n\t" \
    "ds_read_b64 v[42:43], %[ub2] offset:328\n\t" \
    "ds_read_b64_tr_b16 v[16:17], %[hb2] offset:2080\n\t" \
    "v_mfma_f32_16x16x32_bf16 v[124:127], v[140:143], v[132:135], v[124:127]\n\t" \
    "ds_read_b64_tr_b16 v[18:19], %[hb2] offset:2048\n\t" \
    "ds_read_b64 v[156:157], %[ub2] offset:352\n\t" \
    "v_mfma_f32_16x16x32_bf16 v[136:139], v[148:151], v[132:135], v[136:139]\n\t" \
    "ds_read_b64 v[158:159], %[ub2] offset:360\n\t" \
    "v_cndmask_b32_e64 v163, v59, v47, %[m3]\n\t" \
    "L_cv6_9:\n\t" \
    "s_cbranch_execz L_cv6_10\n\t" \
    "v_mfma_f32_16x16x32_bf16 v[116:119], v[140:143], v[100:103], v[116:119]\n\t" \
    "v_cndmask_b32_e64 v167, v67, v59, %[m3]\n\t" \
    "v_cndmask_b32_e64 v162, v58, v46, %[m2]\n\t" \
    "v_mfma_f32_16x16x32_bf16 v[128:131], v[148:151], v[100:103], v[128:131]\n\t" \
    "v_cndmask_b32_e64 v166, v66, v58, %[m2]\n\t" \
    "v_cndmask_b32_e64 v161, v57, v45, %[m1]\n\t" \
    "s_waitcnt lgkmcnt(8)\n\t" \
    "v_mfma_f32_16x16x32_bf16 v[120:123], v[52:55], v[8:11], v[120:123]\n\t" \
    "ds_read_b64 v[4:5], %[ub2] offset:384\n\t" \
    "v_mfma_f32_16x16x32_bf16 v[92:95], v[152:155], v[8:11], v[92:95]\n\t" \
    "ds_read_b64 v[6:7], %[ub2] offset:392\n\t" \
    "ds_read_b64_tr_b16 v[20:21], %[hb2] offset:1824\n\t" \
    "v_mfma_f32_16x16x32_bf16 v[124:127], v[52:55], v[144:147], v[124:127]\n\t" \
    "ds_read_b64_tr_b16 v[22:23], %[hb2] offset:1792\n\t" \
    "ds_read_b64 v[0:1], %[ub2] offset:416\n\t" \
    "v_mfma_f32_16x16x32_bf16 v[136:139], v[152:155], v[144:147], v[136:139]\n\t" \
    "ds_read_b64 v[2:3], %[ub2] offset:424\n\t" \
    "v_cndmask_b32_e64 v165, v65, v57, %[m1]\n\t" \
    "v_mfma_f32_16x16x32_bf16 v[116:119], v[52:55], v[132:135], v[116:119]\n\t" \
    "v_add_f32_dpp v162, v163, v162 wave_ror:1 row_mask:0xf bank_mask:0xf\n\t" \
    "v_add_f32_dpp v166, v167, v166 wave_ror:1 row_mask:0xf bank_mask:0xf\n\t" \
    "v_mfma_f32_16x16x32_bf16 v[128:131], v[152:155], v[132:135], v[128:131]\n\t" \
    "v_fmac_f32 v56, %[dd], v170\n\t" \
    "v_fmac_f32 v64, %[dd], v171\n\t" \
    "s_waitcnt lgkmcnt(6)\n\t" \
    "v_mfma_f32_16x16x32_bf16 v[120:123], v[40:43], v[16:19], v[120:123]\n\t" \
    "ds_read_b64 v[72:73], %[ub2] offset:448\n\t" \
    "v_mfma_f32_16x16x32_bf16 v[92:95], v[156:159], v[16:19], v[92:95]\n\t" \
    "ds_read_b64 v[74:75], %[ub2] offset:456\n\t" \
    "ds_read_b64_tr_b16 v[60:61], %[hb2] offset:1568\n\t" \
    "v_mfma_f32_16x16x32_bf16 v[124:127], v[40:43], v[8:11], v[124:127]\n\t" \
    "ds_read_b64_tr_b16 v[62:63], %[hb2] offset:1536\n\t" \
    "L_cv6_10:\n\t" \
    "s_cbranch_execz L_cv6_11\n\t" \
    "ds_read_b64 v[32:33], %[ub2] offset:480\n\t" \
    "v_mfma_f32_16x16x32_bf16 v[136:139], v[156:159], v[8:11], v[136:139]\n\t" \
    "ds_read_b64 v[34:35], %[ub2] offset:488\n\t" \
    "v_add_f32_dpp v161, v162, v161 wave_ror:1 row_mask:0xf bank_mask:0xf\n\t" \
    "v_mfma_f32_16x16x32_bf16 v[116:119], v[40:43], v[144:147], v[116:119]\n\t" \
    "v_add_f32_dpp v165, v166, v165 wave_ror:1 row_mask:0xf bank_mask:0xf\n\t" \
    "v_add_f32_dpp v56, v161, v56 wave_ror:1 row_mask:0xf bank_mask:0xf\n\t" \
    "v_mfma_f32_16x16x32_bf16 v[128:131], v[156:159], v[144:147], v[128:131]\n\t" \
    "v_add_f32_dpp v64, v165, v64 wave_ror:1 row_mask:0xf bank_mask:0xf\n\t" \
    "v_cvt_pk_bf16_f32 v160, v56, v64\n\t" \
    "s_waitcnt lgkmcnt(6)\n\t" \
    "v_mfma_f32_16x16x32_bf16 v[120:123], v[4:7], v[20:23], v[120:123]\n\t" \
    "ds_read_b64 v[28:29], %[ub2] offset:512\n\t" \
    "v_mfma_f32_16x16x32_bf16 v[92:95], v[0:3], v[20:23], v[92:95]\n\t" \
    "ds_read_b64 v[30:31], %[ub2] offset:520\n\t" \
    "ds_read_b64_tr_b16 v[84:85], %[hb2] offset:1312\n\t" \
    "v_mfma_f32_16x16x32_bf16 v[124:127], v[4:7], v[16:19], v[124:127]\n\t" \
    "ds_read_b64_tr_b16 v[86:87], %[hb2] offset:1280\n\t" \
    "ds_read_b64 v[12:13], %[ub2] offset:544\n\t" \
    "v_mfma_f32_16x16x32_bf16 v[136:139], v[0:3], v[16:19], v[136:139]\n\t" \
    "ds_read_b64 v[14:15], %[ub2] offset:552\n\t" \
    "ds_write_b16 %[ya], v160 offset:256\n\t" \
    "v_mfma_f32_16x16x32_bf16 v[116:119], v[4:7], v[8:11], v[116:119]\n\t" \
    "ds_write_b16_d16_hi %[ya], v160 offset:384\n\t" \
    "ds_write_b32 %[fc], %[fc] offset:4\n\t" \
    "v_mfma_f32_16x16x32_bf16 v[128:131], v[0:3], v[8:11], v[128:131]\n\t" \
    "ds_read_b128 v[176:179], %[yr]\n\t" \
    "s_waitcnt lgkmcnt(10)\n\t" \
    "v_mfma_f32_16x16x32_bf16 v[120:123], v[72:75], v[60:63], v[120:123]\n\t" \
    "ds_read_b64 v[76:77], %[ub2] offset:576\n\t" \
    "v_mfma_f32_16x16x32_bf16 v[92:95], v[32:35], v[60:63], v[92:95]\n\t" \
    "ds_read_b64 v[78:79], %[ub2] offset:584\n\t" \
    "L_cv6_11:\n\t" \
    "s_cbranch_execz L_cv6_12\n\t" \
    "ds_read_b64 v[96:97], %[ub2] offset:608\n\t" \
    "v_mfma_f32_16x16x32_bf16 v[124:127], v[72:75], v[20:23], v[124:127]\n\t" \
    "ds_read_b64 v[98:99], %[ub2] offset:616\n\t" \
    "v_mfma_f32_16x16x32_bf16 v[136:139], v[32:35], v[20:23], v[136:139]\n\t" \
    "v_mfma_f32_16x16x32_bf16 v[116:119], v[72:75], v[16:19], v[116:119]\n\t" \
    "ds_read_b64 v[68:69], %[ub2] offset:640\n\t" \
    "ds_read_b64 v[70:71], %[ub2] offset:648\n\t" \
    "v_mfma_f32_16x16x32_bf16 v[128:131], v[32:35], v[16:19], v[128:131]\n\t" \
    "ds_read_b64 v[108:109], %[ub2] offset:672\n\t" \
    "ds_read_b64 v[110:111], %[ub2] offset:680\n\t" \
    "s_waitcnt lgkmcnt(12)\n\t" \
    "v_mfma_f32_16x16x32_bf16 v[120:123], v[28:31], v[84:87], v[120:123]\n\t" \
    "ds_read_b64_tr_b16 v[80:81], %[hb2] offset:1056\n\t" \
    "v_mfma_f32_16x16x32_bf16 v[92:95], v[12:15], v[84:87], v[92:95]\n\t" \
    "ds_read_b64_tr_b16 v[82:83], %[hb2] offset:1024\n\t" \
    "v_mfma_f32_16x16x32_bf16 v[124:127], v[28:31], v[60:63], v[124:127]\n\t" \
    "ds_read_b64_tr_b16 v[88:89], %[hb2] offset:800\n\t" \
    "ds_read_b64_tr_b16 v[90:91], %[hb2] offset:768\n\t" \
    "v_mfma_f32_16x16x32_bf16 v[136:139], v[12:15], v[60:63], v[136:139]\n\t" \
    "v_mfma_f32_16x16x32_bf16 v[116:119], v[28:31], v[20:23], v[116:119]\n\t" \
    "ds_read_b64 v[24:25], %[ub2] offset:704\n\t" \
    "ds_read_b64 v[26:27], %[ub2] offset:712\n\t" \
    "v_mfma_f32_16x16x32_bf16 v[128:131], v[12:15], v[20:23], v[128:131]\n\t" \
    "ds_read_b64_tr_b16 v[36:37], %[hb2] offset:544\n\t" \
    "ds_read_b64_tr_b16 v[38:39], %[hb2] offset:512\n\t" \
    "s_waitcnt lgkmcnt(12)\n\t" \
    "v_mfma_f32_16x16x32_bf16 v[124:127], v[76:79], v[84:87], v[124:127]\n\t" \
    "ds_read_b64 v[112:113], %[ub2] offset:736\n\t" \
    "v_mfma_f32_16x16x32_bf16 v[136:139], v[96:99], v[84:87], v[136:139]\n\t" \
    "ds_read_b64 v[114:115], %[ub2] offset:744\n\t" \
    "ds_read_b64 v[104:105], %[ub2] offset:768\n\t" \
    "v_mfma_f32_16x16x32_bf16 v[116:119], v[76:79], v[60:63], v[116:119]\n\t" \
    "L_cv6_12:\n\t" \
    "s_cbranch_execz L_cv6_13\n\t" \
    "ds_read_b64 v[106:107], %[ub2] offset:776\n\t" \
    "ds_read_b64_tr_b16 v[48:49], %[hb2] offset:288\n\t" \
    "v_mfma_f32_16x16x32_bf16 v[128:131], v[96:99], v[60:63], v[128:131]\n\t" \
    "ds_read_b64_tr_b16 v[50:51], %[hb2] offset:256\n\t" \
    "ds_read_b64 v[140:141], %[ub2] offset:800\n\t" \
    "s_waitcnt lgkmcnt(14)\n\t" \
    "v_mfma_f32_16x16x32_bf16 v[116:119], v[68:71], v[84:87], v[116:119]\n\t" \
    "ds_read_b64 v[142:143], %[ub2] offset:808\n\t" \
    "v_mfma_f32_16x16x32_bf16 v[128:131], v[108:111], v[84:87], v[128:131]\n\t" \
    "ds_read_b64 v[148:149], %[ub2] offset:832\n\t" \
    "ds_read_b64 v[150:151], %[ub2] offset:840\n\t" \
    "s_waitcnt lgkmcnt(14)\n\t" \
    "v_mfma_f32_16x16x32_bf16 v[120:123], v[76:79], v[80:83], v[120:123]\n\t" \
    "ds_read_b64_tr_b16 v[100:101], %[hb2] offset:32\n\t" \
    "ds_read_b64_tr_b16 v[102:103], %[hb2] offset:0\n\t" \
    "ds_read_b64 v[52:53], %[ub2] offset:864\n\t" \
    "v_mfma_f32_16x16x32_bf16 v[92:95], v[96:99], v[80:83], v[92:95]\n\t" \
    "ds_read_b64 v[54:55], %[ub2] offset:872\n\t" \
    "v_mfma_f32_16x16x32_bf16 v[120:123], v[68:71], v[88:91], v[120:123]\n\t" \
    "v_mfma_f32_16x16x32_bf16 v[92:95], v[108:111], v[88:91], v[92:95]\n\t" \
    "s_waitcnt lgkmcnt(12)\n\t" \
    "v_mfma_f32_16x16x32_bf16 v[120:123], v[24:27], v[36:39], v[120:123]\n\t" \
    "v_mfma_f32_16x16x32_bf16 v[92:95], v[112:115], v[36:39], v[92:95]\n\t" \
    "ds_read_u16_d16_hi v172, %[ua2] offset:864\n\t" \
    "s_waitcnt lgkmcnt(7)\n\t" \
    "v_mfma_f32_16x16x32_bf16 v[120:123], v[104:107], v[48:51], v[120:123]\n\t" \
    "ds_read_b64 v[152:153], %[ub2] offset:896\n\t" \
    "v_mfma_f32_16x16x32_bf16 v[92:95], v[140:143], v[48:51], v[92:95]\n\t" \
    "ds_read_b64 v[154:155], %[ub2] offset:904\n\t" \
    "ds_read_b64 v[132:133], %[ub2] offset:928\n\t" \
    "s_waitcnt lgkmcnt(4)\n\t" \
    "v_mfma_f32_16x16x32_bf16 v[120:123], v[148:151], v[100:103], v[120:123]\n\t" \
    "ds_read_b64 v[134:135], %[ub2] offset:936\n\t" \
    "v_mfma_f32_16x16x32_bf16 v[92:95], v[52:55], v[100:103], v[92:95]\n\t" \
    "L_cv6_13:\n\t" \
    "s_cbranch_execz L_cv6_14\n\t" \
    "v_mfma_f32_16x16x32_bf16 v[124:127], v[68:71], v[80:83], v[124:127]\n\t" \
    "v_mfma_f32_16x16x32_bf16 v[136:139], v[108:111], v[80:83], v[136:139]\n\t" \
    "v_mfma_f32_16x16x32_bf16 v[124:127], v[24:27], v[88:91], v[124:127]\n\t" \
    "v_mfma_f32_16x16x32_bf16 v[136:139], v[112:115], v[88:91], v[136:139]\n\t" \
    "s_nop 3\n\t" \
    "v_cndmask_b32_e64 v163, v95, v123, %[m3]\n\t" \
    "v_cndmask_b32_e64 v162, v94, v122, %[m2]\n\t" \
    "v_mfma_f32_16x16x32_bf16 v[124:127], v[104:107], v[36:39], v[124:127]\n\t" \
    "v_cndmask_b32_e64 v161, v93, v121, %[m1]\n\t" \
    "v_add_f32_dpp v162, v163, v162 wave_ror:1 row_mask:0xf bank_mask:0xf\n\t" \
    "v_mfma_f32_16x16x32_bf16 v[136:139], v[140:143], v[36:39], v[136:139]\n\t" \
    "ds_read_u16_d16_hi v173, %[ua2] offset:896\n\t" \
    "ds_read_u16_d16_hi v174, %[ua2] offset:928\n\t" \
    "v_mfma_f32_16x16x32_bf16 v[124:127], v[148:151], v[48:51], v[124:127]\n\t" \
    "ds_read_b64 v[40:41], %[ub2] offset:960\n\t" \
    "ds_read_b64 v[42:43], %[ub2] offset:968\n\t" \
    "v_mfma_f32_16x16x32_bf16 v[136:139], v[52:55], v[48:51], v[136:139]\n\t" \
    "ds_read_b64 v[44:45], %[ub2] offset:992\n\t" \
    "ds_read_b64 v[46:47], %[ub2] offset:1000\n\t" \
    "s_waitcnt lgkmcnt(6)\n\t" \
    "v_mfma_f32_16x16x32_bf16 v[124:127], v[152:155], v[100:103], v[124:127]\n\t" \
    "v_fmac_f32 v92, %[dd2], v172\n\t" \
    "v_mfma_f32_16x16x32_bf16 v[136:139], v[132:135], v[100:103], v[136:139]\n\t" \
    "v_add_f32_dpp v161, v162, v161 wave_ror:1 row_mask:0xf bank_mask:0xf\n\t" \
    "s_nop 1\n\t" \
    "v_add_f32_dpp v92, v161, v92 wave_ror:1 row_mask:0xf bank_mask:0xf\n\t" \
    "v_mfma_f32_16x16x32_bf16 v[116:119], v[24:27], v[80:83], v[116:119]\n\t" \
    "v_cvt_pk_bf16_f32 v160, v92, v92\n\t" \
    "ds_write_b16 %[ya], v160 offset:0\n\t" \
    "v_mfma_f32_16x16x32_bf16 v[128:131], v[112:115], v[80:83], v[128:131]\n\t" \
    "L_cv6_14:\n\t" \
    "s_cbranch_execz L_cv6_15\n\t" \
    "v_mfma_f32_16x16x32_bf16 v[116:119], v[104:107], v[88:91], v[116:119]\n\t" \
    "v_mfma_f32_16x16x32_bf16 v[128:131], v[140:143], v[88:91], v[128:131]\n\t" \
    "v_cndmask_b32_e64 v163, v127, v95, %[m3]\n\t" \
    "v_cndmask_b32_e64 v167, v139, v127, %[m3]\n\t" \
    "v_mfma_f32_16x16x32_bf16 v[116:119], v[148:151], v[36:39], v[116:119]\n\t" \
    "v_cndmask_b32_e64 v162, v126, v94, %[m2]\n\t" \
    "v_cndmask_b32_e64 v166, v138, v126, %[m2]\n\t" \
    "v_mfma_f32_16x16x32_bf16 v[128:131], v[52:55], v[36:39], v[128:131]\n\t" \
    "ds_read_u16_d16_hi v175, %[ua2] offset:960\n\t" \
    "ds_read_u16_d16_hi v168, %[ua2] offset:992\n\t" \
    "v_mfma_f32_16x16x32_bf16 v[116:119], v[152:155], v[48:51], v[116:119]\n\t" \
    "v_cndmask_b32_e64 v161, v125, v93, %[m1]\n\t" \
    "v_cndmask_b32_e64 v165, v137, v125, %[m1]\n\t" \
    "v_mfma_f32_16x16x32_bf16 v[128:131], v[132:135], v[48:51], v[128:131]\n\t" \
    "v_add_f32_dpp v162, v163, v162 wave_ror:1 row_mask:0xf bank_mask:0xf\n\t" \
    "v_add_f32_dpp v166, v167, v166 wave_ror:1 row_mask:0xf bank_mask:0xf\n\t" \
    "s_waitcnt lgkmcnt(3)\n\t" \
    "v_mfma_f32_16x16x32_bf16 v[116:119], v[40:43], v[100:103], v[116:119]\n\t" \
    "v_fmac_f32 v124, %[dd2], v173\n\t" \
    "v_mfma_f32_16x16x32_bf16 v[128:131], v[44:47], v[100:103], v[128:131]\n\t" \
    "v_fmac_f32 v136, %[dd2], v174\n\t" \
    "v_add_f32_dpp v161, v162, v161 wave_ror:1 row_mask:0xf bank_mask:0xf\n\t" \
    "v_add_f32_dpp v165, v166, v165 wave_ror:1 row_mask:0xf bank_mask:0xf\n\t" \
    "s_nop 0\n\t" \
    "v_add_f32_dpp v124, v161, v124 wave_ror:1 row_mask:0xf bank_mask:0xf\n\t" \
    "v_add_f32_dpp v136, v165, v136 wave_ror:1 row_mask:0xf bank_mask:0xf\n\t" \
    "v_cvt_pk_bf16_f32 v160, v124, v136\n\t" \
    "ds_write_b16 %[ya], v160 offset:128\n\t" \
    "ds_write_b16_d16_hi %[ya], v160 offset:256\n\t" \
    "v_cndmask_b32_e64 v163, v119, v139, %[m3]\n\t" \
    "v_cndmask_b32_e64 v167, v131, v119, %[m3]\n\t" \
    "v_cndmask_b32_e64 v162, v118, v138, %[m2]\n\t" \
    "L_cv6_15:\n\t" \
    "s_cbranch_execz L_cv6_16\n\t" \
    "v_cndmask_b32_e64 v166, v130, v118, %[m2]\n\t" \
    "v_cndmask_b32_e64 v161, v117, v137, %[m1]\n\t" \
    "v_cndmask_b32_e64 v165, v129, v117, %[m1]\n\t" \
    "v_add_f32_dpp v162, v163, v162 wave_ror:1 row_mask:0xf bank_mask:0xf\n\t" \
    "v_add_f32_dpp v166, v167, v166 wave_ror:1 row_mask:0xf bank_mask:0xf\n\t" \
    "s_waitcnt lgkmcnt(2)\n\t" \
    "v_fmac_f32 v116, %[dd2], v175\n\t" \
    "v_fmac_f32 v128, %[dd2], v168\n\t" \
    "v_add_f32_dpp v161, v162, v161 wave_ror:1 row_mask:0xf bank_mask:0xf\n\t" \
    "v_add_f32_dpp v165, v166, v165 wave_ror:1 row_mask:0xf bank_mask:0xf\n\t" \
    "s_nop 0\n\t" \
    "v_add_f32_dpp v116, v161, v116 wave_ror:1 row_mask:0xf bank_mask:0xf\n\t" \
    "v_add_f32_dpp v128, v165, v128 wave_ror:1 row_mask:0xf bank_mask:0xf\n\t" \
    "v_cvt_pk_bf16_f32 v160, v116, v128\n\t" \
    "ds_write_b16 %[ya], v160 offset:384\n\t" \
    "ds_write_b16_d16_hi %[ya], v160 offset:512\n\t" \
    "ds_read_b128 v[180:183], %[yr]\n\t" \
    "s_waitcnt vmcnt(0)\n\t" \
    "s_mov_b32 exec_hi, 0x0\n\t" \
    "buffer_store_dwordx4 v[176:179], %[l16], %[rs], %[yo] offen offset:3584 sc1\n\t" \
    "s_mov_b32 exec_hi, -1\n\t" \
    "s_waitcnt lgkmcnt(0)\n\t" \
    "s_mov_b32 exec_hi, 0xff\n\t" \
    "buffer_store_dwordx4 v[180:183], %[l16], %[rs], %[yo2] offen offset:3456 sc1\n\t" \
    "s_mov_b32 exec_hi, -1\n\t" \
    "s_nop 1\n\t" \
    "L_cv6_16:\n\t" \
    "s_cbranch_execz L_back\n\t" \
    "s_branch CVE_%=\n\t" \
    "CVE_%=:\n\t"
#define A_QKV \
    "s_cmp_lg_u32 %[ly], 0\n\t" \
    "s_cbranch_scc1 L_qkv1_0\n\t" \
    "L_qkv0_0:\n\t" \
    "s_cbranch_execz L_qkv0_1\n\t" \
    "s_nop 4\n\t" \
    "s_mov_b32 s64, %[ab]\n\t" \
    "s_add_u32 s77, %[wv], %[ws]\n\t" \
    "s_add_u32 s88, %[w4], %[ws]\n\t" \
    "s_add_u32 m0, %[wv], 0\n\t" \
    "s_add_u32 s76, s77, 0\n\t" \
    "buffer_load_dwordx4 %[l16], %[rs], s76 offen offset:0 sc1 lds\n\t" \
    "buffer_load_dwordx4 %[l16], %[rs], s76 offen offset:1024 sc1 lds\n\t" \
    "buffer_load_dwordx4 %[l16], %[rs], s76 offen offset:2048 sc1 lds\n\t" \
    "buffer_load_dwordx4 %[l16], %[rs], s76 offen offset:3072 sc1 lds\n\t" \
    "s_add_u32 m0, %[wv], 4096\n\t" \
    "s_add_u32 s76, s77, 4096\n\t" \
    "buffer_load_dwordx4 %[l16], %[rs], s76 offen offset:0 sc1 lds\n\t" \
    "buffer_load_dwordx4 v[0:3], %[l16], %[rs], s64 offen offset:0 sc1\n\t" \
    "s_add_u32 s68, %[ab], 16384\n\t" \
    "buffer_load_dwordx4 v[4:7], %[l16], %[rs], s68 offen offset:2048 sc1\n\t" \
    "buffer_load_dwordx4 v[8:11], %[l16], %[rs], s64 offen offset:1024 sc1\n\t" \
    "buffer_load_dwordx4 v[12:15], %[l16], %[rs], s68 offen offset:3072 sc1\n\t" \
    "v_mov_b32 v216, -1\n\t" \
    "v_mov_b32 v217, -1\n\t" \
    "v_mov_b32 v218, -1\n\t" \
    "v_mov_b32 v219, -1\n\t" \
    "s_mov_b32 s75, %[ls]\n\t" \
    "SL0_%=:\n\t" \
    "ds_read_b32 v222, %[fg] offset:32\n\t" \
    "s_sub_u32 s75, s75, 1\n\t" \
    "s_cbranch_scc1 SLe0_%=\n\t" \
    "s_waitcnt lgkmcnt(0)\n\t" \
    "v_cmp_gt_u32 vcc, 1, v222\n\t" \
    "s_cbranch_vccnz SL0_%=\n\t" \
    "SLe0_%=:\n\t" \
    "s_waitcnt lgkmcnt(0)\n\t" \
    "v_readfirstlane_b32 %[sl], v222\n\t" \
    "s_nop 3\n\t" \
    "s_and_b32 %[sl], %[sl], 0x2000\n\t" \
    "s_xor_b32 s81, %[sl], %[f1]\n\t" \
    "L_qkv0_1:\n\t" \
    "s_cbranch_execz L_qkv0_2\n\t" \
    "s_add_u32 s78, %[po], s81\n\t" \
    "s_add_u32 s79, %[p1], s81\n\t" \
    "s_add_u32 s80, %[xo], s81\n\t" \
    "s_add_u32 s81, s80, 34816\n\t" \
    "buffer_store_dwordx4 v[216:219], %[l16], %[rs], s78 offen sc1\n\t" \
    "buffer_store_dwordx4 v[216:219], %[l16], %[rs], s79 offen sc1\n\t" \
    "buffer_store_dwordx2 v[216:217], %[ox], %[rs], s80 offen sc1\n\t" \
    "buffer_store_dwordx2 v[216:217], %[ox], %[rs], s81 offen sc1\n\t" \
    "s_add_u32 m0, %[wv], 27648\n\t" \
    "s_add_u32 s76, s77, 27648\n\t" \
    "buffer_load_dwordx4 %[l16], %[rs], s76 offen offset:0 sc1 lds\n\t" \
    "buffer_load_dwordx4 %[l16], %[rs], s76 offen offset:1024 sc1 lds\n\t" \
    "buffer_load_dwordx4 %[l16], %[rs], s76 offen offset:2048 sc1 lds\n\t" \
    "buffer_load_dwordx4 %[l16], %[rs], s76 offen offset:3072 sc1 lds\n\t" \
    "s_add_u32 m0, %[wv], 31744\n\t" \
    "s_add_u32 s76, s77, 31744\n\t" \
    "buffer_load_dwordx4 %[l16], %[rs], s76 offen offset:0 sc1 lds\n\t" \
    "buffer_load_dwordx4 v[16:19], %[l16], %[rs], s64 offen offset:2048 sc1\n\t" \
    "s_add_u32 s69, %[ab], 20480\n\t" \
    "buffer_load_dwordx4 v[20:23], %[l16], %[rs], s69 offen offset:0 sc1\n\t" \
    "s_add_u32 m0, %[wv], 55296\n\t" \
    "s_add_u32 s76, s77, 55296\n\t" \
    "buffer_load_dwordx4 %[l16], %[rs], s76 offen offset:0 sc1 lds\n\t" \
    "buffer_load_dwordx4 %[l16], %[rs], s76 offen offset:1024 sc1 lds\n\t" \
    "buffer_load_dwordx4 %[l16], %[rs], s76 offen offset:2048 sc1 lds\n\t" \
    "buffer_load_dwordx4 %[l16], %[rs], s76 offen offset:3072 sc1 lds\n\t" \
    "s_add_u32 m0, %[wv], 59392\n\t" \
    "s_add_u32 s76, s77, 59392\n\t" \
    "buffer_load_dwordx4 %[l16], %[rs], s76 offen offset:0 sc1 lds\n\t" \
    "buffer_load_dwordx4 v[24:27], %[l16], %[rs], s64 offen offset:3072 sc1\n\t" \
    "buffer_load_dwordx4 v[28:31], %[l16], %[rs], s69 offen offset:1024 sc1\n\t" \
    "L_qkv0_2:\n\t" \
    "s_cbranch_execz L_qkv0_3\n\t" \
    "s_add_u32 m0, %[wv], 82944\n\t" \
    "s_add_u32 s76, s77, 82944\n\t" \
    "buffer_load_dwordx4 %[l16], %[rs], s76 offen offset:0 sc1 lds\n\t" \
    "buffer_load_dwordx4 %[l16], %[rs], s76 offen offset:1024 sc1 lds\n\t" \
    "buffer_load_dwordx4 %[l16], %[rs], s76 offen offset:2048 sc1 lds\n\t" \
    "buffer_load_dwordx4 %[l16], %[rs], s76 offen offset:3072 sc1 lds\n\t" \
    "s_add_u32 m0, %[wv], 87040\n\t" \
    "s_add_u32 s76, s77, 87040\n\t" \
    "buffer_load_dwordx4 %[l16], %[rs], s76 offen offset:0 sc1 lds\n\t" \
    "s_add_u32 m0, %[w4], 0\n\t" \
    "s_add_u32 s76, s88, 0\n\t" \
    "buffer_load_dwordx4 %[l16], %[rs], s76 offen offset:0 sc1 lds\n\t" \
    "buffer_load_dwordx4 %[l16], %[rs], s76 offen offset:1024 sc1 lds\n\t" \
    "buffer_load_dwordx4 %[l16], %[rs], s76 offen offset:2048 sc1 lds\n\t" \
    "buffer_load_dwordx4 %[l16], %[rs], s76 offen offset:3072 sc1 lds\n\t" \
    "s_add_u32 s65, %[ab], 4096\n\t" \
    "buffer_load_dwordx4 v[32:35], %[l16], %[rs], s65 offen offset:0 sc1\n\t" \
    "buffer_load_dwordx4 v[36:39], %[l16], %[rs], s69 offen offset:2048 sc1\n\t" \
    "buffer_load_dwordx4 v[40:43], %[l16], %[rs], s65 offen offset:1024 sc1\n\t" \
    "buffer_load_dwordx4 v[44:47], %[l16], %[rs], s69 offen offset:3072 sc1\n\t" \
    "s_add_u32 s66, %[ab], 8192\n\t" \
    "s_add_u32 s67, %[ab], 12288\n\t" \
    "s_add_u32 s70, %[ab], 24576\n\t" \
    "s_add_u32 s71, %[ab], 28672\n\t" \
    "s_add_u32 s72, %[ab], 32768\n\t" \
    "v_mov_b32 v221, 1\n\t" \
    TSA(3) \
    "s_waitcnt vmcnt(35)\n\t" \
    "v_mov_b32 v220, 1\n\t" \
    "ds_write_b32 %[fa], v220 offset:32\n\t" \
    "s_mov_b32 s75, %[ls]\n\t" \
    "W0_%=:\n\t" \
    "ds_read_b32 v222, %[fg] offset:0\n\t" \
    "s_sub_u32 s75, s75, 1\n\t" \
    "L_qkv0_3:\n\t" \
    "s_cbranch_execz L_qkv0_4\n\t" \
    "s_cbranch_scc1 We0_%=\n\t" \
    "s_waitcnt lgkmcnt(0)\n\t" \
    "v_cmp_gt_u32 vcc, 1, v222\n\t" \
    "s_cbranch_vccnz W0_%=\n\t" \
    "We0_%=:\n\t" \
    TSA(4) \
    "ds_read_b128 v[184:187], %[l16] offset:0\n\t" \
    "ds_read_b128 v[188:191], %[l16] offset:1024\n\t" \
    "ds_read_b128 v[192:195], %[l16] offset:2048\n\t" \
    "ds_read_b128 v[196:199], %[l16] offset:3072\n\t" \
    "ds_read_b128 v[200:203], %[xrd] offset:0\n\t" \
    "ds_read_b128 v[204:207], %[l16] offset:6912\n\t" \
    "ds_read_b128 v[208:211], %[l16] offset:7936\n\t" \
    "ds_read_b128 v[212:215], %[l16] offset:8960\n\t" \
    TSA(5) \
    "s_waitcnt vmcnt(33) lgkmcnt(7)\n\t" \
    "v_mfma_f32_16x16x32_bf16 v[164:167], v[184:187], v[4:7], 0\n\t" \
    "ds_read_b128 v[216:219], %[l16] offset:9984\n\t" \
    "buffer_load_dwordx4 v[48:51], %[l16], %[rs], s65 offen offset:2048 sc1\n\t" \
    "v_mfma_f32_16x16x32_bf16 v[144:147], v[184:187], v[0:3], 0\n\t" \
    "s_waitcnt vmcnt(23)\n\t" \
    "v_mov_b32 v220, 2\n\t" \
    "ds_write_b32 %[fa], v220 offset:32\n\t" \
    "s_waitcnt lgkmcnt(8)\n\t" \
    "v_mfma_f32_16x16x32_bf16 v[168:171], v[188:191], v[4:7], 0\n\t" \
    "ds_read_b128 v[184:187], %[xrd] offset:6912\n\t" \
    "v_mfma_f32_16x16x32_bf16 v[148:151], v[188:191], v[0:3], 0\n\t" \
    "buffer_load_dwordx4 v[52:55], %[l16], %[rs], s70 offen offset:0 sc1\n\t" \
    "s_waitcnt lgkmcnt(8)\n\t" \
    "v_mfma_f32_16x16x32_bf16 v[172:175], v[192:195], v[4:7], 0\n\t" \
    "ds_read_b128 v[188:191], %[l16] offset:13824\n\t" \
    "s_waitcnt vmcnt(17)\n\t" \
    "v_mov_b32 v220, 3\n\t" \
    "ds_write_b32 %[fa], v220 offset:32\n\t" \
    "v_mfma_f32_16x16x32_bf16 v[152:155], v[192:195], v[0:3], 0\n\t" \
    "s_waitcnt vmcnt(6)\n\t" \
    "v_mov_b32 v220, 4\n\t" \
    "ds_write_b32 %[fa], v220 offset:32\n\t" \
    "s_waitcnt lgkmcnt(10)\n\t" \
    "L_qkv0_4:\n\t" \
    "s_cbranch_execz L_qkv0_5\n\t" \
    "v_mfma_f32_16x16x32_bf16 v[176:179], v[196:199], v[4:7], 0\n\t" \
    "ds_read_b128 v[192:195], %[l16] offset:14848\n\t" \
    "buffer_load_dwordx4 v[56:59], %[l16], %[rs], s65 offen offset:3072 sc1\n\t" \
    "v_mfma_f32_16x16x32_bf16 v[156:159], v[196:199], v[0:3], 0\n\t" \
    "s_waitcnt lgkmcnt(7)\n\t" \
    "v_mfma_f32_16x16x32_bf16 v[180:183], v[200:203], v[4:7], 0\n\t" \
    "ds_read_b128 v[196:199], %[l16] offset:15872\n\t" \
    "v_mfma_f32_16x16x32_bf16 v[160:163], v[200:203], v[0:3], 0\n\t" \
    "buffer_load_dwordx4 v[60:63], %[l16], %[rs], s70 offen offset:1024 sc1\n\t" \
    "v_mfma_f32_16x16x32_bf16 v[164:167], v[204:207], v[12:15], v[164:167]\n\t" \
    "ds_read_b128 v[200:203], %[l16] offset:16896\n\t" \
    "v_mfma_f32_16x16x32_bf16 v[144:147], v[204:207], v[8:11], v[144:147]\n\t" \
    "v_mfma_f32_16x16x32_bf16 v[168:171], v[208:211], v[12:15], v[168:171]\n\t" \
    "ds_read_b128 v[204:207], %[xrd] offset:13824\n\t" \
    "buffer_load_dwordx4 v[64:67], %[l16], %[rs], s66 offen offset:0 sc1\n\t" \
    "v_mfma_f32_16x16x32_bf16 v[148:151], v[208:211], v[8:11], v[148:151]\n\t" \
    "v_mfma_f32_16x16x32_bf16 v[172:175], v[212:215], v[12:15], v[172:175]\n\t" \
    "ds_read_b128 v[208:211], %[l16] offset:20736\n\t" \
    "v_mfma_f32_16x16x32_bf16 v[152:155], v[212:215], v[8:11], v[152:155]\n\t" \
    "buffer_load_dwordx4 v[68:71], %[l16], %[rs], s70 offen offset:2048 sc1\n\t" \
    "s_waitcnt lgkmcnt(4)\n\t" \
    "v_mfma_f32_16x16x32_bf16 v[176:179], v[216:219], v[12:15], v[176:179]\n\t" \
    "ds_read_b128 v[212:215], %[l16] offset:21760\n\t" \
    "v_mfma_f32_16x16x32_bf16 v[156:159], v[216:219], v[8:11], v[156:159]\n\t" \
    "v_mfma_f32_16x16x32_bf16 v[180:183], v[184:187], v[12:15], v[180:183]\n\t" \
    "ds_read_b128 v[216:219], %[l16] offset:22784\n\t" \
    "buffer_load_dwordx4 v[72:75], %[l16], %[rs], s66 offen offset:1024 sc1\n\t" \
    "v_mfma_f32_16x16x32_bf16 v[160:163], v[184:187], v[8:11], v[160:163]\n\t" \
    "L_qkv0_5:\n\t" \
    "s_cbranch_execz L_qkv0_6\n\t" \
    "v_mfma_f32_16x16x32_bf16 v[164:167], v[188:191], v[20:23], v[164:167]\n\t" \
    "ds_read_b128 v[184:187], %[l16] offset:23808\n\t" \
    "v_mfma_f32_16x16x32_bf16 v[144:147], v[188:191], v[16:19], v[144:147]\n\t" \
    "buffer_load_dwordx4 v[76:79], %[l16], %[rs], s70 offen offset:3072 sc1\n\t" \
    "ds_read_b32 v223, %[fg] offset:0\n\t" \
    "v_mfma_f32_16x16x32_bf16 v[168:171], v[192:195], v[20:23], v[168:171]\n\t" \
    "ds_read_b128 v[188:191], %[xrd] offset:20736\n\t" \
    "v_mfma_f32_16x16x32_bf16 v[148:151], v[192:195], v[16:19], v[148:151]\n\t" \
    "s_waitcnt lgkmcnt(5)\n\t" \
    "v_mfma_f32_16x16x32_bf16 v[172:175], v[196:199], v[20:23], v[172:175]\n\t" \
    "buffer_load_dwordx4 v[80:83], %[l16], %[rs], s66 offen offset:2048 sc1\n\t" \
    "v_mfma_f32_16x16x32_bf16 v[152:155], v[196:199], v[16:19], v[152:155]\n\t" \
    "v_mfma_f32_16x16x32_bf16 v[176:179], v[200:203], v[20:23], v[176:179]\n\t" \
    "s_waitcnt lgkmcnt(1)\n\t" \
    "v_cmp_gt_u32 vcc, 2, v223\n\t" \
    "s_cbranch_vccnz WS20_%=\n\t" \
    "WB20_%=:\n\t" \
    "v_mfma_f32_16x16x32_bf16 v[156:159], v[200:203], v[16:19], v[156:159]\n\t" \
    "ds_read_b128 v[192:195], %[l16] offset:27648\n\t" \
    "buffer_load_dwordx4 v[84:87], %[l16], %[rs], s71 offen offset:0 sc1\n\t" \
    "v_mfma_f32_16x16x32_bf16 v[180:183], v[204:207], v[20:23], v[180:183]\n\t" \
    "ds_read_b128 v[196:199], %[l16] offset:28672\n\t" \
    "v_mfma_f32_16x16x32_bf16 v[160:163], v[204:207], v[16:19], v[160:163]\n\t" \
    "ds_read_b128 v[200:203], %[l16] offset:29696\n\t" \
    "v_mfma_f32_16x16x32_bf16 v[164:167], v[208:211], v[28:31], v[164:167]\n\t" \
    "ds_read_b128 v[204:207], %[l16] offset:30720\n\t" \
    "buffer_load_dwordx4 v[88:91], %[l16], %[rs], s66 offen offset:3072 sc1\n\t" \
    "v_mfma_f32_16x16x32_bf16 v[144:147], v[208:211], v[24:27], v[144:147]\n\t" \
    "L_qkv0_6:\n\t" \
    "s_cbranch_execz L_qkv0_7\n\t" \
    "v_mfma_f32_16x16x32_bf16 v[168:171], v[212:215], v[28:31], v[168:171]\n\t" \
    "ds_read_b128 v[208:211], %[xrd] offset:27648\n\t" \
    "v_mfma_f32_16x16x32_bf16 v[148:151], v[212:215], v[24:27], v[148:151]\n\t" \
    "buffer_load_dwordx4 v[92:95], %[l16], %[rs], s71 offen offset:1024 sc1\n\t" \
    "v_mfma_f32_16x16x32_bf16 v[172:175], v[216:219], v[28:31], v[172:175]\n\t" \
    "ds_read_b128 v[212:215], %[l16] offset:34560\n\t" \
    "v_mfma_f32_16x16x32_bf16 v[152:155], v[216:219], v[24:27], v[152:155]\n\t" \
    "v_mfma_f32_16x16x32_bf16 v[176:179], v[184:187], v[28:31], v[176:179]\n\t" \
    "ds_read_b128 v[216:219], %[l16] offset:35584\n\t" \
    "buffer_load_dwordx4 v[96:99], %[l16], %[rs], s67 offen offset:0 sc1\n\t" \
    "v_mfma_f32_16x16x32_bf16 v[156:159], v[184:187], v[24:27], v[156:159]\n\t" \
    "s_waitcnt lgkmcnt(4)\n\t" \
    "v_mfma_f32_16x16x32_bf16 v[180:183], v[188:191], v[28:31], v[180:183]\n\t" \
    "ds_read_b128 v[184:187], %[l16] offset:36608\n\t" \
    "v_mfma_f32_16x16x32_bf16 v[160:163], v[188:191], v[24:27], v[160:163]\n\t" \
    "buffer_load_dwordx4 v[100:103], %[l16], %[rs], s71 offen offset:2048 sc1\n\t" \
    "s_waitcnt vmcnt(16)\n\t" \
    "v_mfma_f32_16x16x32_bf16 v[164:167], v[192:195], v[36:39], v[164:167]\n\t" \
    "ds_read_b128 v[188:191], %[l16] offset:37632\n\t" \
    "v_mfma_f32_16x16x32_bf16 v[144:147], v[192:195], v[32:35], v[144:147]\n\t" \
    "v_mfma_f32_16x16x32_bf16 v[168:171], v[196:199], v[36:39], v[168:171]\n\t" \
    "ds_read_b128 v[192:195], %[xrd] offset:34560\n\t" \
    "buffer_load_dwordx4 v[104:107], %[l16], %[rs], s67 offen offset:1024 sc1\n\t" \
    "v_mfma_f32_16x16x32_bf16 v[148:151], v[196:199], v[32:35], v[148:151]\n\t" \
    "v_mfma_f32_16x16x32_bf16 v[172:175], v[200:203], v[36:39], v[172:175]\n\t" \
    "ds_read_b128 v[196:199], %[l16] offset:41472\n\t" \
    "v_mfma_f32_16x16x32_bf16 v[152:155], v[200:203], v[32:35], v[152:155]\n\t" \
    "buffer_load_dwordx4 v[108:111], %[l16], %[rs], s71 offen offset:3072 sc1\n\t" \
    "s_waitcnt lgkmcnt(4)\n\t" \
    "v_mfma_f32_16x16x32_bf16 v[176:179], v[204:207], v[36:39], v[176:179]\n\t" \
    "ds_read_b128 v[200:203], %[l16] offset:42496\n\t" \
    "v_mfma_f32_16x16x32_bf16 v[156:159], v[204:207], v[32:35], v[156:159]\n\t" \
    "L_qkv0_7:\n\t" \
    "s_cbranch_execz L_qkv0_8\n\t" \
    "v_mfma_f32_16x16x32_bf16 v[180:183], v[208:211], v[36:39], v[180:183]\n\t" \
    "ds_read_b128 v[204:207], %[l16] offset:43520\n\t" \
    "buffer_load_dwordx4 v[112:115], %[l16], %[rs], s67 offen offset:2048 sc1\n\t" \
    "v_mfma_f32_16x16x32_bf16 v[160:163], v[208:211], v[32:35], v[160:163]\n\t" \
    "s_waitcnt vmcnt(17)\n\t" \
    "v_mfma_f32_16x16x32_bf16 v[164:167], v[212:215], v[44:47], v[164:167]\n\t" \
    "ds_read_b128 v[208:211], %[l16] offset:44544\n\t" \
    "v_mfma_f32_16x16x32_bf16 v[144:147], v[212:215], v[40:43], v[144:147]\n\t" \
    "buffer_load_dwordx4 v[116:119], %[l16], %[rs], s72 offen offset:0 sc1\n\t" \
    "v_mfma_f32_16x16x32_bf16 v[168:171], v[216:219], v[44:47], v[168:171]\n\t" \
    "ds_read_b128 v[212:215], %[xrd] offset:41472\n\t" \
    "v_mfma_f32_16x16x32_bf16 v[148:151], v[216:219], v[40:43], v[148:151]\n\t" \
    "s_waitcnt lgkmcnt(4)\n\t" \
    "v_mfma_f32_16x16x32_bf16 v[172:175], v[184:187], v[44:47], v[172:175]\n\t" \
    "ds_read_b128 v[216:219], %[l16] offset:48384\n\t" \
    "buffer_load_dwordx4 v[120:123], %[l16], %[rs], s67 offen offset:3072 sc1\n\t" \
    "v_mfma_f32_16x16x32_bf16 v[152:155], v[184:187], v[40:43], v[152:155]\n\t" \
    "v_mfma_f32_16x16x32_bf16 v[176:179], v[188:191], v[44:47], v[176:179]\n\t" \
    "ds_read_b128 v[184:187], %[l16] offset:49408\n\t" \
    "v_mfma_f32_16x16x32_bf16 v[156:159], v[188:191], v[40:43], v[156:159]\n\t" \
    "buffer_load_dwordx4 v[124:127], %[l16], %[rs], s72 offen offset:1024 sc1\n\t" \
    "v_mfma_f32_16x16x32_bf16 v[180:183], v[192:195], v[44:47], v[180:183]\n\t" \
    "ds_read_b128 v[188:191], %[l16] offset:50432\n\t" \
    "v_mfma_f32_16x16x32_bf16 v[160:163], v[192:195], v[40:43], v[160:163]\n\t" \
    "s_waitcnt vmcnt(18)\n\t" \
    "v_mfma_f32_16x16x32_bf16 v[164:167], v[196:199], v[52:55], v[164:167]\n\t" \
    "ds_read_b128 v[192:195], %[l16] offset:51456\n\t" \
    "buffer_load_dwordx4 v[128:131], %[l16], %[rs], s68 offen offset:0 sc1\n\t" \
    "v_mfma_f32_16x16x32_bf16 v[144:147], v[196:199], v[48:51], v[144:147]\n\t" \
    "ds_read_b32 v223, %[fg] offset:0\n\t" \
    "s_waitcnt lgkmcnt(5)\n\t" \
    "v_mfma_f32_16x16x32_bf16 v[168:171], v[200:203], v[52:55], v[168:171]\n\t" \
    "ds_read_b128 v[196:199], %[xrd] offset:48384\n\t" \
    "L_qkv0_8:\n\t" \
    "s_cbranch_execz L_back\n\t" \
    "v_mfma_f32_16x16x32_bf16 v[148:151], v[200:203], v[48:51], v[148:151]\n\t" \
    "buffer_load_dwordx4 v[132:135], %[l16], %[rs], s72 offen offset:2048 sc1\n\t" \
    "v_mfma_f32_16x16x32_bf16 v[172:175], v[204:207], v[52:55], v[172:175]\n\t" \
    "v_mfma_f32_16x16x32_bf16 v[152:155], v[204:207], v[48:51], v[152:155]\n\t" \
    "v_mfma_f32_16x16x32_bf16 v[176:179], v[208:211], v[52:55], v[176:179]\n\t" \
    "buffer_load_dwordx4 v[136:139], %[l16], %[rs], s68 offen offset:1024 sc1\n\t" \
    "s_waitcnt lgkmcnt(1)\n\t" \
    "v_cmp_gt_u32 vcc, 3, v223\n\t" \
    "s_cbranch_vccnz WS30_%=\n\t" \
    "WB30_%=:\n\t" \
    "v_mfma_f32_16x16x32_bf16 v[156:159], v[208:211], v[48:51], v[156:159]\n\t" \
    "ds_read_b128 v[200:203], %[l16] offset:55296\n\t" \
    "v_mfma_f32_16x16x32_bf16 v[180:183], v[212:215], v[52:55], v[180:183]\n\t" \
    "ds_read_b128 v[204:207], %[l16] offset:56320\n\t" \
    "v_mfma_f32_16x16x32_bf16 v[160:163], v[212:215], v[48:51], v[160:163]\n\t" \
    "ds_read_b128 v[208:211], %[l16] offset:57344\n\t" \
    "buffer_load_dwordx4 v[140:143], %[l16], %[rs], s72 offen offset:3072 sc1\n\t" \
    "s_waitcnt vmcnt(20)\n\t" \
    "v_mfma_f32_16x16x32_bf16 v[164:167], v[216:219], v[60:63], v[164:167]\n\t" \
    "ds_read_b128 v[212:215], %[l16] offset:58368\n\t" \
    "v_mfma_f32_16x16x32_bf16 v[144:147], v[216:219], v[56:59], v[144:147]\n\t" \
    "v_mfma_f32_16x16x32_bf16 v[168:171], v[184:187], v[60:63], v[168:171]\n\t" \
    "ds_read_b128 v[216:219], %[xrd] offset:55296\n\t" \
    "v_mfma_f32_16x16x32_bf16 v[148:151], v[184:187], v[56:59], v[148:151]\n\t" \
    "v_mfma_f32_16x16x32_bf16 v[172:175], v[188:191], v[60:63], v[172:175]\n\t" \
    "ds_read_b128 v[184:187], %[wr2] offset:0\n\t" \
    "v_mfma_f32_16x16x32_bf16 v[152:155], v[188:191], v[56:59], v[152:155]\n\t" \
    "v_mfma_f32_16x16x32_bf16 v[176:179], v[192:195], v[60:63], v[176:179]\n\t" \
    "ds_read_b128 v[188:191], %[wr2] offset:1024\n\t" \
    "v_mfma_f32_16x16x32_bf16 v[156:159], v[192:195], v[56:59], v[156:159]\n\t" \
    "s_waitcnt lgkmcnt(4)\n\t" \
    "v_mfma_f32_16x16x32_bf16 v[180:183], v[196:199], v[60:63], v[180:183]\n\t" \
    "ds_read_b128 v[192:195], %[wr2] offset:2048\n\t" \
    "v_mfma_f32_16x16x32_bf16 v[160:163], v[196:199], v[56:59], v[160:163]\n\t" \
    "s_waitcnt vmcnt(18)\n\t" \
    "L_qkv0_9:\n\t" \
    "s_cbranch_execz L_qkv0_10\n\t" \
    "v_mfma_f32_16x16x32_bf16 v[164:167], v[200:203], v[68:71], v[164:167]\n\t" \
    "ds_read_b128 v[196:199], %[wr2] offset:3072\n\t" \
    "v_mfma_f32_16x16x32_bf16 v[144:147], v[200:203], v[64:67], v[144:147]\n\t" \
    "v_mfma_f32_16x16x32_bf16 v[168:171], v[204:207], v[68:71], v[168:171]\n\t" \
    "ds_read_b128 v[200:203], %[xr2] offset:0\n\t" \
    "v_mfma_f32_16x16x32_bf16 v[148:151], v[204:207], v[64:67], v[148:151]\n\t" \
    "v_mfma_f32_16x16x32_bf16 v[172:175], v[208:211], v[68:71], v[172:175]\n\t" \
    "ds_read_b128 v[204:207], %[wr2] offset:6912\n\t" \
    "v_mfma_f32_16x16x32_bf16 v[152:155], v[208:211], v[64:67], v[152:155]\n\t" \
    "s_waitcnt lgkmcnt(4)\n\t" \
    "v_mfma_f32_16x16x32_bf16 v[176:179], v[212:215], v[68:71], v[176:179]\n\t" \
    "ds_read_b128 v[208:211], %[wr2] offset:7936\n\t" \
    "v_mfma_f32_16x16x32_bf16 v[156:159], v[212:215], v[64:67], v[156:159]\n\t" \
    "v_mfma_f32_16x16x32_bf16 v[180:183], v[216:219], v[68:71], v[180:183]\n\t" \
    "ds_read_b128 v[212:215], %[wr2] offset:8960\n\t" \
    "v_mfma_f32_16x16x32_bf16 v[160:163], v[216:219], v[64:67], v[160:163]\n\t" \
    "s_waitcnt vmcnt(16)\n\t" \
    "v_mfma_f32_16x16x32_bf16 v[164:167], v[184:187], v[76:79], v[164:167]\n\t" \
    "ds_read_b128 v[216:219], %[wr2] offset:9984\n\t" \
    "s_mov_b64 s[82:83], 0xffff\n\t" \
    "v_mfma_f32_16x16x32_bf16 v[144:147], v[184:187], v[72:75], v[144:147]\n\t" \
    "v_mfma_f32_16x16x32_bf16 v[168:171], v[188:191], v[76:79], v[168:171]\n\t" \
    "ds_read_b128 v[184:187], %[xr2] offset:6912\n\t" \
    "s_mov_b32 s84, 0xffff0000\n\t" \
    "v_mfma_f32_16x16x32_bf16 v[148:151], v[188:191], v[72:75], v[148:151]\n\t" \
    "s_waitcnt lgkmcnt(4)\n\t" \
    "v_mfma_f32_16x16x32_bf16 v[172:175], v[192:195], v[76:79], v[172:175]\n\t" \
    "ds_read_b128 v[188:191], %[wr2] offset:13824\n\t" \
    "s_mov_b32 s85, 0\n\t" \
    "v_mfma_f32_16x16x32_bf16 v[152:155], v[192:195], v[72:75], v[152:155]\n\t" \
    "L_qkv0_10:\n\t" \
    "s_cbranch_execz L_qkv0_11\n\t" \
    "v_mfma_f32_16x16x32_bf16 v[176:179], v[196:199], v[76:79], v[176:179]\n\t" \
    "ds_read_b128 v[192:195], %[wr2] offset:14848\n\t" \
    "s_mov_b32 s86, 0x3eae1a93\n\t" \
    "v_mfma_f32_16x16x32_bf16 v[156:159], v[196:199], v[72:75], v[156:159]\n\t" \
    "v_mfma_f32_16x16x32_bf16 v[180:183], v[200:203], v[76:79], v[180:183]\n\t" \
    "ds_read_b128 v[196:199], %[wr2] offset:15872\n\t" \
    "s_mov_b32 s87, 0xbeae1a93\n\t" \
    "v_mfma_f32_16x16x32_bf16 v[160:163], v[200:203], v[72:75], v[160:163]\n\t" \
    "s_waitcnt vmcnt(14)\n\t" \
    "v_mfma_f32_16x16x32_bf16 v[164:167], v[204:207], v[84:87], v[164:167]\n\t" \
    "ds_read_b128 v[200:203], %[wr2] offset:16896\n\t" \
    "v_mfma_f32_16x16x32_bf16 v[144:147], v[204:207], v[80:83], v[144:147]\n\t" \
    "ds_read_b32 v223, %[fg] offset:0\n\t" \
    "s_waitcnt lgkmcnt(5)\n\t" \
    "v_mfma_f32_16x16x32_bf16 v[168:171], v[208:211], v[84:87], v[168:171]\n\t" \
    "ds_read_b128 v[204:207], %[xr2] offset:13824\n\t" \
    "v_mfma_f32_16x16x32_bf16 v[148:151], v[208:211], v[80:83], v[148:151]\n\t" \
    "v_mfma_f32_16x16x32_bf16 v[172:175], v[212:215], v[84:87], v[172:175]\n\t" \
    "v_mfma_f32_16x16x32_bf16 v[152:155], v[212:215], v[80:83], v[152:155]\n\t" \
    "v_mfma_f32_16x16x32_bf16 v[176:179], v[216:219], v[84:87], v[176:179]\n\t" \
    "s_waitcnt lgkmcnt(1)\n\t" \
    "v_cmp_gt_u32 vcc, 4, v223\n\t" \
    "s_cbranch_vccnz WS40_%=\n\t" \
    "WB40_%=:\n\t" \
    "v_mfma_f32_16x16x32_bf16 v[156:159], v[216:219], v[80:83], v[156:159]\n\t" \
    "ds_read_b128 v[208:211], %[wr2] offset:22784\n\t" \
    "v_mfma_f32_16x16x32_bf16 v[180:183], v[184:187], v[84:87], v[180:183]\n\t" \
    "ds_read_b128 v[212:215], %[wr2] offset:23808\n\t" \
    "v_mfma_f32_16x16x32_bf16 v[160:163], v[184:187], v[80:83], v[160:163]\n\t" \
    "ds_read_b128 v[216:219], %[xr2] offset:20736\n\t" \
    "s_waitcnt vmcnt(12)\n\t" \
    "v_mfma_f32_16x16x32_bf16 v[164:167], v[188:191], v[92:95], v[164:167]\n\t" \
    "ds_read_b128 v[184:187], %[wr2] offset:29696\n\t" \
    "v_mfma_f32_16x16x32_bf16 v[144:147], v[188:191], v[88:91], v[144:147]\n\t" \
    "L_qkv0_11:\n\t" \
    "s_cbranch_execz L_qkv0_12\n\t" \
    "v_mfma_f32_16x16x32_bf16 v[168:171], v[192:195], v[92:95], v[168:171]\n\t" \
    "ds_read_b128 v[188:191], %[wr2] offset:30720\n\t" \
    "v_mfma_f32_16x16x32_bf16 v[148:151], v[192:195], v[88:91], v[148:151]\n\t" \
    "v_mfma_f32_16x16x32_bf16 v[172:175], v[196:199], v[92:95], v[172:175]\n\t" \
    "ds_read_b128 v[192:195], %[xr2] offset:27648\n\t" \
    "v_mfma_f32_16x16x32_bf16 v[152:155], v[196:199], v[88:91], v[152:155]\n\t" \
    "v_mfma_f32_16x16x32_bf16 v[176:179], v[200:203], v[92:95], v[176:179]\n\t" \
    "ds_read_b128 v[196:199], %[wr2] offset:36608\n\t" \
    "v_mfma_f32_16x16x32_bf16 v[156:159], v[200:203], v[88:91], v[156:159]\n\t" \
    "s_waitcnt lgkmcnt(4)\n\t" \
    "v_mfma_f32_16x16x32_bf16 v[180:183], v[204:207], v[92:95], v[180:183]\n\t" \
    "ds_read_b128 v[200:203], %[wr2] offset:37632\n\t" \
    "v_mfma_f32_16x16x32_bf16 v[160:163], v[204:207], v[88:91], v[160:163]\n\t" \
    "s_waitcnt vmcnt(10)\n\t" \
    "v_mfma_f32_16x16x32_bf16 v[172:175], v[208:211], v[100:103], v[172:175]\n\t" \
    "ds_read_b128 v[204:207], %[xr2] offset:34560\n\t" \
    "v_mfma_f32_16x16x32_bf16 v[152:155], v[208:211], v[96:99], v[152:155]\n\t" \
    "v_mfma_f32_16x16x32_bf16 v[176:179], v[212:215], v[100:103], v[176:179]\n\t" \
    "ds_read_b128 v[208:211], %[wr2] offset:43520\n\t" \
    "v_mfma_f32_16x16x32_bf16 v[156:159], v[212:215], v[96:99], v[156:159]\n\t" \
    "v_mfma_f32_16x16x32_bf16 v[180:183], v[216:219], v[100:103], v[180:183]\n\t" \
    "ds_read_b128 v[212:215], %[wr2] offset:44544\n\t" \
    "v_mfma_f32_16x16x32_bf16 v[160:163], v[216:219], v[96:99], v[160:163]\n\t" \
    "s_waitcnt vmcnt(8) lgkmcnt(4)\n\t" \
    "v_mfma_f32_16x16x32_bf16 v[172:175], v[184:187], v[108:111], v[172:175]\n\t" \
    "ds_read_b128 v[216:219], %[xr2] offset:41472\n\t" \
    "v_mfma_f32_16x16x32_bf16 v[152:155], v[184:187], v[104:107], v[152:155]\n\t" \
    "L_qkv0_12:\n\t" \
    "s_cbranch_execz L_qkv0_13\n\t" \
    "v_mfma_f32_16x16x32_bf16 v[176:179], v[188:191], v[108:111], v[176:179]\n\t" \
    "ds_read_b128 v[184:187], %[wr2] offset:50432\n\t" \
    "v_mfma_f32_16x16x32_bf16 v[156:159], v[188:191], v[104:107], v[156:159]\n\t" \
    "v_mfma_f32_16x16x32_bf16 v[180:183], v[192:195], v[108:111], v[180:183]\n\t" \
    "ds_read_b128 v[188:191], %[wr2] offset:51456\n\t" \
    "v_mfma_f32_16x16x32_bf16 v[160:163], v[192:195], v[104:107], v[160:163]\n\t" \
    "s_waitcnt vmcnt(6)\n\t" \
    "v_mfma_f32_16x16x32_bf16 v[172:175], v[196:199], v[116:119], v[172:175]\n\t" \
    "ds_read_b128 v[192:195], %[xr2] offset:48384\n\t" \
    "v_mfma_f32_16x16x32_bf16 v[152:155], v[196:199], v[112:115], v[152:155]\n\t" \
    "s_waitcnt lgkmcnt(4)\n\t" \
    "v_mfma_f32_16x16x32_bf16 v[176:179], v[200:203], v[116:119], v[176:179]\n\t" \
    "ds_read_b128 v[196:199], %[wr2] offset:57344\n\t" \
    "v_mfma_f32_16x16x32_bf16 v[156:159], v[200:203], v[112:115], v[156:159]\n\t" \
    "v_mfma_f32_16x16x32_bf16 v[180:183], v[204:207], v[116:119], v[180:183]\n\t" \
    "ds_read_b128 v[200:203], %[wr2] offset:58368\n\t" \
    "v_mfma_f32_16x16x32_bf16 v[160:163], v[204:207], v[112:115], v[160:163]\n\t" \
    "s_waitcnt vmcnt(4)\n\t" \
    "v_mfma_f32_16x16x32_bf16 v[172:175], v[208:211], v[124:127], v[172:175]\n\t" \
    "ds_read_b128 v[204:207], %[xr2] offset:55296\n\t" \
    "v_mfma_f32_16x16x32_bf16 v[152:155], v[208:211], v[120:123], v[152:155]\n\t" \
    "v_mfma_f32_16x16x32_bf16 v[176:179], v[212:215], v[124:127], v[176:179]\n\t" \
    "ds_read_b128 v[208:211], %[wr2] offset:20736\n\t" \
    "v_mfma_f32_16x16x32_bf16 v[156:159], v[212:215], v[120:123], v[156:159]\n\t" \
    "s_waitcnt lgkmcnt(4)\n\t" \
    "v_mfma_f32_16x16x32_bf16 v[180:183], v[216:219], v[124:127], v[180:183]\n\t" \
    "ds_read_b128 v[212:215], %[wr2] offset:27648\n\t" \
    "v_mfma_f32_16x16x32_bf16 v[160:163], v[216:219], v[120:123], v[160:163]\n\t" \
    "s_waitcnt vmcnt(2)\n\t" \
    "v_mfma_f32_16x16x32_bf16 v[172:175], v[184:187], v[132:135], v[172:175]\n\t" \
    "ds_read_b128 v[216:219], %[wr2] offset:34560\n\t" \
    "v_mfma_f32_16x16x32_bf16 v[152:155], v[184:187], v[128:131], v[152:155]\n\t" \
    "L_qkv0_13:\n\t" \
    "s_cbranch_execz L_qkv0_14\n\t" \
    "v_mfma_f32_16x16x32_bf16 v[176:179], v[188:191], v[132:135], v[176:179]\n\t" \
    "ds_read_b128 v[184:187], %[wr2] offset:41472\n\t" \
    "v_mfma_f32_16x16x32_bf16 v[156:159], v[188:191], v[128:131], v[156:159]\n\t" \
    "v_mfma_f32_16x16x32_bf16 v[180:183], v[192:195], v[132:135], v[180:183]\n\t" \
    "v_mfma_f32_16x16x32_bf16 v[160:163], v[192:195], v[128:131], v[160:163]\n\t" \
    "s_waitcnt vmcnt(0) lgkmcnt(3)\n\t" \
    "v_mfma_f32_16x16x32_bf16 v[172:175], v[196:199], v[140:143], v[172:175]\n\t" \
    "v_mfma_f32_16x16x32_bf16 v[152:155], v[196:199], v[136:139], v[152:155]\n\t" \
    "v_mfma_f32_16x16x32_bf16 v[176:179], v[200:203], v[140:143], v[176:179]\n\t" \
    "v_mfma_f32_16x16x32_bf16 v[156:159], v[200:203], v[136:139], v[156:159]\n\t" \
    "v_mfma_f32_16x16x32_bf16 v[180:183], v[204:207], v[140:143], v[180:183]\n\t" \
    "v_mfma_f32_16x16x32_bf16 v[160:163], v[204:207], v[136:139], v[160:163]\n\t" \
    "ds_read_b128 v[188:191], %[wr2] offset:48384\n\t" \
    "v_mfma_f32_16x16x32_bf16 v[164:167], v[208:211], v[100:103], v[164:167]\n\t" \
    "v_mfma_f32_16x16x32_bf16 v[144:147], v[208:211], v[96:99], v[144:147]\n\t" \
    "ds_read_b128 v[192:195], %[wr2] offset:55296\n\t" \
    "s_waitcnt lgkmcnt(2)\n\t" \
    "v_mfma_f32_16x16x32_bf16 v[164:167], v[212:215], v[108:111], v[164:167]\n\t" \
    "v_cvt_pk_bf16_f32 v172, v172, v173\n\t" \
    "v_cvt_pk_bf16_f32 v173, v174, v175\n\t" \
    "v_mfma_f32_16x16x32_bf16 v[144:147], v[212:215], v[104:107], v[144:147]\n\t" \
    "ds_read_b128 v[196:199], %[wr2] offset:21760\n\t" \
    "v_cvt_pk_bf16_f32 v176, v176, v177\n\t" \
    "v_mfma_f32_16x16x32_bf16 v[164:167], v[216:219], v[116:119], v[164:167]\n\t" \
    "v_cvt_pk_bf16_f32 v177, v178, v179\n\t" \
    "v_cvt_pk_bf16_f32 v174, v180, v181\n\t" \
    "v_mfma_f32_16x16x32_bf16 v[144:147], v[216:219], v[112:115], v[144:147]\n\t" \
    "ds_read_b128 v[200:203], %[wr2] offset:28672\n\t" \
    "ds_read_b32 v223, %[fg] offset:16\n\t" \
    "v_mfma_f32_16x16x32_bf16 v[164:167], v[184:187], v[124:127], v[164:167]\n\t" \
    "v_cvt_pk_bf16_f32 v175, v182, v183\n\t" \
    "s_cmp_eq_u32 %[wave], 3\n\t" \
    "s_cbranch_scc1 PK0_%=\n\t" \
    "L_qkv0_14:\n\t" \
    "s_cbranch_execz L_qkv0_15\n\t" \
    "ds_write_b64 %[sx], v[174:175] offset:2432\n\t" \
    "ds_write_b64 %[sx], v[172:173] offset:2816\n\t" \
    "ds_write_b64 %[sx], v[176:177] offset:3328\n\t" \
    "ds_write_b32 %[fa], v221 offset:4\n\t" \
    "PK0_%=:\n\t" \
    "v_mfma_f32_16x16x32_bf16 v[144:147], v[184:187], v[120:123], v[144:147]\n\t" \
    "ds_read_b128 v[204:207], %[wr2] offset:35584\n\t" \
    "s_waitcnt lgkmcnt(5)\n\t" \
    "v_mfma_f32_16x16x32_bf16 v[164:167], v[188:191], v[132:135], v[164:167]\n\t" \
    "v_cvt_pk_bf16_f32 v178, v180, v181\n\t" \
    "v_cvt_pk_bf16_f32 v179, v182, v183\n\t" \
    "v_mfma_f32_16x16x32_bf16 v[144:147], v[188:191], v[128:131], v[144:147]\n\t" \
    "ds_read_b128 v[208:211], %[wr2] offset:42496\n\t" \
    "s_waitcnt lgkmcnt(5)\n\t" \
    "v_mfma_f32_16x16x32_bf16 v[164:167], v[192:195], v[140:143], v[164:167]\n\t" \
    "s_waitcnt lgkmcnt(2)\n\t" \
    "v_cmp_gt_u32 vcc, 5, v223\n\t" \
    "s_cbranch_vccnz WS50_%=\n\t" \
    "WB50_%=:\n\t" \
    "v_mfma_f32_16x16x32_bf16 v[144:147], v[192:195], v[136:139], v[144:147]\n\t" \
    "ds_read_b128 v[212:215], %[wr2] offset:49408\n\t" \
    "v_cvt_pk_bf16_f32 v152, v152, v153\n\t" \
    "v_mfma_f32_16x16x32_bf16 v[168:171], v[196:199], v[100:103], v[168:171]\n\t" \
    "v_cvt_pk_bf16_f32 v153, v154, v155\n\t" \
    "v_cvt_pk_bf16_f32 v156, v156, v157\n\t" \
    "v_mfma_f32_16x16x32_bf16 v[148:151], v[196:199], v[96:99], v[148:151]\n\t" \
    "ds_read_b128 v[216:219], %[wr2] offset:56320\n\t" \
    "v_cvt_pk_bf16_f32 v157, v158, v159\n\t" \
    "v_mfma_f32_16x16x32_bf16 v[168:171], v[200:203], v[108:111], v[168:171]\n\t" \
    "v_cvt_pk_bf16_f32 v154, v160, v161\n\t" \
    "v_cvt_pk_bf16_f32 v155, v162, v163\n\t" \
    "v_mfma_f32_16x16x32_bf16 v[148:151], v[200:203], v[104:107], v[148:151]\n\t" \
    "ds_read_b128 v[184:187], %[l16] offset:4864\n\t" \
    "s_waitcnt lgkmcnt(4)\n\t" \
    "v_mfma_f32_16x16x32_bf16 v[168:171], v[204:207], v[116:119], v[168:171]\n\t" \
    "v_cvt_pk_bf16_f32 v158, v160, v161\n\t" \
    "L_qkv0_15:\n\t" \
    "s_cbranch_execz L_qkv0_16\n\t" \
    "v_cvt_pk_bf16_f32 v159, v162, v163\n\t" \
    "v_mfma_f32_16x16x32_bf16 v[180:183], v[172:175], %[sel], 0\n\t" \
    "v_cvt_pk_bf16_f32 v164, v164, v165\n\t" \
    "v_cvt_pk_bf16_f32 v165, v166, v167\n\t" \
    "v_mfma_f32_16x16x32_bf16 v[148:151], v[204:207], v[112:115], v[148:151]\n\t" \
    "ds_read_b128 v[188:191], %[l16] offset:5888\n\t" \
    "s_waitcnt lgkmcnt(4)\n\t" \
    "v_mfma_f32_16x16x32_bf16 v[168:171], v[208:211], v[124:127], v[168:171]\n\t" \
    "v_mov_b32 v166, 0\n\t" \
    "v_cndmask_b32_e64 v167, 0, v174, s[82:83]\n\t" \
    "v_mfma_f32_16x16x32_bf16 v[148:151], v[208:211], v[120:123], v[148:151]\n\t" \
    "ds_read_b128 v[192:195], %[l16] offset:11776\n\t" \
    "s_waitcnt lgkmcnt(4)\n\t" \
    "v_mfma_f32_16x16x32_bf16 v[168:171], v[212:215], v[132:135], v[168:171]\n\t" \
    "v_cvt_pk_bf16_f32 v144, v144, v145\n\t" \
    "v_cvt_pk_bf16_f32 v145, v146, v147\n\t" \
    "v_mfma_f32_16x16x32_bf16 v[148:151], v[212:215], v[128:131], v[148:151]\n\t" \
    "ds_read_b128 v[196:199], %[l16] offset:12800\n\t" \
    "s_waitcnt lgkmcnt(4)\n\t" \
    "v_mfma_f32_16x16x32_bf16 v[168:171], v[216:219], v[140:143], v[168:171]\n\t" \
    "v_mov_b32 v146, 0\n\t" \
    "v_cndmask_b32_e64 v147, 0, v154, s[82:83]\n\t" \
    "v_mfma_f32_16x16x32_bf16 v[148:151], v[216:219], v[136:139], v[148:151]\n\t" \
    TSA(6) \
    "ds_read_b128 v[200:203], %[l16] offset:18688\n\t" \
    "s_waitcnt lgkmcnt(4)\n\t" \
    "v_mfma_f32_16x16x32_bf16 v[160:163], v[0:3], v[184:187], 0\n\t" \
    "v_cvt_pk_bf16_f32 v180, v180, v181\n\t" \
    "v_cvt_pk_bf16_f32 v181, v182, v183\n\t" \
    "v_mfma_f32_16x16x32_bf16 v[204:207], v[4:7], v[184:187], 0\n\t" \
    "ds_read_b128 v[208:211], %[l16] offset:19712\n\t" \
    "s_waitcnt lgkmcnt(4)\n\t" \
    "v_mfma_f32_16x16x32_bf16 v[212:215], v[0:3], v[188:191], 0\n\t" \
    "v_cvt_pk_bf16_f32 v168, v168, v169\n\t" \
    "v_cvt_pk_bf16_f32 v169, v170, v171\n\t" \
    "L_qkv0_16:\n\t" \
    "s_cbranch_execz L_qkv0_17\n\t" \
    "v_mfma_f32_16x16x32_bf16 v[216:219], v[152:155], %[sel], 0\n\t" \
    "v_mov_b32 v170, 0\n\t" \
    "v_cndmask_b32_e64 v171, 0, v174, s[84:85]\n\t" \
    "v_mfma_f32_16x16x32_bf16 v[184:187], v[4:7], v[188:191], 0\n\t" \
    "ds_read_b128 v[0:3], %[l16] offset:25600\n\t" \
    "s_waitcnt lgkmcnt(4)\n\t" \
    "v_mfma_f32_16x16x32_bf16 v[160:163], v[8:11], v[192:195], v[160:163]\n\t" \
    "v_cvt_pk_bf16_f32 v148, v148, v149\n\t" \
    "v_cvt_pk_bf16_f32 v149, v150, v151\n\t" \
    "v_mfma_f32_16x16x32_bf16 v[204:207], v[12:15], v[192:195], v[204:207]\n\t" \
    "ds_read_b128 v[188:191], %[l16] offset:26624\n\t" \
    "s_waitcnt lgkmcnt(4)\n\t" \
    "v_mfma_f32_16x16x32_bf16 v[212:215], v[8:11], v[196:199], v[212:215]\n\t" \
    "v_cvt_pk_bf16_f32 v182, v216, v217\n\t" \
    "v_cvt_pk_bf16_f32 v183, v218, v219\n\t" \
    "v_mfma_f32_16x16x32_bf16 v[4:7], v[172:175], v[164:167], %[mc]\n\t" \
    "v_cvt_pk_bf16_f32 v216, v216, v217\n\t" \
    "v_cvt_pk_bf16_f32 v217, v218, v219\n\t" \
    "v_mfma_f32_16x16x32_bf16 v[192:195], v[152:155], v[164:167], v[224:227]\n\t" \
    "v_mov_b32 v150, 0\n\t" \
    "v_cndmask_b32_e64 v151, 0, v154, s[84:85]\n\t" \
    "v_mfma_f32_16x16x32_bf16 v[8:11], v[152:155], v[144:147], %[mc]\n\t" \
    "v_mfma_f32_16x16x32_bf16 v[172:175], v[176:179], v[168:171], %[mc]\n\t" \
    "v_mfma_f32_16x16x32_bf16 v[164:167], v[156:159], v[168:171], v[224:227]\n\t" \
    "v_mfma_f32_16x16x32_bf16 v[152:155], v[156:159], v[148:151], %[mc]\n\t" \
    "v_mfma_f32_16x16x32_bf16 v[184:187], v[12:15], v[196:199], v[184:187]\n\t" \
    "ds_read_b128 v[176:179], %[l16] offset:32512\n\t" \
    "s_waitcnt lgkmcnt(1)\n\t" \
    "v_mfma_f32_16x16x32_bf16 v[160:163], v[16:19], v[200:203], v[160:163]\n\t" \
    "ds_read_b32 v222, %[fa]\n\t" \
    "ds_read2_b64 v[156:159], %[sx] offset0:48 offset1:0\n\t" \
    "ds_read2_b64 v[168:171], %[sx] offset0:112 offset1:0\n\t" \
    "v_mfma_f32_16x16x32_bf16 v[204:207], v[20:23], v[200:203], v[204:207]\n\t" \
    "L_qkv0_17:\n\t" \
    "s_cbranch_execz L_back\n\t" \
    "ds_read_b128 v[196:199], %[l16] offset:33536\n\t" \
    "v_maximum3_f32 v4, v4, v5, v192\n\t" \
    "v_mfma_f32_16x16x32_bf16 v[212:215], v[16:19], v[208:211], v[212:215]\n\t" \
    "v_maximum3_f32 v4, v4, v193, v193\n\t" \
    "v_maximum3_f32 v6, v6, v7, v194\n\t" \
    "v_mfma_f32_16x16x32_bf16 v[184:187], v[20:23], v[208:211], v[184:187]\n\t" \
    "ds_read_b128 v[12:15], %[l16] offset:39424\n\t" \
    "v_maximum3_f32 v6, v6, v195, v195\n\t" \
    "v_mfma_f32_16x16x32_bf16 v[160:163], v[24:27], v[0:3], v[160:163]\n\t" \
    "v_maximum3_f32 v172, v172, v173, v164\n\t" \
    "v_maximum3_f32 v172, v172, v165, v165\n\t" \
    "v_mfma_f32_16x16x32_bf16 v[204:207], v[28:31], v[0:3], v[204:207]\n\t" \
    "ds_read_b128 v[200:203], %[l16] offset:40448\n\t" \
    "v_maximum3_f32 v174, v174, v175, v166\n\t" \
    "v_mfma_f32_16x16x32_bf16 v[212:215], v[24:27], v[188:191], v[212:215]\n\t" \
    "ds_read_b128 v[16:19], %[l16] offset:46336\n\t" \
    "s_waitcnt lgkmcnt(4)\n\t" \
    "v_cmp_gt_u32 vcc, v221, v222\n\t" \
    "s_cbranch_vccnz KS0_%=\n\t" \
    "KB0_%=:\n\t" \
    "v_mfma_f32_16x16x32_bf16 v[208:211], v[156:159], v[144:147], v[224:227]\n\t" \
    "v_maximum3_f32 v174, v174, v167, v167\n\t" \
    "v_permlane32_swap_b32 v4, v172\n\t" \
    "v_mfma_f32_16x16x32_bf16 v[20:23], v[168:171], v[148:151], v[224:227]\n\t" \
    "v_permlane32_swap_b32 v6, v174\n\t" \
    "v_mfma_f32_16x16x32_bf16 v[192:195], v[156:159], %[sel], 0\n\t" \
    "v_mfma_f32_16x16x32_bf16 v[184:187], v[28:31], v[188:191], v[184:187]\n\t" \
    "ds_read_b128 v[0:3], %[l16] offset:47360\n\t" \
    "v_mfma_f32_16x16x32_bf16 v[160:163], v[32:35], v[176:179], v[160:163]\n\t" \
    "ds_read_b128 v[24:27], %[l16] offset:53248\n\t" \
    "v_maximum3_f32 v8, v8, v9, v208\n\t" \
    "v_mfma_f32_16x16x32_bf16 v[204:207], v[36:39], v[176:179], v[204:207]\n\t" \
    "ds_read_b128 v[144:147], %[l16] offset:54272\n\t" \
    "s_waitcnt lgkmcnt(5)\n\t" \
    "L_qkv0_18:\n\t" \
    "s_cbranch_execz L_qkv0_19\n\t" \
    "v_mfma_f32_16x16x32_bf16 v[212:215], v[32:35], v[196:199], v[212:215]\n\t" \
    "ds_read_b128 v[164:167], %[l16] offset:60160\n\t" \
    "v_maximum3_f32 v8, v8, v209, v209\n\t" \
    "v_mfma_f32_16x16x32_bf16 v[184:187], v[36:39], v[196:199], v[184:187]\n\t" \
    "ds_read_b128 v[168:171], %[l16] offset:61184\n\t" \
    "v_maximum3_f32 v10, v10, v11, v210\n\t" \
    "v_mfma_f32_16x16x32_bf16 v[160:163], v[40:43], v[12:15], v[160:163]\n\t" \
    "ds_read_b128 v[148:151], %[wr2] offset:4864\n\t" \
    "v_maximum3_f32 v10, v10, v211, v211\n\t" \
    "v_mfma_f32_16x16x32_bf16 v[204:207], v[44:47], v[12:15], v[204:207]\n\t" \
    "v_maximum3_f32 v152, v152, v153, v20\n\t" \
    "s_waitcnt lgkmcnt(6)\n\t" \
    "v_mfma_f32_16x16x32_bf16 v[212:215], v[40:43], v[200:203], v[212:215]\n\t" \
    "ds_read_b128 v[156:159], %[wr2] offset:5888\n\t" \
    "v_maximum3_f32 v152, v152, v21, v21\n\t" \
    "v_mfma_f32_16x16x32_bf16 v[184:187], v[44:47], v[200:203], v[184:187]\n\t" \
    "v_maximum3_f32 v154, v154, v155, v22\n\t" \
    "v_maximum3_f32 v154, v154, v23, v23\n\t" \
    "v_mfma_f32_16x16x32_bf16 v[160:163], v[48:51], v[16:19], v[160:163]\n\t" \
    "ds_read_b128 v[188:191], %[wr2] offset:11776\n\t" \
    "v_permlane32_swap_b32 v8, v152\n\t" \
    "v_mfma_f32_16x16x32_bf16 v[204:207], v[52:55], v[16:19], v[204:207]\n\t" \
    "v_permlane32_swap_b32 v10, v154\n\t" \
    "s_waitcnt lgkmcnt(5)\n\t" \
    "v_mfma_f32_16x16x32_bf16 v[212:215], v[48:51], v[0:3], v[212:215]\n\t" \
    "ds_read_b128 v[28:31], %[wr2] offset:12800\n\t" \
    "v_permlane16_swap_b32 v8, v4\n\t" \
    "v_mfma_f32_16x16x32_bf16 v[184:187], v[52:55], v[0:3], v[184:187]\n\t" \
    "v_permlane16_swap_b32 v152, v172\n\t" \
    "v_permlane16_swap_b32 v10, v6\n\t" \
    "v_mfma_f32_16x16x32_bf16 v[160:163], v[56:59], v[24:27], v[160:163]\n\t" \
    "ds_read_b128 v[176:179], %[wr2] offset:18688\n\t" \
    "L_qkv0_19:\n\t" \
    "s_cbranch_execz L_qkv0_20\n\t" \
    "v_permlane16_swap_b32 v154, v174\n\t" \
    "v_mfma_f32_16x16x32_bf16 v[204:207], v[60:63], v[24:27], v[204:207]\n\t" \
    "v_maximum3_f32 v9, v8, v4, v152\n\t" \
    "v_maximum3_f32 v5, v172, v10, v6\n\t" \
    "v_mfma_f32_16x16x32_bf16 v[212:215], v[56:59], v[144:147], v[212:215]\n\t" \
    "ds_read_b128 v[32:35], %[wr2] offset:19712\n\t" \
    "v_maximum3_f32 v9, v9, v154, v174\n\t" \
    "v_mfma_f32_16x16x32_bf16 v[184:187], v[60:63], v[144:147], v[184:187]\n\t" \
    "v_maximum3_f32 v9, v9, v5, v5\n\t" \
    "s_waitcnt lgkmcnt(4)\n\t" \
    "v_mfma_f32_16x16x32_bf16 v[160:163], v[64:67], v[164:167], v[160:163]\n\t" \
    "ds_read_b128 v[196:199], %[wr2] offset:25600\n\t" \
    "v_mul_f32 v9, s87, v9\n\t" \
    "v_mfma_f32_16x16x32_bf16 v[204:207], v[68:71], v[164:167], v[204:207]\n\t" \
    "ds_read_b128 v[36:39], %[wr2] offset:26624\n\t" \
    "v_fma_f32 v8, v8, s86, v9\n\t" \
    "v_mfma_f32_16x16x32_bf16 v[212:215], v[64:67], v[168:171], v[212:215]\n\t" \
    "ds_read_b128 v[12:15], %[wr2] offset:32512\n\t" \
    "v_fma_f32 v10, v10, s86, v9\n\t" \
    "v_mfma_f32_16x16x32_bf16 v[184:187], v[68:71], v[168:171], v[184:187]\n\t" \
    "ds_read_b128 v[40:43], %[wr2] offset:33536\n\t" \
    "v_fma_f32 v4, v4, s86, v9\n\t" \
    "v_mfma_f32_16x16x32_bf16 v[160:163], v[72:75], v[148:151], v[160:163]\n\t" \
    "ds_read_b128 v[200:203], %[wr2] offset:39424\n\t" \
    "v_fma_f32 v6, v6, s86, v9\n\t" \
    "v_mfma_f32_16x16x32_bf16 v[204:207], v[76:79], v[148:151], v[204:207]\n\t" \
    "ds_read_b128 v[44:47], %[wr2] offset:40448\n\t" \
    "v_fma_f32 v152, v152, s86, v9\n\t" \
    "v_mfma_f32_16x16x32_bf16 v[212:215], v[72:75], v[156:159], v[212:215]\n\t" \
    "ds_read_b128 v[20:23], %[wr2] offset:46336\n\t" \
    "v_fma_f32 v154, v154, s86, v9\n\t" \
    "v_mfma_f32_16x16x32_bf16 v[184:187], v[76:79], v[156:159], v[184:187]\n\t" \
    "L_qkv0_20:\n\t" \
    "s_cbranch_execz L_qkv0_21\n\t" \
    "ds_read_b128 v[16:19], %[wr2] offset:47360\n\t" \
    "s_waitcnt lgkmcnt(8)\n\t" \
    "v_mfma_f32_16x16x32_bf16 v[160:163], v[80:83], v[188:191], v[160:163]\n\t" \
    "ds_read_b128 v[48:51], %[wr2] offset:53248\n\t" \
    "v_fma_f32 v172, v172, s86, v9\n\t" \
    "v_mfma_f32_16x16x32_bf16 v[204:207], v[84:87], v[188:191], v[204:207]\n\t" \
    "ds_read_b128 v[0:3], %[wr2] offset:54272\n\t" \
    "v_fma_f32 v174, v174, s86, v9\n\t" \
    "v_mfma_f32_16x16x32_bf16 v[212:215], v[80:83], v[28:31], v[212:215]\n\t" \
    "ds_read_b128 v[52:55], %[wr2] offset:60160\n\t" \
    "v_cvt_pk_bf16_f32 v218, v192, v193\n\t" \
    "v_mfma_f32_16x16x32_bf16 v[184:187], v[84:87], v[28:31], v[184:187]\n\t" \
    "ds_read_b128 v[24:27], %[wr2] offset:61184\n\t" \
    "v_cvt_pk_bf16_f32 v219, v194, v195\n\t" \
    "v_mfma_f32_16x16x32_bf16 v[160:163], v[88:91], v[176:179], v[160:163]\n\t" \
    "v_exp_f32 v8, v8\n\t" \
    "v_mfma_f32_16x16x32_bf16 v[204:207], v[92:95], v[176:179], v[204:207]\n\t" \
    "v_exp_f32 v10, v10\n\t" \
    "v_mfma_f32_16x16x32_bf16 v[212:215], v[88:91], v[32:35], v[212:215]\n\t" \
    "v_exp_f32 v4, v4\n\t" \
    "v_mfma_f32_16x16x32_bf16 v[184:187], v[92:95], v[32:35], v[184:187]\n\t" \
    "v_add_f32 v9, v8, v10\n\t" \
    "s_waitcnt lgkmcnt(4)\n\t" \
    "v_mfma_f32_16x16x32_bf16 v[204:207], v[100:103], v[196:199], v[204:207]\n\t" \
    "v_exp_f32 v6, v6\n\t" \
    "v_mfma_f32_16x16x32_bf16 v[184:187], v[100:103], v[36:39], v[184:187]\n\t" \
    "v_exp_f32 v152, v152\n\t" \
    "v_mfma_f32_16x16x32_bf16 v[204:207], v[108:111], v[12:15], v[204:207]\n\t" \
    "v_exp_f32 v154, v154\n\t" \
    "v_mfma_f32_16x16x32_bf16 v[184:187], v[108:111], v[40:43], v[184:187]\n\t" \
    "v_exp_f32 v172, v172\n\t" \
    "v_mfma_f32_16x16x32_bf16 v[204:207], v[116:119], v[200:203], v[204:207]\n\t" \
    "L_qkv0_21:\n\t" \
    "s_cbranch_execz L_qkv0_22\n\t" \
    "v_exp_f32 v174, v174\n\t" \
    "v_mfma_f32_16x16x32_bf16 v[184:187], v[116:119], v[44:47], v[184:187]\n\t" \
    "v_add_f32 v5, v4, v6\n\t" \
    "v_add_f32 v153, v152, v154\n\t" \
    "v_mfma_f32_16x16x32_bf16 v[204:207], v[124:127], v[20:23], v[204:207]\n\t" \
    "v_add_f32 v173, v172, v174\n\t" \
    "v_add_f32 v9, v9, v5\n\t" \
    "v_mfma_f32_16x16x32_bf16 v[184:187], v[124:127], v[16:19], v[184:187]\n\t" \
    "v_add_f32 v153, v153, v173\n\t" \
    "s_waitcnt lgkmcnt(0)\n\t" \
    "v_mfma_f32_16x16x32_bf16 v[204:207], v[132:135], v[48:51], v[204:207]\n\t" \
    "v_add_f32 v9, v9, v153\n\t" \
    "v_mfma_f32_16x16x32_bf16 v[184:187], v[132:135], v[0:3], v[184:187]\n\t" \
    "v_rcp_f32 v9, v9\n\t" \
    "v_mfma_f32_16x16x32_bf16 v[204:207], v[140:143], v[52:55], v[204:207]\n\t" \
    "v_mul_f32 v8, v8, v9\n\t" \
    "v_mul_f32 v10, v10, v9\n\t" \
    "v_mfma_f32_16x16x32_bf16 v[184:187], v[140:143], v[24:27], v[184:187]\n\t" \
    "v_mul_f32 v4, v4, v9\n\t" \
    "v_mul_f32 v6, v6, v9\n\t" \
    "v_mfma_f32_16x16x32_bf16 v[160:163], v[96:99], v[196:199], v[160:163]\n\t" \
    "v_mul_f32 v152, v152, v9\n\t" \
    "v_mul_f32 v154, v154, v9\n\t" \
    "v_mfma_f32_16x16x32_bf16 v[160:163], v[104:107], v[12:15], v[160:163]\n\t" \
    "v_cvt_pk_bf16_f32 v204, v204, v205\n\t" \
    "v_cvt_pk_bf16_f32 v205, v206, v207\n\t" \
    "v_mfma_f32_16x16x32_bf16 v[160:163], v[112:115], v[200:203], v[160:163]\n\t" \
    "v_cvt_pk_bf16_f32 v184, v184, v185\n\t" \
    "v_cvt_pk_bf16_f32 v185, v186, v187\n\t" \
    "v_mfma_f32_16x16x32_bf16 v[160:163], v[120:123], v[20:23], v[160:163]\n\t" \
    "s_cmp_eq_u32 %[wave], 3\n\t" \
    "s_cbranch_scc1 PV0_%=\n\t" \
    "L_qkv0_22:\n\t" \
    "s_cbranch_execz L_qkv0_23\n\t" \
    "ds_write_b64 %[sx], v[204:205] offset:3840\n\t" \
    "ds_write_b64 %[sx], v[184:185] offset:4352\n\t" \
    "ds_write_b32 %[fa], v221 offset:20\n\t" \
    "PV0_%=:\n\t" \
    "v_mfma_f32_16x16x32_bf16 v[160:163], v[128:131], v[48:51], v[160:163]\n\t" \
    "v_mul_f32 v172, v172, v9\n\t" \
    "v_mul_f32 v174, v174, v9\n\t" \
    "v_mfma_f32_16x16x32_bf16 v[160:163], v[136:139], v[52:55], v[160:163]\n\t" \
    "v_cvt_pk_bf16_f32 v8, v8, v10\n\t" \
    "v_cvt_pk_bf16_f32 v4, v4, v6\n\t" \
    "v_mfma_f32_16x16x32_bf16 v[212:215], v[96:99], v[36:39], v[212:215]\n\t" \
    "v_cvt_pk_bf16_f32 v152, v152, v154\n\t" \
    "v_cvt_pk_bf16_f32 v172, v172, v174\n\t" \
    "v_mfma_f32_16x16x32_bf16 v[212:215], v[104:107], v[40:43], v[212:215]\n\t" \
    "v_permlane16_swap_b32 v8, v4\n\t" \
    "v_permlane16_swap_b32 v152, v172\n\t" \
    "v_mfma_f32_16x16x32_bf16 v[212:215], v[112:115], v[44:47], v[212:215]\n\t" \
    "ds_read_b32 v222, %[fa] offset:16\n\t" \
    "ds_read_b64 v[146:147], %[sx] offset:1408\n\t" \
    "ds_read_b64 v[58:59], %[sx] offset:1920\n\t" \
    "v_mfma_f32_16x16x32_bf16 v[212:215], v[120:123], v[16:19], v[212:215]\n\t" \
    "v_permlane32_swap_b32 v4, v172\n\t" \
    "v_permlane32_swap_b32 v8, v152\n\t" \
    "v_mfma_f32_16x16x32_bf16 v[212:215], v[128:131], v[0:3], v[212:215]\n\t" \
    "v_perm_b32 v7, v4, v4, v231\n\t" \
    "v_perm_b32 v6, v4, v4, v230\n\t" \
    "v_mfma_f32_16x16x32_bf16 v[212:215], v[136:139], v[24:27], v[212:215]\n\t" \
    "v_perm_b32 v5, v4, v4, v229\n\t" \
    "v_perm_b32 v4, v4, v4, v228\n\t" \
    TSA(9) \
    "v_perm_b32 v11, v8, v8, v231\n\t" \
    "v_perm_b32 v10, v8, v8, v230\n\t" \
    "v_mfma_f32_16x16x32_bf16 v[60:63], v[180:183], v[4:7], 0\n\t" \
    "L_qkv0_23:\n\t" \
    "s_cbranch_execz L_qkv0_24\n\t" \
    "v_perm_b32 v9, v8, v8, v229\n\t" \
    "v_perm_b32 v8, v8, v8, v228\n\t" \
    "v_perm_b32 v175, v172, v172, v231\n\t" \
    "v_perm_b32 v174, v172, v172, v230\n\t" \
    "v_mfma_f32_16x16x32_bf16 v[164:167], v[216:219], v[8:11], 0\n\t" \
    "v_perm_b32 v173, v172, v172, v229\n\t" \
    "v_perm_b32 v172, v172, v172, v228\n\t" \
    "v_perm_b32 v155, v152, v152, v231\n\t" \
    "v_perm_b32 v154, v152, v152, v230\n\t" \
    "v_mfma_f32_16x16x32_bf16 v[64:67], v[180:183], v[172:175], 0\n\t" \
    "v_perm_b32 v153, v152, v152, v229\n\t" \
    "v_perm_b32 v152, v152, v152, v228\n\t" \
    "s_waitcnt lgkmcnt(0)\n\t" \
    "v_cmp_gt_u32 vcc, v221, v222\n\t" \
    "s_cbranch_vccnz VS0_%=\n\t" \
    "VB0_%=:\n\t" \
    "v_mfma_f32_16x16x32_bf16 v[168:171], v[216:219], v[152:155], 0\n\t" \
    "v_cvt_pk_bf16_f32 v206, v160, v161\n\t" \
    "v_cvt_pk_bf16_f32 v207, v162, v163\n\t" \
    "v_cvt_pk_bf16_f32 v144, v160, v161\n\t" \
    "v_cvt_pk_bf16_f32 v145, v162, v163\n\t" \
    "v_mfma_f32_16x16x32_bf16 v[68:71], v[204:207], v[4:7], 0\n\t" \
    "v_cvt_pk_bf16_f32 v186, v212, v213\n\t" \
    "v_mfma_f32_16x16x32_bf16 v[148:151], v[144:147], v[8:11], 0\n\t" \
    "v_cvt_pk_bf16_f32 v187, v214, v215\n\t" \
    "v_cvt_pk_bf16_f32 v56, v212, v213\n\t" \
    "v_cvt_pk_bf16_f32 v57, v214, v215\n\t" \
    "v_mfma_f32_16x16x32_bf16 v[72:75], v[184:187], v[172:175], 0\n\t" \
    "v_cvt_pk_bf16_f32 v60, v60, v61\n\t" \
    "v_mfma_f32_16x16x32_bf16 v[156:159], v[56:59], v[152:155], 0\n\t" \
    "v_cvt_pk_bf16_f32 v61, v66, v67\n\t" \
    "buffer_store_dwordx2 v[60:61], %[ox], %[rs], s81 offen sc1\n\t" \
    "v_cvt_pk_bf16_f32 v164, v164, v165\n\t" \
    "L_qkv0_24:\n\t" \
    "s_cbranch_execz L_qkv0_25\n\t" \
    "v_cvt_pk_bf16_f32 v165, v170, v171\n\t" \
    "buffer_store_dwordx2 v[164:165], %[ox], %[rs], s80 offen sc1\n\t" \
    "v_cvt_pk_bf16_f32 v68, v68, v69\n\t" \
    "v_cvt_pk_bf16_f32 v69, v70, v71\n\t" \
    "v_cvt_pk_bf16_f32 v148, v148, v149\n\t" \
    "v_cvt_pk_bf16_f32 v149, v150, v151\n\t" \
    "v_cvt_pk_bf16_f32 v70, v72, v73\n\t" \
    "v_cvt_pk_bf16_f32 v71, v74, v75\n\t" \
    "buffer_store_dwordx4 v[68:71], %[l16], %[rs], s79 offen sc1\n\t" \
    "v_cvt_pk_bf16_f32 v150, v156, v157\n\t" \
    "v_cvt_pk_bf16_f32 v151, v158, v159\n\t" \
    "buffer_store_dwordx4 v[148:151], %[l16], %[rs], s78 offen sc1\n\t" \
    "L_qkv0_25:\n\t" \
    "s_cbranch_execz L_back\n\t" \
    "s_branch END_%=\n\t" \
    "L_back:\n\t" \
    "s_setpc_b64 s[92:93]\n\t" \
    "WS20_%=:\n\t" \
    "s_mov_b32 s75, %[ls]\n\t" \
    "WV20_%=:\n\t" \
    "ds_read_b32 v223, %[fg] offset:0\n\t" \
    "s_sub_u32 s75, s75, 1\n\t" \
    "s_cbranch_scc1 WV2e0_%=\n\t" \
    "s_waitcnt lgkmcnt(0)\n\t" \
    "v_cmp_gt_u32 vcc, 2, v223\n\t" \
    "s_cbranch_vccnz WV20_%=\n\t" \
    "WV2e0_%=:\n\t" \
    "s_branch WB20_%=\n\t" \
    "WS30_%=:\n\t" \
    "s_mov_b32 s75, %[ls]\n\t" \
    "WV30_%=:\n\t" \
    "ds_read_b32 v223, %[fg] offset:0\n\t" \
    "s_sub_u32 s75, s75, 1\n\t" \
    "s_cbranch_scc1 WV3e0_%=\n\t" \
    "s_waitcnt lgkmcnt(0)\n\t" \
    "v_cmp_gt_u32 vcc, 3, v223\n\t" \
    "s_cbranch_vccnz WV30_%=\n\t" \
    "WV3e0_%=:\n\t" \
    "s_branch WB30_%=\n\t" \
    "WS40_%=:\n\t" \
    "s_mov_b32 s75, %[ls]\n\t" \
    "WV40_%=:\n\t" \
    "ds_read_b32 v223, %[fg] offset:0\n\t" \
    "s_sub_u32 s75, s75, 1\n\t" \
    "s_cbranch_scc1 WV4e0_%=\n\t" \
    "s_waitcnt lgkmcnt(0)\n\t" \
    "v_cmp_gt_u32 vcc, 4, v223\n\t" \
    "s_cbranch_vccnz WV40_%=\n\t" \
    "WV4e0_%=:\n\t" \
    "s_branch WB40_%=\n\t" \
    "WS50_%=:\n\t" \
    "s_mov_b32 s75, %[ls]\n\t" \
    "WV50_%=:\n\t" \
    "ds_read_b32 v223, %[fg] offset:16\n\t" \
    "s_sub_u32 s75, s75, 1\n\t" \
    "s_cbranch_scc1 WV5e0_%=\n\t" \
    "s_waitcnt lgkmcnt(0)\n\t" \
    "v_cmp_gt_u32 vcc, 5, v223\n\t" \
    "s_cbranch_vccnz WV50_%=\n\t" \
    "WV5e0_%=:\n\t" \
    "s_branch WB50_%=\n\t" \
    "KS0_%=:\n\t" \
    "s_nop 3\n\t" \
    "s_cmp_lg_u32 %[first], 0\n\t" \
    "s_cbranch_scc1 KW0_%=\n\t" \
    "v_mov_b32 v156, 0\n\t" \
    "v_mov_b32 v157, 0\n\t" \
    "v_mov_b32 v158, 0\n\t" \
    "v_mov_b32 v159, 0\n\t" \
    "v_mov_b32 v168, 0\n\t" \
    "v_mov_b32 v169, 0\n\t" \
    "v_mov_b32 v170, 0\n\t" \
    "v_mov_b32 v171, 0\n\t" \
    "v_mov_b32 v224, 0xff800000\n\t" \
    "v_mov_b32 v225, 0xff800000\n\t" \
    "v_mov_b32 v226, 0xff800000\n\t" \
    "v_mov_b32 v227, 0xff800000\n\t" \
    "s_nop 1\n\t" \
    "s_branch KB0_%=\n\t" \
    "KW0_%=:\n\t" \
    "s_mov_b32 s75, %[ls]\n\t" \
    "KL0_%=:\n\t" \
    "ds_read_b32 v222, %[fa] offset:0\n\t" \
    "s_sub_u32 s75, s75, 1\n\t" \
    "s_cbranch_scc1 KLe0_%=\n\t" \
    "s_waitcnt lgkmcnt(0)\n\t" \
    "v_cmp_gt_u32 vcc, v221, v222\n\t" \
    "s_cbranch_vccnz KL0_%=\n\t" \
    "KLe0_%=:\n\t" \
    "ds_read2_b64 v[156:159], %[sx] offset0:48 offset1:0\n\t" \
    "ds_read2_b64 v[168:171], %[sx] offset0:112 offset1:0\n\t" \
    "s_waitcnt lgkmcnt(0)\n\t" \
    "s_branch KB0_%=\n\t" \
    "VS0_%=:\n\t" \
    "s_nop 3\n\t" \
    "s_cmp_lg_u32 %[first], 0\n\t" \
    "s_cbranch_scc1 VW0_%=\n\t" \
    "v_mov_b32 v146, 0\n\t" \
    "v_mov_b32 v147, 0\n\t" \
    "v_mov_b32 v58, 0\n\t" \
    "v_mov_b32 v59, 0\n\t" \
    "s_nop 1\n\t" \
    "s_branch VB0_%=\n\t" \
    "VW0_%=:\n\t" \
    "s_mov_b32 s75, %[ls]\n\t" \
    "VL0_%=:\n\t" \
    "ds_read_b32 v222, %[fa] offset:16\n\t" \
    "s_sub_u32 s75, s75, 1\n\t" \
    "s_cbranch_scc1 VLe0_%=\n\t" \
    "s_waitcnt lgkmcnt(0)\n\t" \
    "v_cmp_gt_u32 vcc, v221, v222\n\t" \
    "s_cbranch_vccnz VL0_%=\n\t" \
    "VLe0_%=:\n\t" \
    "ds_read_b64 v[146:147], %[sx] offset:1408\n\t" \
    "ds_read_b64 v[58:59], %[sx] offset:1920\n\t" \
    "s_waitcnt lgkmcnt(0)\n\t" \
    "s_branch VB0_%=\n\t" \
    "L_qkv1_0:\n\t" \
    "s_cbranch_execz L_qkv1_1\n\t" \
    "s_nop 4\n\t" \
    "s_mov_b32 s64, %[ab]\n\t" \
    "buffer_load_dwordx4 v[0:3], %[l16], %[rs], s64 offen offset:0 sc1\n\t" \
    "s_add_u32 s68, %[ab], 32768\n\t" \
    "buffer_load_dwordx4 v[4:7], %[l16], %[rs], s68 offen offset:2048 sc1\n\t" \
    "buffer_load_dwordx4 v[8:11], %[l16], %[rs], s64 offen offset:1024 sc1\n\t" \
    "buffer_load_dwordx4 v[12:15], %[l16], %[rs], s68 offen offset:3072 sc1\n\t" \
    "buffer_load_dwordx4 v[16:19], %[l16], %[rs], s64 offen offset:2048 sc1\n\t" \
    "s_add_u32 s69, %[ab], 36864\n\t" \
    "buffer_load_dwordx4 v[20:23], %[l16], %[rs], s69 offen offset:0 sc1\n\t" \
    "buffer_load_dwordx4 v[24:27], %[l16], %[rs], s64 offen offset:3072 sc1\n\t" \
    "buffer_load_dwordx4 v[28:31], %[l16], %[rs], s69 offen offset:1024 sc1\n\t" \
    "s_add_u32 s65, %[ab], 4096\n\t" \
    "buffer_load_dwordx4 v[32:35], %[l16], %[rs], s65 offen offset:0 sc1\n\t" \
    "buffer_load_dwordx4 v[36:39], %[l16], %[rs], s69 offen offset:2048 sc1\n\t" \
    "buffer_load_dwordx4 v[40:43], %[l16], %[rs], s65 offen offset:1024 sc1\n\t" \
    "buffer_load_dwordx4 v[44:47], %[l16], %[rs], s69 offen offset:3072 sc1\n\t" \
    "s_add_u32 s66, %[ab], 16384\n\t" \
    "s_add_u32 s67, %[ab], 20480\n\t" \
    "s_add_u32 s70, %[ab], 49152\n\t" \
    "s_add_u32 s71, %[ab], 53248\n\t" \
    "s_add_u32 s72, %[ab], 65536\n\t" \
    "v_mov_b32 v221, 2\n\t" \
    "s_mov_b32 s74, %[gs]\n\t" \
    TSA(24) \
    "ds_read_b128 v[184:187], %[l16] offset:0\n\t" \
    "ds_read_b128 v[188:191], %[l16] offset:1024\n\t" \
    "ds_read_b128 v[192:195], %[l16] offset:2048\n\t" \
    "ds_read_b128 v[196:199], %[l16] offset:3072\n\t" \
    "ds_read_b128 v[200:203], %[xrd] offset:0\n\t" \
    "ds_read_b128 v[204:207], %[l16] offset:6912\n\t" \
    "ds_read_b128 v[208:211], %[l16] offset:7936\n\t" \
    "ds_read_b128 v[212:215], %[l16] offset:8960\n\t" \
    "L_qkv1_1:\n\t" \
    "s_cbranch_execz L_qkv1_2\n\t" \
    "s_waitcnt vmcnt(11)\n\t" \
    "v_max3_u32 v220, v0, v1, 0\n\t" \
    "v_max3_u32 v220, v2, v3, v220\n\t" \
    "s_waitcnt vmcnt(10)\n\t" \
    "v_max3_u32 v220, v4, v5, v220\n\t" \
    "v_max3_u32 v220, v6, v7, v220\n\t" \
    "s_waitcnt vmcnt(9)\n\t" \
    "v_max3_u32 v220, v8, v9, v220\n\t" \
    "v_max3_u32 v220, v10, v11, v220\n\t" \
    "s_waitcnt vmcnt(8)\n\t" \
    "v_max3_u32 v220, v12, v13, v220\n\t" \
    "v_max3_u32 v220, v14, v15, v220\n\t" \
    "s_waitcnt vmcnt(7)\n\t" \
    "v_max3_u32 v220, v16, v17, v220\n\t" \
    "v_max3_u32 v220, v18, v19, v220\n\t" \
    "s_waitcnt vmcnt(6)\n\t" \
    "v_max3_u32 v220, v20, v21, v220\n\t" \
    "v_max3_u32 v220, v22, v23, v220\n\t" \
    "v_cmp_eq_u32 vcc, -1, v220\n\t" \
    "s_cbranch_vccnz R01_%=\n\t" \
    "C01_%=:\n\t" \
    TSA(25) \
    "s_waitcnt lgkmcnt(7)\n\t" \
    "v_mfma_f32_16x16x32_bf16 v[164:167], v[184:187], v[4:7], 0\n\t" \
    "ds_read_b128 v[216:219], %[l16] offset:9984\n\t" \
    "buffer_load_dwordx4 v[48:51], %[l16], %[rs], s65 offen offset:2048 sc1\n\t" \
    "v_mfma_f32_16x16x32_bf16 v[144:147], v[184:187], v[0:3], 0\n\t" \
    "s_waitcnt lgkmcnt(7)\n\t" \
    "v_mfma_f32_16x16x32_bf16 v[168:171], v[188:191], v[4:7], 0\n\t" \
    "ds_read_b128 v[184:187], %[xrd] offset:6912\n\t" \
    "v_mfma_f32_16x16x32_bf16 v[148:151], v[188:191], v[0:3], 0\n\t" \
    "s_waitcnt lgkmcnt(7)\n\t" \
    "v_mfma_f32_16x16x32_bf16 v[172:175], v[192:195], v[4:7], 0\n\t" \
    "ds_read_b128 v[188:191], %[l16] offset:13824\n\t" \
    "buffer_load_dwordx4 v[52:55], %[l16], %[rs], s70 offen offset:0 sc1\n\t" \
    "v_mfma_f32_16x16x32_bf16 v[152:155], v[192:195], v[0:3], 0\n\t" \
    "s_waitcnt lgkmcnt(7)\n\t" \
    "v_mfma_f32_16x16x32_bf16 v[176:179], v[196:199], v[4:7], 0\n\t" \
    "ds_read_b128 v[192:195], %[l16] offset:14848\n\t" \
    "L_qkv1_2:\n\t" \
    "s_cbranch_execz L_qkv1_3\n\t" \
    "v_mfma_f32_16x16x32_bf16 v[156:159], v[196:199], v[0:3], 0\n\t" \
    "s_waitcnt lgkmcnt(4)\n\t" \
    "v_mfma_f32_16x16x32_bf16 v[180:183], v[200:203], v[4:7], 0\n\t" \
    "ds_read_b128 v[196:199], %[l16] offset:15872\n\t" \
    "v_mfma_f32_16x16x32_bf16 v[160:163], v[200:203], v[0:3], 0\n\t" \
    "v_mfma_f32_16x16x32_bf16 v[164:167], v[204:207], v[12:15], v[164:167]\n\t" \
    "ds_read_b128 v[200:203], %[l16] offset:16896\n\t" \
    "buffer_load_dwordx4 v[56:59], %[l16], %[rs], s65 offen offset:3072 sc1\n\t" \
    "v_mfma_f32_16x16x32_bf16 v[144:147], v[204:207], v[8:11], v[144:147]\n\t" \
    "v_mfma_f32_16x16x32_bf16 v[168:171], v[208:211], v[12:15], v[168:171]\n\t" \
    "ds_read_b128 v[204:207], %[xrd] offset:13824\n\t" \
    "v_mfma_f32_16x16x32_bf16 v[148:151], v[208:211], v[8:11], v[148:151]\n\t" \
    "v_mfma_f32_16x16x32_bf16 v[172:175], v[212:215], v[12:15], v[172:175]\n\t" \
    "ds_read_b128 v[208:211], %[l16] offset:20736\n\t" \
    "buffer_load_dwordx4 v[60:63], %[l16], %[rs], s70 offen offset:1024 sc1\n\t" \
    "v_mfma_f32_16x16x32_bf16 v[152:155], v[212:215], v[8:11], v[152:155]\n\t" \
    "s_waitcnt lgkmcnt(4)\n\t" \
    "v_mfma_f32_16x16x32_bf16 v[176:179], v[216:219], v[12:15], v[176:179]\n\t" \
    "ds_read_b128 v[212:215], %[l16] offset:21760\n\t" \
    "v_mfma_f32_16x16x32_bf16 v[156:159], v[216:219], v[8:11], v[156:159]\n\t" \
    "s_waitcnt vmcnt(8)\n\t" \
    "v_max3_u32 v220, v24, v25, 0\n\t" \
    "v_mfma_f32_16x16x32_bf16 v[180:183], v[184:187], v[12:15], v[180:183]\n\t" \
    "ds_read_b128 v[216:219], %[l16] offset:22784\n\t" \
    "v_max3_u32 v220, v26, v27, v220\n\t" \
    "v_mfma_f32_16x16x32_bf16 v[160:163], v[184:187], v[8:11], v[160:163]\n\t" \
    "v_max3_u32 v220, v28, v29, v220\n\t" \
    "v_mfma_f32_16x16x32_bf16 v[164:167], v[188:191], v[20:23], v[164:167]\n\t" \
    "ds_read_b128 v[184:187], %[l16] offset:23808\n\t" \
    "buffer_load_dwordx4 v[64:67], %[l16], %[rs], s66 offen offset:0 sc1\n\t" \
    "v_mfma_f32_16x16x32_bf16 v[144:147], v[188:191], v[16:19], v[144:147]\n\t" \
    "v_max3_u32 v220, v30, v31, v220\n\t" \
    "s_waitcnt vmcnt(7)\n\t" \
    "L_qkv1_3:\n\t" \
    "s_cbranch_execz L_qkv1_4\n\t" \
    "v_max3_u32 v220, v32, v33, v220\n\t" \
    "v_mfma_f32_16x16x32_bf16 v[168:171], v[192:195], v[20:23], v[168:171]\n\t" \
    "ds_read_b128 v[188:191], %[xrd] offset:20736\n\t" \
    "v_max3_u32 v220, v34, v35, v220\n\t" \
    "v_mfma_f32_16x16x32_bf16 v[148:151], v[192:195], v[16:19], v[148:151]\n\t" \
    "v_max3_u32 v220, v36, v37, v220\n\t" \
    "s_waitcnt lgkmcnt(4)\n\t" \
    "v_mfma_f32_16x16x32_bf16 v[172:175], v[196:199], v[20:23], v[172:175]\n\t" \
    "ds_read_b128 v[192:195], %[l16] offset:27648\n\t" \
    "buffer_load_dwordx4 v[68:71], %[l16], %[rs], s70 offen offset:2048 sc1\n\t" \
    "v_mfma_f32_16x16x32_bf16 v[152:155], v[196:199], v[16:19], v[152:155]\n\t" \
    "v_max3_u32 v220, v38, v39, v220\n\t" \
    "s_waitcnt vmcnt(6)\n\t" \
    "v_max3_u32 v220, v40, v41, v220\n\t" \
    "v_mfma_f32_16x16x32_bf16 v[176:179], v[200:203], v[20:23], v[176:179]\n\t" \
    "ds_read_b128 v[196:199], %[l16] offset:28672\n\t" \
    "v_max3_u32 v220, v42, v43, v220\n\t" \
    "v_mfma_f32_16x16x32_bf16 v[156:159], v[200:203], v[16:19], v[156:159]\n\t" \
    "v_max3_u32 v220, v44, v45, v220\n\t" \
    "v_mfma_f32_16x16x32_bf16 v[180:183], v[204:207], v[20:23], v[180:183]\n\t" \
    "ds_read_b128 v[200:203], %[l16] offset:29696\n\t" \
    "v_max3_u32 v220, v46, v47, v220\n\t" \
    "v_mfma_f32_16x16x32_bf16 v[160:163], v[204:207], v[16:19], v[160:163]\n\t" \
    "v_cmp_eq_u32 vcc, -1, v220\n\t" \
    "s_cbranch_vccnz R11_%=\n\t" \
    "C11_%=:\n\t" \
    "v_mfma_f32_16x16x32_bf16 v[164:167], v[208:211], v[28:31], v[164:167]\n\t" \
    "ds_read_b128 v[204:207], %[l16] offset:30720\n\t" \
    "buffer_load_dwordx4 v[72:75], %[l16], %[rs], s66 offen offset:1024 sc1\n\t" \
    "v_mfma_f32_16x16x32_bf16 v[144:147], v[208:211], v[24:27], v[144:147]\n\t" \
    "s_waitcnt lgkmcnt(4)\n\t" \
    "v_mfma_f32_16x16x32_bf16 v[168:171], v[212:215], v[28:31], v[168:171]\n\t" \
    "ds_read_b128 v[208:211], %[xrd] offset:27648\n\t" \
    "v_mfma_f32_16x16x32_bf16 v[148:151], v[212:215], v[24:27], v[148:151]\n\t" \
    "L_qkv1_4:\n\t" \
    "s_cbranch_execz L_qkv1_5\n\t" \
    "v_mfma_f32_16x16x32_bf16 v[172:175], v[216:219], v[28:31], v[172:175]\n\t" \
    "ds_read_b128 v[212:215], %[l16] offset:34560\n\t" \
    "buffer_load_dwordx4 v[76:79], %[l16], %[rs], s70 offen offset:3072 sc1\n\t" \
    "v_mfma_f32_16x16x32_bf16 v[152:155], v[216:219], v[24:27], v[152:155]\n\t" \
    "v_mfma_f32_16x16x32_bf16 v[176:179], v[184:187], v[28:31], v[176:179]\n\t" \
    "ds_read_b128 v[216:219], %[l16] offset:35584\n\t" \
    "v_mfma_f32_16x16x32_bf16 v[156:159], v[184:187], v[24:27], v[156:159]\n\t" \
    "v_mfma_f32_16x16x32_bf16 v[180:183], v[188:191], v[28:31], v[180:183]\n\t" \
    "ds_read_b128 v[184:187], %[l16] offset:36608\n\t" \
    "v_mfma_f32_16x16x32_bf16 v[160:163], v[188:191], v[24:27], v[160:163]\n\t" \
    "s_waitcnt lgkmcnt(4)\n\t" \
    "v_mfma_f32_16x16x32_bf16 v[164:167], v[192:195], v[36:39], v[164:167]\n\t" \
    "ds_read_b128 v[188:191], %[l16] offset:37632\n\t" \
    "buffer_load_dwordx4 v[80:83], %[l16], %[rs], s66 offen offset:2048 sc1\n\t" \
    "v_mfma_f32_16x16x32_bf16 v[144:147], v[192:195], v[32:35], v[144:147]\n\t" \
    "v_mfma_f32_16x16x32_bf16 v[168:171], v[196:199], v[36:39], v[168:171]\n\t" \
    "ds_read_b128 v[192:195], %[xrd] offset:34560\n\t" \
    "v_mfma_f32_16x16x32_bf16 v[148:151], v[196:199], v[32:35], v[148:151]\n\t" \
    "v_mfma_f32_16x16x32_bf16 v[172:175], v[200:203], v[36:39], v[172:175]\n\t" \
    "ds_read_b128 v[196:199], %[l16] offset:41472\n\t" \
    "buffer_load_dwordx4 v[84:87], %[l16], %[rs], s71 offen offset:0 sc1\n\t" \
    "v_mfma_f32_16x16x32_bf16 v[152:155], v[200:203], v[32:35], v[152:155]\n\t" \
    "v_mfma_f32_16x16x32_bf16 v[176:179], v[204:207], v[36:39], v[176:179]\n\t" \
    "ds_read_b128 v[200:203], %[l16] offset:42496\n\t" \
    "v_mfma_f32_16x16x32_bf16 v[156:159], v[204:207], v[32:35], v[156:159]\n\t" \
    "s_waitcnt vmcnt(8)\n\t" \
    "v_max3_u32 v220, v48, v49, 0\n\t" \
    "s_waitcnt lgkmcnt(4)\n\t" \
    "v_mfma_f32_16x16x32_bf16 v[180:183], v[208:211], v[36:39], v[180:183]\n\t" \
    "ds_read_b128 v[204:207], %[l16] offset:43520\n\t" \
    "v_max3_u32 v220, v50, v51, v220\n\t" \
    "v_mfma_f32_16x16x32_bf16 v[160:163], v[208:211], v[32:35], v[160:163]\n\t" \
    "v_max3_u32 v220, v52, v53, v220\n\t" \
    "L_qkv1_5:\n\t" \
    "s_cbranch_execz L_qkv1_6\n\t" \
    "v_mfma_f32_16x16x32_bf16 v[164:167], v[212:215], v[44:47], v[164:167]\n\t" \
    "ds_read_b128 v[208:211], %[l16] offset:44544\n\t" \
    "buffer_load_dwordx4 v[88:91], %[l16], %[rs], s66 offen offset:3072 sc1\n\t" \
    "v_mfma_f32_16x16x32_bf16 v[144:147], v[212:215], v[40:43], v[144:147]\n\t" \
    "v_max3_u32 v220, v54, v55, v220\n\t" \
    "s_waitcnt vmcnt(7)\n\t" \
    "v_max3_u32 v220, v56, v57, v220\n\t" \
    "v_mfma_f32_16x16x32_bf16 v[168:171], v[216:219], v[44:47], v[168:171]\n\t" \
    "ds_read_b128 v[212:215], %[xrd] offset:41472\n\t" \
    "v_max3_u32 v220, v58, v59, v220\n\t" \
    "v_mfma_f32_16x16x32_bf16 v[148:151], v[216:219], v[40:43], v[148:151]\n\t" \
    "v_max3_u32 v220, v60, v61, v220\n\t" \
    "v_mfma_f32_16x16x32_bf16 v[172:175], v[184:187], v[44:47], v[172:175]\n\t" \
    "ds_read_b128 v[216:219], %[l16] offset:48384\n\t" \
    "buffer_load_dwordx4 v[92:95], %[l16], %[rs], s71 offen offset:1024 sc1\n\t" \
    "v_mfma_f32_16x16x32_bf16 v[152:155], v[184:187], v[40:43], v[152:155]\n\t" \
    "v_max3_u32 v220, v62, v63, v220\n\t" \
    "s_waitcnt lgkmcnt(4)\n\t" \
    "v_mfma_f32_16x16x32_bf16 v[176:179], v[188:191], v[44:47], v[176:179]\n\t" \
    "ds_read_b128 v[184:187], %[l16] offset:49408\n\t" \
    "s_waitcnt vmcnt(6)\n\t" \
    "v_max3_u32 v220, v64, v65, v220\n\t" \
    "v_mfma_f32_16x16x32_bf16 v[156:159], v[188:191], v[40:43], v[156:159]\n\t" \
    "v_max3_u32 v220, v66, v67, v220\n\t" \
    "v_max3_u32 v220, v68, v69, v220\n\t" \
    "v_mfma_f32_16x16x32_bf16 v[180:183], v[192:195], v[44:47], v[180:183]\n\t" \
    "ds_read_b128 v[188:191], %[l16] offset:50432\n\t" \
    "v_max3_u32 v220, v70, v71, v220\n\t" \
    "v_mfma_f32_16x16x32_bf16 v[160:163], v[192:195], v[40:43], v[160:163]\n\t" \
    "v_cmp_eq_u32 vcc, -1, v220\n\t" \
    "s_cbranch_vccnz R21_%=\n\t" \
    "C21_%=:\n\t" \
    "v_mfma_f32_16x16x32_bf16 v[164:167], v[196:199], v[52:55], v[164:167]\n\t" \
    "ds_read_b128 v[192:195], %[l16] offset:51456\n\t" \
    "L_qkv1_6:\n\t" \
    "s_cbranch_execz L_qkv1_7\n\t" \
    "buffer_load_dwordx4 v[96:99], %[l16], %[rs], s67 offen offset:0 sc1\n\t" \
    "v_mfma_f32_16x16x32_bf16 v[144:147], v[196:199], v[48:51], v[144:147]\n\t" \
    "v_mfma_f32_16x16x32_bf16 v[168:171], v[200:203], v[52:55], v[168:171]\n\t" \
    "ds_read_b128 v[196:199], %[xrd] offset:48384\n\t" \
    "v_mfma_f32_16x16x32_bf16 v[148:151], v[200:203], v[48:51], v[148:151]\n\t" \
    "s_waitcnt lgkmcnt(4)\n\t" \
    "v_mfma_f32_16x16x32_bf16 v[172:175], v[204:207], v[52:55], v[172:175]\n\t" \
    "ds_read_b128 v[200:203], %[l16] offset:55296\n\t" \
    "buffer_load_dwordx4 v[100:103], %[l16], %[rs], s71 offen offset:2048 sc1\n\t" \
    "v_mfma_f32_16x16x32_bf16 v[152:155], v[204:207], v[48:51], v[152:155]\n\t" \
    "v_mfma_f32_16x16x32_bf16 v[176:179], v[208:211], v[52:55], v[176:179]\n\t" \
    "ds_read_b128 v[204:207], %[l16] offset:56320\n\t" \
    "v_mfma_f32_16x16x32_bf16 v[156:159], v[208:211], v[48:51], v[156:159]\n\t" \
    "v_mfma_f32_16x16x32_bf16 v[180:183], v[212:215], v[52:55], v[180:183]\n\t" \
    "ds_read_b128 v[208:211], %[l16] offset:57344\n\t" \
    "v_mfma_f32_16x16x32_bf16 v[160:163], v[212:215], v[48:51], v[160:163]\n\t" \
    "v_mfma_f32_16x16x32_bf16 v[164:167], v[216:219], v[60:63], v[164:167]\n\t" \
    "ds_read_b128 v[212:215], %[l16] offset:58368\n\t" \
    "buffer_load_dwordx4 v[104:107], %[l16], %[rs], s67 offen offset:1024 sc1\n\t" \
    "v_mfma_f32_16x16x32_bf16 v[144:147], v[216:219], v[56:59], v[144:147]\n\t" \
    "s_waitcnt lgkmcnt(4)\n\t" \
    "v_mfma_f32_16x16x32_bf16 v[168:171], v[184:187], v[60:63], v[168:171]\n\t" \
    "ds_read_b128 v[216:219], %[xrd] offset:55296\n\t" \
    "v_mfma_f32_16x16x32_bf16 v[148:151], v[184:187], v[56:59], v[148:151]\n\t" \
    "v_mfma_f32_16x16x32_bf16 v[172:175], v[188:191], v[60:63], v[172:175]\n\t" \
    "ds_read_b128 v[184:187], %[wr2] offset:0\n\t" \
    "buffer_load_dwordx4 v[108:111], %[l16], %[rs], s71 offen offset:3072 sc1\n\t" \
    "v_mfma_f32_16x16x32_bf16 v[152:155], v[188:191], v[56:59], v[152:155]\n\t" \
    "L_qkv1_7:\n\t" \
    "s_cbranch_execz L_qkv1_8\n\t" \
    "v_mfma_f32_16x16x32_bf16 v[176:179], v[192:195], v[60:63], v[176:179]\n\t" \
    "ds_read_b128 v[188:191], %[wr2] offset:1024\n\t" \
    "v_mfma_f32_16x16x32_bf16 v[156:159], v[192:195], v[56:59], v[156:159]\n\t" \
    "s_waitcnt vmcnt(8)\n\t" \
    "v_max3_u32 v220, v72, v73, 0\n\t" \
    "v_mfma_f32_16x16x32_bf16 v[180:183], v[196:199], v[60:63], v[180:183]\n\t" \
    "ds_read_b128 v[192:195], %[wr2] offset:2048\n\t" \
    "v_max3_u32 v220, v74, v75, v220\n\t" \
    "v_mfma_f32_16x16x32_bf16 v[160:163], v[196:199], v[56:59], v[160:163]\n\t" \
    "v_max3_u32 v220, v76, v77, v220\n\t" \
    "s_waitcnt lgkmcnt(4)\n\t" \
    "v_mfma_f32_16x16x32_bf16 v[164:167], v[200:203], v[68:71], v[164:167]\n\t" \
    "ds_read_b128 v[196:199], %[wr2] offset:3072\n\t" \
    "buffer_load_dwordx4 v[112:115], %[l16], %[rs], s67 offen offset:2048 sc1\n\t" \
    "v_mfma_f32_16x16x32_bf16 v[144:147], v[200:203], v[64:67], v[144:147]\n\t" \
    "v_max3_u32 v220, v78, v79, v220\n\t" \
    "s_waitcnt vmcnt(7)\n\t" \
    "v_max3_u32 v220, v80, v81, v220\n\t" \
    "v_mfma_f32_16x16x32_bf16 v[168:171], v[204:207], v[68:71], v[168:171]\n\t" \
    "ds_read_b128 v[200:203], %[xr2] offset:0\n\t" \
    "v_max3_u32 v220, v82, v83, v220\n\t" \
    "v_mfma_f32_16x16x32_bf16 v[148:151], v[204:207], v[64:67], v[148:151]\n\t" \
    "v_max3_u32 v220, v84, v85, v220\n\t" \
    "v_mfma_f32_16x16x32_bf16 v[172:175], v[208:211], v[68:71], v[172:175]\n\t" \
    "ds_read_b128 v[204:207], %[wr2] offset:6912\n\t" \
    "buffer_load_dwordx4 v[116:119], %[l16], %[rs], s72 offen offset:0 sc1\n\t" \
    "v_mfma_f32_16x16x32_bf16 v[152:155], v[208:211], v[64:67], v[152:155]\n\t" \
    "v_max3_u32 v220, v86, v87, v220\n\t" \
    "s_waitcnt vmcnt(6)\n\t" \
    "v_max3_u32 v220, v88, v89, v220\n\t" \
    "v_mfma_f32_16x16x32_bf16 v[176:179], v[212:215], v[68:71], v[176:179]\n\t" \
    "ds_read_b128 v[208:211], %[wr2] offset:7936\n\t" \
    "v_max3_u32 v220, v90, v91, v220\n\t" \
    "L_qkv1_8:\n\t" \
    "s_cbranch_execz L_back\n\t" \
    "v_mfma_f32_16x16x32_bf16 v[156:159], v[212:215], v[64:67], v[156:159]\n\t" \
    "v_max3_u32 v220, v92, v93, v220\n\t" \
    "s_waitcnt lgkmcnt(4)\n\t" \
    "v_mfma_f32_16x16x32_bf16 v[180:183], v[216:219], v[68:71], v[180:183]\n\t" \
    "ds_read_b128 v[212:215], %[wr2] offset:8960\n\t" \
    "v_max3_u32 v220, v94, v95, v220\n\t" \
    "v_mfma_f32_16x16x32_bf16 v[160:163], v[216:219], v[64:67], v[160:163]\n\t" \
    "v_cmp_eq_u32 vcc, -1, v220\n\t" \
    "s_cbranch_vccnz R31_%=\n\t" \
    "C31_%=:\n\t" \
    "v_mfma_f32_16x16x32_bf16 v[164:167], v[184:187], v[76:79], v[164:167]\n\t" \
    "ds_read_b128 v[216:219], %[wr2] offset:9984\n\t" \
    "buffer_load_dwordx4 v[120:123], %[l16], %[rs], s67 offen offset:3072 sc1\n\t" \
    "v_mfma_f32_16x16x32_bf16 v[144:147], v[184:187], v[72:75], v[144:147]\n\t" \
    "s_xor_b32 s81, %[sl], %[f2]\n\t" \
    "v_mfma_f32_16x16x32_bf16 v[168:171], v[188:191], v[76:79], v[168:171]\n\t" \
    "ds_read_b128 v[184:187], %[xr2] offset:6912\n\t" \
    "s_add_u32 s78, %[po], s81\n\t" \
    "v_mfma_f32_16x16x32_bf16 v[148:151], v[188:191], v[72:75], v[148:151]\n\t" \
    "v_mfma_f32_16x16x32_bf16 v[172:175], v[192:195], v[76:79], v[172:175]\n\t" \
    "ds_read_b128 v[188:191], %[wr2] offset:13824\n\t" \
    "buffer_load_dwordx4 v[124:127], %[l16], %[rs], s72 offen offset:1024 sc1\n\t" \
    "v_mfma_f32_16x16x32_bf16 v[152:155], v[192:195], v[72:75], v[152:155]\n\t" \
    "s_add_u32 s79, %[p1], s81\n\t" \
    "s_waitcnt lgkmcnt(4)\n\t" \
    "v_mfma_f32_16x16x32_bf16 v[176:179], v[196:199], v[76:79], v[176:179]\n\t" \
    "ds_read_b128 v[192:195], %[wr2] offset:14848\n\t" \
    "s_add_u32 s80, %[xo], s81\n\t" \
    "v_mfma_f32_16x16x32_bf16 v[156:159], v[196:199], v[72:75], v[156:159]\n\t" \
    "L_qkv1_9:\n\t" \
    "s_cbranch_execz L_qkv1_10\n\t" \
    "v_mfma_f32_16x16x32_bf16 v[180:183], v[200:203], v[76:79], v[180:183]\n\t" \
    "ds_read_b128 v[196:199], %[wr2] offset:15872\n\t" \
    "s_add_u32 s81, s80, 34816\n\t" \
    "v_mfma_f32_16x16x32_bf16 v[160:163], v[200:203], v[72:75], v[160:163]\n\t" \
    "v_mfma_f32_16x16x32_bf16 v[164:167], v[204:207], v[84:87], v[164:167]\n\t" \
    "ds_read_b128 v[200:203], %[wr2] offset:16896\n\t" \
    "buffer_load_dwordx4 v[128:131], %[l16], %[rs], s68 offen offset:0 sc1\n\t" \
    "v_mfma_f32_16x16x32_bf16 v[144:147], v[204:207], v[80:83], v[144:147]\n\t" \
    "s_mov_b64 s[82:83], 0xffff\n\t" \
    "v_mfma_f32_16x16x32_bf16 v[168:171], v[208:211], v[84:87], v[168:171]\n\t" \
    "ds_read_b128 v[204:207], %[xr2] offset:13824\n\t" \
    "s_mov_b32 s84, 0xffff0000\n\t" \
    "v_mfma_f32_16x16x32_bf16 v[148:151], v[208:211], v[80:83], v[148:151]\n\t" \
    "s_waitcnt lgkmcnt(4)\n\t" \
    "v_mfma_f32_16x16x32_bf16 v[172:175], v[212:215], v[84:87], v[172:175]\n\t" \
    "ds_read_b128 v[208:211], %[wr2] offset:22784\n\t" \
    "buffer_load_dwordx4 v[132:135], %[l16], %[rs], s72 offen offset:2048 sc1\n\t" \
    "v_mfma_f32_16x16x32_bf16 v[152:155], v[212:215], v[80:83], v[152:155]\n\t" \
    "s_mov_b32 s85, 0\n\t" \
    "v_mfma_f32_16x16x32_bf16 v[176:179], v[216:219], v[84:87], v[176:179]\n\t" \
    "ds_read_b128 v[212:215], %[wr2] offset:23808\n\t" \
    "s_mov_b32 s86, 0x3eae1a93\n\t" \
    "v_mfma_f32_16x16x32_bf16 v[156:159], v[216:219], v[80:83], v[156:159]\n\t" \
    "s_waitcnt vmcnt(8)\n\t" \
    "v_max3_u32 v220, v96, v97, 0\n\t" \
    "v_mfma_f32_16x16x32_bf16 v[180:183], v[184:187], v[84:87], v[180:183]\n\t" \
    "ds_read_b128 v[216:219], %[xr2] offset:20736\n\t" \
    "v_max3_u32 v220, v98, v99, v220\n\t" \
    "v_mfma_f32_16x16x32_bf16 v[160:163], v[184:187], v[80:83], v[160:163]\n\t" \
    "s_mov_b32 s87, 0xbeae1a93\n\t" \
    "v_max3_u32 v220, v100, v101, v220\n\t" \
    "v_mfma_f32_16x16x32_bf16 v[164:167], v[188:191], v[92:95], v[164:167]\n\t" \
    "L_qkv1_10:\n\t" \
    "s_cbranch_execz L_qkv1_11\n\t" \
    "ds_read_b128 v[184:187], %[wr2] offset:29696\n\t" \
    "buffer_load_dwordx4 v[136:139], %[l16], %[rs], s68 offen offset:1024 sc1\n\t" \
    "v_mfma_f32_16x16x32_bf16 v[144:147], v[188:191], v[88:91], v[144:147]\n\t" \
    "v_max3_u32 v220, v102, v103, v220\n\t" \
    "s_waitcnt lgkmcnt(4)\n\t" \
    "v_mfma_f32_16x16x32_bf16 v[168:171], v[192:195], v[92:95], v[168:171]\n\t" \
    "ds_read_b128 v[188:191], %[wr2] offset:30720\n\t" \
    "s_waitcnt vmcnt(7)\n\t" \
    "v_max3_u32 v220, v104, v105, v220\n\t" \
    "v_mfma_f32_16x16x32_bf16 v[148:151], v[192:195], v[88:91], v[148:151]\n\t" \
    "v_max3_u32 v220, v106, v107, v220\n\t" \
    "v_max3_u32 v220, v108, v109, v220\n\t" \
    "v_mfma_f32_16x16x32_bf16 v[172:175], v[196:199], v[92:95], v[172:175]\n\t" \
    "ds_read_b128 v[192:195], %[xr2] offset:27648\n\t" \
    "buffer_load_dwordx4 v[140:143], %[l16], %[rs], s72 offen offset:3072 sc1\n\t" \
    "v_mfma_f32_16x16x32_bf16 v[152:155], v[196:199], v[88:91], v[152:155]\n\t" \
    "v_max3_u32 v220, v110, v111, v220\n\t" \
    "s_waitcnt vmcnt(6)\n\t" \
    "v_max3_u32 v220, v112, v113, v220\n\t" \
    "v_mfma_f32_16x16x32_bf16 v[176:179], v[200:203], v[92:95], v[176:179]\n\t" \
    "ds_read_b128 v[196:199], %[wr2] offset:36608\n\t" \
    "v_max3_u32 v220, v114, v115, v220\n\t" \
    "v_mfma_f32_16x16x32_bf16 v[156:159], v[200:203], v[88:91], v[156:159]\n\t" \
    "v_max3_u32 v220, v116, v117, v220\n\t" \
    "v_mfma_f32_16x16x32_bf16 v[180:183], v[204:207], v[92:95], v[180:183]\n\t" \
    "ds_read_b128 v[200:203], %[wr2] offset:37632\n\t" \
    "v_max3_u32 v220, v118, v119, v220\n\t" \
    "v_mfma_f32_16x16x32_bf16 v[160:163], v[204:207], v[88:91], v[160:163]\n\t" \
    "v_cmp_eq_u32 vcc, -1, v220\n\t" \
    "s_cbranch_vccnz R41_%=\n\t" \
    "C41_%=:\n\t" \
    "s_waitcnt lgkmcnt(4)\n\t" \
    "v_mfma_f32_16x16x32_bf16 v[172:175], v[208:211], v[100:103], v[172:175]\n\t" \
    "ds_read_b128 v[204:207], %[xr2] offset:34560\n\t" \
    "v_mfma_f32_16x16x32_bf16 v[152:155], v[208:211], v[96:99], v[152:155]\n\t" \
    "L_qkv1_11:\n\t" \
    "s_cbranch_execz L_qkv1_12\n\t" \
    "v_mfma_f32_16x16x32_bf16 v[176:179], v[212:215], v[100:103], v[176:179]\n\t" \
    "ds_read_b128 v[208:211], %[wr2] offset:43520\n\t" \
    "v_mfma_f32_16x16x32_bf16 v[156:159], v[212:215], v[96:99], v[156:159]\n\t" \
    "v_mfma_f32_16x16x32_bf16 v[180:183], v[216:219], v[100:103], v[180:183]\n\t" \
    "ds_read_b128 v[212:215], %[wr2] offset:44544\n\t" \
    "v_mfma_f32_16x16x32_bf16 v[160:163], v[216:219], v[96:99], v[160:163]\n\t" \
    "s_waitcnt vmcnt(4)\n\t" \
    "v_max3_u32 v220, v120, v121, 0\n\t" \
    "v_mfma_f32_16x16x32_bf16 v[172:175], v[184:187], v[108:111], v[172:175]\n\t" \
    "ds_read_b128 v[216:219], %[xr2] offset:41472\n\t" \
    "v_max3_u32 v220, v122, v123, v220\n\t" \
    "v_mfma_f32_16x16x32_bf16 v[152:155], v[184:187], v[104:107], v[152:155]\n\t" \
    "v_max3_u32 v220, v124, v125, v220\n\t" \
    "s_waitcnt lgkmcnt(4)\n\t" \
    "v_mfma_f32_16x16x32_bf16 v[176:179], v[188:191], v[108:111], v[176:179]\n\t" \
    "ds_read_b128 v[184:187], %[wr2] offset:50432\n\t" \
    "v_max3_u32 v220, v126, v127, v220\n\t" \
    "v_mfma_f32_16x16x32_bf16 v[156:159], v[188:191], v[104:107], v[156:159]\n\t" \
    "s_waitcnt vmcnt(2)\n\t" \
    "v_max3_u32 v220, v128, v129, v220\n\t" \
    "v_mfma_f32_16x16x32_bf16 v[180:183], v[192:195], v[108:111], v[180:183]\n\t" \
    "ds_read_b128 v[188:191], %[wr2] offset:51456\n\t" \
    "v_max3_u32 v220, v130, v131, v220\n\t" \
    "v_mfma_f32_16x16x32_bf16 v[160:163], v[192:195], v[104:107], v[160:163]\n\t" \
    "v_max3_u32 v220, v132, v133, v220\n\t" \
    "v_mfma_f32_16x16x32_bf16 v[172:175], v[196:199], v[116:119], v[172:175]\n\t" \
    "ds_read_b128 v[192:195], %[xr2] offset:48384\n\t" \
    "v_max3_u32 v220, v134, v135, v220\n\t" \
    "v_mfma_f32_16x16x32_bf16 v[152:155], v[196:199], v[112:115], v[152:155]\n\t" \
    "s_waitcnt vmcnt(0)\n\t" \
    "v_max3_u32 v220, v136, v137, v220\n\t" \
    "v_mfma_f32_16x16x32_bf16 v[176:179], v[200:203], v[116:119], v[176:179]\n\t" \
    "ds_read_b128 v[196:199], %[wr2] offset:57344\n\t" \
    "L_qkv1_12:\n\t" \
    "s_cbranch_execz L_qkv1_13\n\t" \
    "v_max3_u32 v220, v138, v139, v220\n\t" \
    "v_mfma_f32_16x16x32_bf16 v[156:159], v[200:203], v[112:115], v[156:159]\n\t" \
    "v_max3_u32 v220, v140, v141, v220\n\t" \
    "s_waitcnt lgkmcnt(4)\n\t" \
    "v_mfma_f32_16x16x32_bf16 v[180:183], v[204:207], v[116:119], v[180:183]\n\t" \
    "ds_read_b128 v[200:203], %[wr2] offset:58368\n\t" \
    "v_max3_u32 v220, v142, v143, v220\n\t" \
    "v_mfma_f32_16x16x32_bf16 v[160:163], v[204:207], v[112:115], v[160:163]\n\t" \
    "v_cmp_eq_u32 vcc, -1, v220\n\t" \
    "s_cbranch_vccnz R51_%=\n\t" \
    "C51_%=:\n\t" \
    "v_mfma_f32_16x16x32_bf16 v[172:175], v[208:211], v[124:127], v[172:175]\n\t" \
    "ds_read_b128 v[204:207], %[xr2] offset:55296\n\t" \
    "v_mfma_f32_16x16x32_bf16 v[152:155], v[208:211], v[120:123], v[152:155]\n\t" \
    "v_mfma_f32_16x16x32_bf16 v[176:179], v[212:215], v[124:127], v[176:179]\n\t" \
    "ds_read_b128 v[208:211], %[wr2] offset:20736\n\t" \
    "v_mfma_f32_16x16x32_bf16 v[156:159], v[212:215], v[120:123], v[156:159]\n\t" \
    "v_mfma_f32_16x16x32_bf16 v[180:183], v[216:219], v[124:127], v[180:183]\n\t" \
    "ds_read_b128 v[212:215], %[wr2] offset:27648\n\t" \
    "v_mfma_f32_16x16x32_bf16 v[160:163], v[216:219], v[120:123], v[160:163]\n\t" \
    "s_waitcnt lgkmcnt(4)\n\t" \
    "v_mfma_f32_16x16x32_bf16 v[172:175], v[184:187], v[132:135], v[172:175]\n\t" \
    "ds_read_b128 v[216:219], %[wr2] offset:34560\n\t" \
    "v_mfma_f32_16x16x32_bf16 v[152:155], v[184:187], v[128:131], v[152:155]\n\t" \
    "v_mfma_f32_16x16x32_bf16 v[176:179], v[188:191], v[132:135], v[176:179]\n\t" \
    "ds_read_b128 v[184:187], %[wr2] offset:41472\n\t" \
    "v_mfma_f32_16x16x32_bf16 v[156:159], v[188:191], v[128:131], v[156:159]\n\t" \
    "v_mfma_f32_16x16x32_bf16 v[180:183], v[192:195], v[132:135], v[180:183]\n\t" \
    "L_qkv1_13:\n\t" \
    "s_cbranch_execz L_qkv1_14\n\t" \
    "v_mfma_f32_16x16x32_bf16 v[160:163], v[192:195], v[128:131], v[160:163]\n\t" \
    "v_mfma_f32_16x16x32_bf16 v[172:175], v[196:199], v[140:143], v[172:175]\n\t" \
    "v_mfma_f32_16x16x32_bf16 v[152:155], v[196:199], v[136:139], v[152:155]\n\t" \
    "s_waitcnt lgkmcnt(2)\n\t" \
    "v_mfma_f32_16x16x32_bf16 v[176:179], v[200:203], v[140:143], v[176:179]\n\t" \
    "v_mfma_f32_16x16x32_bf16 v[156:159], v[200:203], v[136:139], v[156:159]\n\t" \
    "v_mfma_f32_16x16x32_bf16 v[180:183], v[204:207], v[140:143], v[180:183]\n\t" \
    "v_mfma_f32_16x16x32_bf16 v[160:163], v[204:207], v[136:139], v[160:163]\n\t" \
    "ds_read_b128 v[188:191], %[wr2] offset:48384\n\t" \
    "v_mfma_f32_16x16x32_bf16 v[164:167], v[208:211], v[100:103], v[164:167]\n\t" \
    "v_cvt_pk_bf16_f32 v172, v172, v173\n\t" \
    "v_cvt_pk_bf16_f32 v173, v174, v175\n\t" \
    "v_mfma_f32_16x16x32_bf16 v[144:147], v[208:211], v[96:99], v[144:147]\n\t" \
    "ds_read_b128 v[192:195], %[wr2] offset:55296\n\t" \
    "v_cvt_pk_bf16_f32 v176, v176, v177\n\t" \
    "v_mfma_f32_16x16x32_bf16 v[164:167], v[212:215], v[108:111], v[164:167]\n\t" \
    "v_cvt_pk_bf16_f32 v177, v178, v179\n\t" \
    "v_cvt_pk_bf16_f32 v174, v180, v181\n\t" \
    "v_mfma_f32_16x16x32_bf16 v[144:147], v[212:215], v[104:107], v[144:147]\n\t" \
    "ds_read_b128 v[196:199], %[wr2] offset:21760\n\t" \
    "s_waitcnt lgkmcnt(3)\n\t" \
    "v_mfma_f32_16x16x32_bf16 v[164:167], v[216:219], v[116:119], v[164:167]\n\t" \
    "v_cvt_pk_bf16_f32 v175, v182, v183\n\t" \
    "s_cmp_eq_u32 %[wave], 3\n\t" \
    "s_cbranch_scc1 PK1_%=\n\t" \
    "ds_write_b64 %[sx], v[174:175] offset:2432\n\t" \
    "ds_write_b64 %[sx], v[172:173] offset:2816\n\t" \
    "ds_write_b64 %[sx], v[176:177] offset:3328\n\t" \
    "ds_write_b32 %[fa], v221 offset:4\n\t" \
    "PK1_%=:\n\t" \
    "v_mfma_f32_16x16x32_bf16 v[144:147], v[216:219], v[112:115], v[144:147]\n\t" \
    "ds_read_b128 v[200:203], %[wr2] offset:28672\n\t" \
    "v_cvt_pk_bf16_f32 v178, v180, v181\n\t" \
    "v_mfma_f32_16x16x32_bf16 v[164:167], v[184:187], v[124:127], v[164:167]\n\t" \
    "L_qkv1_14:\n\t" \
    "s_cbranch_execz L_qkv1_15\n\t" \
    "v_cvt_pk_bf16_f32 v179, v182, v183\n\t" \
    "v_cvt_pk_bf16_f32 v152, v152, v153\n\t" \
    "v_mfma_f32_16x16x32_bf16 v[144:147], v[184:187], v[120:123], v[144:147]\n\t" \
    "ds_read_b128 v[204:207], %[wr2] offset:35584\n\t" \
    "s_waitcnt lgkmcnt(4)\n\t" \
    "v_mfma_f32_16x16x32_bf16 v[164:167], v[188:191], v[132:135], v[164:167]\n\t" \
    "v_cvt_pk_bf16_f32 v153, v154, v155\n\t" \
    "v_cvt_pk_bf16_f32 v156, v156, v157\n\t" \
    "v_mfma_f32_16x16x32_bf16 v[144:147], v[188:191], v[128:131], v[144:147]\n\t" \
    "ds_read_b128 v[208:211], %[wr2] offset:42496\n\t" \
    "s_waitcnt lgkmcnt(4)\n\t" \
    "v_mfma_f32_16x16x32_bf16 v[164:167], v[192:195], v[140:143], v[164:167]\n\t" \
    "v_cvt_pk_bf16_f32 v157, v158, v159\n\t" \
    "v_cvt_pk_bf16_f32 v154, v160, v161\n\t" \
    "v_mfma_f32_16x16x32_bf16 v[144:147], v[192:195], v[136:139], v[144:147]\n\t" \
    "ds_read_b128 v[212:215], %[wr2] offset:49408\n\t" \
    "s_waitcnt lgkmcnt(4)\n\t" \
    "v_mfma_f32_16x16x32_bf16 v[168:171], v[196:199], v[100:103], v[168:171]\n\t" \
    "v_cvt_pk_bf16_f32 v155, v162, v163\n\t" \
    "v_cvt_pk_bf16_f32 v158, v160, v161\n\t" \
    "v_mfma_f32_16x16x32_bf16 v[148:151], v[196:199], v[96:99], v[148:151]\n\t" \
    "ds_read_b128 v[216:219], %[wr2] offset:56320\n\t" \
    "s_waitcnt lgkmcnt(4)\n\t" \
    "v_mfma_f32_16x16x32_bf16 v[168:171], v[200:203], v[108:111], v[168:171]\n\t" \
    "s_movk_i32 s65, 4096\n\t" \
    "v_cvt_pk_bf16_f32 v159, v162, v163\n\t" \
    "v_mfma_f32_16x16x32_bf16 v[180:183], v[172:175], %[sel], 0\n\t" \
    "v_cvt_pk_bf16_f32 v164, v164, v165\n\t" \
    "v_cvt_pk_bf16_f32 v165, v166, v167\n\t" \
    "v_mfma_f32_16x16x32_bf16 v[148:151], v[200:203], v[104:107], v[148:151]\n\t" \
    "ds_read_b128 v[184:187], %[l16] offset:4864\n\t" \
    "s_waitcnt lgkmcnt(4)\n\t" \
    "v_mfma_f32_16x16x32_bf16 v[168:171], v[204:207], v[116:119], v[168:171]\n\t" \
    "s_movk_i32 s66, 8192\n\t" \
    "L_qkv1_15:\n\t" \
    "s_cbranch_execz L_qkv1_16\n\t" \
    "v_mov_b32 v166, 0\n\t" \
    "v_mfma_f32_16x16x32_bf16 v[148:151], v[204:207], v[112:115], v[148:151]\n\t" \
    "ds_read_b128 v[188:191], %[l16] offset:5888\n\t" \
    "s_waitcnt lgkmcnt(4)\n\t" \
    "v_mfma_f32_16x16x32_bf16 v[168:171], v[208:211], v[124:127], v[168:171]\n\t" \
    "s_movk_i32 s67, 12288\n\t" \
    "v_cndmask_b32_e64 v167, 0, v174, s[82:83]\n\t" \
    "v_mfma_f32_16x16x32_bf16 v[148:151], v[208:211], v[120:123], v[148:151]\n\t" \
    "ds_read_b128 v[192:195], %[l16] offset:11776\n\t" \
    "s_waitcnt lgkmcnt(4)\n\t" \
    "v_mfma_f32_16x16x32_bf16 v[168:171], v[212:215], v[132:135], v[168:171]\n\t" \
    "s_movk_i32 s68, 16384\n\t" \
    "v_cvt_pk_bf16_f32 v144, v144, v145\n\t" \
    "v_mfma_f32_16x16x32_bf16 v[148:151], v[212:215], v[128:131], v[148:151]\n\t" \
    "ds_read_b128 v[196:199], %[l16] offset:12800\n\t" \
    "s_waitcnt lgkmcnt(4)\n\t" \
    "v_mfma_f32_16x16x32_bf16 v[168:171], v[216:219], v[140:143], v[168:171]\n\t" \
    "v_cvt_pk_bf16_f32 v145, v146, v147\n\t" \
    "v_mov_b32 v146, 0\n\t" \
    "v_mfma_f32_16x16x32_bf16 v[148:151], v[216:219], v[136:139], v[148:151]\n\t" \
    TSA(26) \
    "ds_read_b128 v[160:163], %[l16] offset:18688\n\t" \
    "s_waitcnt lgkmcnt(4)\n\t" \
    "v_mfma_f32_16x16x32_bf16 v[200:203], v[0:3], v[184:187], 0\n\t" \
    "v_cndmask_b32_e64 v147, 0, v154, s[82:83]\n\t" \
    "v_cvt_pk_bf16_f32 v180, v180, v181\n\t" \
    "v_mfma_f32_16x16x32_bf16 v[204:207], v[4:7], v[184:187], 0\n\t" \
    "ds_read_b128 v[208:211], %[l16] offset:19712\n\t" \
    "s_waitcnt lgkmcnt(4)\n\t" \
    "v_mfma_f32_16x16x32_bf16 v[212:215], v[0:3], v[188:191], 0\n\t" \
    "v_cvt_pk_bf16_f32 v181, v182, v183\n\t" \
    "v_cvt_pk_bf16_f32 v168, v168, v169\n\t" \
    "v_mfma_f32_16x16x32_bf16 v[216:219], v[152:155], %[sel], 0\n\t" \
    "v_cvt_pk_bf16_f32 v169, v170, v171\n\t" \
    "v_mov_b32 v170, 0\n\t" \
    "L_qkv1_16:\n\t" \
    "s_cbranch_execz L_qkv1_17\n\t" \
    "v_mfma_f32_16x16x32_bf16 v[184:187], v[4:7], v[188:191], 0\n\t" \
    "ds_read_b128 v[0:3], %[l16] offset:25600\n\t" \
    "s_waitcnt lgkmcnt(4)\n\t" \
    "v_mfma_f32_16x16x32_bf16 v[200:203], v[8:11], v[192:195], v[200:203]\n\t" \
    "v_cndmask_b32_e64 v171, 0, v174, s[84:85]\n\t" \
    "v_cvt_pk_bf16_f32 v148, v148, v149\n\t" \
    "v_mfma_f32_16x16x32_bf16 v[204:207], v[12:15], v[192:195], v[204:207]\n\t" \
    "ds_read_b128 v[188:191], %[l16] offset:26624\n\t" \
    "s_waitcnt lgkmcnt(4)\n\t" \
    "v_mfma_f32_16x16x32_bf16 v[212:215], v[8:11], v[196:199], v[212:215]\n\t" \
    "v_cvt_pk_bf16_f32 v182, v216, v217\n\t" \
    "v_cvt_pk_bf16_f32 v183, v218, v219\n\t" \
    "v_mfma_f32_16x16x32_bf16 v[4:7], v[172:175], v[164:167], %[mc]\n\t" \
    "v_cvt_pk_bf16_f32 v216, v216, v217\n\t" \
    "v_cvt_pk_bf16_f32 v217, v218, v219\n\t" \
    "v_mfma_f32_16x16x32_bf16 v[192:195], v[152:155], v[164:167], v[224:227]\n\t" \
    "v_cvt_pk_bf16_f32 v149, v150, v151\n\t" \
    "v_mov_b32 v150, 0\n\t" \
    "v_mfma_f32_16x16x32_bf16 v[8:11], v[152:155], v[144:147], %[mc]\n\t" \
    "v_cndmask_b32_e64 v151, 0, v154, s[84:85]\n\t" \
    "v_mfma_f32_16x16x32_bf16 v[172:175], v[176:179], v[168:171], %[mc]\n\t" \
    "v_mfma_f32_16x16x32_bf16 v[164:167], v[156:159], v[168:171], v[224:227]\n\t" \
    "v_mfma_f32_16x16x32_bf16 v[152:155], v[156:159], v[148:151], %[mc]\n\t" \
    "v_mfma_f32_16x16x32_bf16 v[184:187], v[12:15], v[196:199], v[184:187]\n\t" \
    "ds_read_b128 v[176:179], %[l16] offset:32512\n\t" \
    "s_waitcnt lgkmcnt(1)\n\t" \
    "v_mfma_f32_16x16x32_bf16 v[200:203], v[16:19], v[160:163], v[200:203]\n\t" \
    "ds_read_b32 v222, %[fa]\n\t" \
    "ds_read2_b64 v[156:159], %[sx] offset0:48 offset1:0\n\t" \
    "ds_read2_b64 v[168:171], %[sx] offset0:112 offset1:0\n\t" \
    "v_mfma_f32_16x16x32_bf16 v[204:207], v[20:23], v[160:163], v[204:207]\n\t" \
    "ds_read_b128 v[196:199], %[l16] offset:33536\n\t" \
    "v_maximum3_f32 v4, v4, v5, v192\n\t" \
    "L_qkv1_17:\n\t" \
    "s_cbranch_execz L_back\n\t" \
    "v_mfma_f32_16x16x32_bf16 v[212:215], v[16:19], v[208:211], v[212:215]\n\t" \
    "v_maximum3_f32 v4, v4, v193, v193\n\t" \
    "v_maximum3_f32 v6, v6, v7, v194\n\t" \
    "v_mfma_f32_16x16x32_bf16 v[184:187], v[20:23], v[208:211], v[184:187]\n\t" \
    "ds_read_b128 v[12:15], %[l16] offset:39424\n\t" \
    "v_maximum3_f32 v6, v6, v195, v195\n\t" \
    "v_mfma_f32_16x16x32_bf16 v[200:203], v[24:27], v[0:3], v[200:203]\n\t" \
    "v_maximum3_f32 v172, v172, v173, v164\n\t" \
    "v_maximum3_f32 v172, v172, v165, v165\n\t" \
    "v_mfma_f32_16x16x32_bf16 v[204:207], v[28:31], v[0:3], v[204:207]\n\t" \
    "ds_read_b128 v[160:163], %[l16] offset:40448\n\t" \
    "v_maximum3_f32 v174, v174, v175, v166\n\t" \
    "v_mfma_f32_16x16x32_bf16 v[212:215], v[24:27], v[188:191], v[212:215]\n\t" \
    "buffer_load_dwordx4 v[24:27], %[l16], %[r9], 0 offen offset:0 sc1\n\t" \
    "s_waitcnt lgkmcnt(3)\n\t" \
    "v_cmp_gt_u32 vcc, v221, v222\n\t" \
    "s_cbranch_vccnz KS1_%=\n\t" \
    "KB1_%=:\n\t" \
    "v_mfma_f32_16x16x32_bf16 v[16:19], v[156:159], v[144:147], v[224:227]\n\t" \
    "v_maximum3_f32 v174, v174, v167, v167\n\t" \
    "v_permlane32_swap_b32 v4, v172\n\t" \
    "v_mfma_f32_16x16x32_bf16 v[208:211], v[168:171], v[148:151], v[224:227]\n\t" \
    "v_permlane32_swap_b32 v6, v174\n\t" \
    "v_mfma_f32_16x16x32_bf16 v[20:23], v[156:159], %[sel], 0\n\t" \
    "v_mfma_f32_16x16x32_bf16 v[184:187], v[28:31], v[188:191], v[184:187]\n\t" \
    "buffer_load_dwordx4 v[28:31], %[l16], %[r9], 0 offen offset:1024 sc1\n\t" \
    "ds_read_b128 v[192:195], %[l16] offset:46336\n\t" \
    "v_mfma_f32_16x16x32_bf16 v[200:203], v[32:35], v[176:179], v[200:203]\n\t" \
    "ds_read_b128 v[0:3], %[l16] offset:47360\n\t" \
    "v_maximum3_f32 v8, v8, v9, v16\n\t" \
    "v_mfma_f32_16x16x32_bf16 v[204:207], v[36:39], v[176:179], v[204:207]\n\t" \
    "ds_read_b128 v[144:147], %[l16] offset:53248\n\t" \
    "s_waitcnt lgkmcnt(4)\n\t" \
    "v_mfma_f32_16x16x32_bf16 v[212:215], v[32:35], v[196:199], v[212:215]\n\t" \
    "L_qkv1_18:\n\t" \
    "s_cbranch_execz L_qkv1_19\n\t" \
    "buffer_load_dwordx4 v[32:35], %[l16], %[r9], 0 offen offset:2048 sc1\n\t" \
    "ds_read_b128 v[164:167], %[l16] offset:54272\n\t" \
    "v_mfma_f32_16x16x32_bf16 v[184:187], v[36:39], v[196:199], v[184:187]\n\t" \
    "buffer_load_dwordx4 v[36:39], %[l16], %[r9], 0 offen offset:3072 sc1\n\t" \
    "ds_read_b128 v[168:171], %[l16] offset:60160\n\t" \
    "v_mfma_f32_16x16x32_bf16 v[200:203], v[40:43], v[12:15], v[200:203]\n\t" \
    "ds_read_b128 v[148:151], %[l16] offset:61184\n\t" \
    "v_maximum3_f32 v8, v8, v17, v17\n\t" \
    "v_mfma_f32_16x16x32_bf16 v[204:207], v[44:47], v[12:15], v[204:207]\n\t" \
    "ds_read_b128 v[156:159], %[wr2] offset:4864\n\t" \
    "s_waitcnt lgkmcnt(7)\n\t" \
    "v_mfma_f32_16x16x32_bf16 v[212:215], v[40:43], v[160:163], v[212:215]\n\t" \
    "buffer_load_dwordx4 v[40:43], %[l16], %[r9], s65 offen offset:0 sc1\n\t" \
    "ds_read_b128 v[188:191], %[wr2] offset:5888\n\t" \
    "v_mfma_f32_16x16x32_bf16 v[184:187], v[44:47], v[160:163], v[184:187]\n\t" \
    "buffer_load_dwordx4 v[44:47], %[l16], %[r9], s65 offen offset:1024 sc1\n\t" \
    "s_waitcnt lgkmcnt(7)\n\t" \
    "v_mfma_f32_16x16x32_bf16 v[200:203], v[48:51], v[192:195], v[200:203]\n\t" \
    "ds_read_b128 v[176:179], %[wr2] offset:11776\n\t" \
    "v_maximum3_f32 v10, v10, v11, v18\n\t" \
    "v_mfma_f32_16x16x32_bf16 v[204:207], v[52:55], v[192:195], v[204:207]\n\t" \
    "v_maximum3_f32 v10, v10, v19, v19\n\t" \
    "s_waitcnt lgkmcnt(6)\n\t" \
    "v_mfma_f32_16x16x32_bf16 v[212:215], v[48:51], v[0:3], v[212:215]\n\t" \
    "buffer_load_dwordx4 v[48:51], %[l16], %[r9], s65 offen offset:2048 sc1\n\t" \
    "ds_read_b128 v[196:199], %[wr2] offset:12800\n\t" \
    "v_mfma_f32_16x16x32_bf16 v[184:187], v[52:55], v[0:3], v[184:187]\n\t" \
    "buffer_load_dwordx4 v[52:55], %[l16], %[r9], s65 offen offset:3072 sc1\n\t" \
    "v_maximum3_f32 v152, v152, v153, v208\n\t" \
    "v_mfma_f32_16x16x32_bf16 v[200:203], v[56:59], v[144:147], v[200:203]\n\t" \
    "ds_read_b128 v[12:15], %[wr2] offset:18688\n\t" \
    "v_maximum3_f32 v152, v152, v209, v209\n\t" \
    "v_mfma_f32_16x16x32_bf16 v[204:207], v[60:63], v[144:147], v[204:207]\n\t" \
    "L_qkv1_19:\n\t" \
    "s_cbranch_execz L_qkv1_20\n\t" \
    "v_maximum3_f32 v154, v154, v155, v210\n\t" \
    "s_waitcnt lgkmcnt(4)\n\t" \
    "v_mfma_f32_16x16x32_bf16 v[212:215], v[56:59], v[164:167], v[212:215]\n\t" \
    "buffer_load_dwordx4 v[56:59], %[l16], %[r9], s66 offen offset:0 sc1\n\t" \
    "ds_read_b128 v[160:163], %[wr2] offset:19712\n\t" \
    "v_mfma_f32_16x16x32_bf16 v[184:187], v[60:63], v[164:167], v[184:187]\n\t" \
    "buffer_load_dwordx4 v[60:63], %[l16], %[r9], s66 offen offset:1024 sc1\n\t" \
    "v_maximum3_f32 v154, v154, v211, v211\n\t" \
    "v_mfma_f32_16x16x32_bf16 v[200:203], v[64:67], v[168:171], v[200:203]\n\t" \
    "ds_read_b128 v[192:195], %[wr2] offset:25600\n\t" \
    "v_permlane32_swap_b32 v8, v152\n\t" \
    "v_mfma_f32_16x16x32_bf16 v[204:207], v[68:71], v[168:171], v[204:207]\n\t" \
    "ds_read_b128 v[0:3], %[wr2] offset:26624\n\t" \
    "v_permlane32_swap_b32 v10, v154\n\t" \
    "v_mfma_f32_16x16x32_bf16 v[212:215], v[64:67], v[148:151], v[212:215]\n\t" \
    "buffer_load_dwordx4 v[64:67], %[l16], %[r9], s66 offen offset:2048 sc1\n\t" \
    "ds_read_b128 v[144:147], %[wr2] offset:32512\n\t" \
    "v_mfma_f32_16x16x32_bf16 v[184:187], v[68:71], v[148:151], v[184:187]\n\t" \
    "buffer_load_dwordx4 v[68:71], %[l16], %[r9], s66 offen offset:3072 sc1\n\t" \
    "ds_read_b128 v[164:167], %[wr2] offset:33536\n\t" \
    "v_mfma_f32_16x16x32_bf16 v[200:203], v[72:75], v[156:159], v[200:203]\n\t" \
    "ds_read_b128 v[208:211], %[wr2] offset:39424\n\t" \
    "v_permlane16_swap_b32 v8, v4\n\t" \
    "v_mfma_f32_16x16x32_bf16 v[204:207], v[76:79], v[156:159], v[204:207]\n\t" \
    "ds_read_b128 v[168:171], %[wr2] offset:40448\n\t" \
    "s_waitcnt lgkmcnt(7)\n\t" \
    "v_mfma_f32_16x16x32_bf16 v[212:215], v[72:75], v[188:191], v[212:215]\n\t" \
    "buffer_load_dwordx4 v[72:75], %[l16], %[r9], s67 offen offset:0 sc1\n\t" \
    "ds_read_b128 v[148:151], %[wr2] offset:46336\n\t" \
    "v_mfma_f32_16x16x32_bf16 v[184:187], v[76:79], v[188:191], v[184:187]\n\t" \
    "buffer_load_dwordx4 v[76:79], %[l16], %[r9], s67 offen offset:1024 sc1\n\t" \
    "ds_read_b128 v[156:159], %[wr2] offset:47360\n\t" \
    "L_qkv1_20:\n\t" \
    "s_cbranch_execz L_qkv1_21\n\t" \
    "v_mfma_f32_16x16x32_bf16 v[200:203], v[80:83], v[176:179], v[200:203]\n\t" \
    "ds_read_b128 v[188:191], %[wr2] offset:53248\n\t" \
    "v_permlane16_swap_b32 v152, v172\n\t" \
    "v_mfma_f32_16x16x32_bf16 v[204:207], v[84:87], v[176:179], v[204:207]\n\t" \
    "ds_read_b128 v[176:179], %[wr2] offset:54272\n\t" \
    "v_permlane16_swap_b32 v10, v6\n\t" \
    "v_mfma_f32_16x16x32_bf16 v[212:215], v[80:83], v[196:199], v[212:215]\n\t" \
    "buffer_load_dwordx4 v[80:83], %[l16], %[r9], s67 offen offset:2048 sc1\n\t" \
    "v_permlane16_swap_b32 v154, v174\n\t" \
    "v_mfma_f32_16x16x32_bf16 v[184:187], v[84:87], v[196:199], v[184:187]\n\t" \
    "buffer_load_dwordx4 v[84:87], %[l16], %[r9], s67 offen offset:3072 sc1\n\t" \
    "ds_read_b128 v[196:199], %[wr2] offset:60160\n\t" \
    "v_mfma_f32_16x16x32_bf16 v[200:203], v[88:91], v[12:15], v[200:203]\n\t" \
    "v_maximum3_f32 v9, v8, v4, v152\n\t" \
    "v_maximum3_f32 v5, v172, v10, v6\n\t" \
    "v_mfma_f32_16x16x32_bf16 v[204:207], v[92:95], v[12:15], v[204:207]\n\t" \
    "ds_read_b128 v[12:15], %[wr2] offset:61184\n\t" \
    "s_waitcnt lgkmcnt(6)\n\t" \
    "v_mfma_f32_16x16x32_bf16 v[212:215], v[88:91], v[160:163], v[212:215]\n\t" \
    "buffer_load_dwordx4 v[88:91], %[l16], %[r9], s68 offen offset:0 sc1\n\t" \
    "v_maximum3_f32 v9, v9, v154, v174\n\t" \
    "v_mfma_f32_16x16x32_bf16 v[184:187], v[92:95], v[160:163], v[184:187]\n\t" \
    "buffer_load_dwordx4 v[92:95], %[l16], %[r9], s68 offen offset:1024 sc1\n\t" \
    "v_maximum3_f32 v9, v9, v5, v5\n\t" \
    "v_mfma_f32_16x16x32_bf16 v[204:207], v[100:103], v[192:195], v[204:207]\n\t" \
    "v_mul_f32 v9, s87, v9\n\t" \
    "v_fma_f32 v8, v8, s86, v9\n\t" \
    "v_mfma_f32_16x16x32_bf16 v[184:187], v[100:103], v[0:3], v[184:187]\n\t" \
    "v_fma_f32 v10, v10, s86, v9\n\t" \
    "v_fma_f32 v4, v4, s86, v9\n\t" \
    "v_mfma_f32_16x16x32_bf16 v[204:207], v[108:111], v[144:147], v[204:207]\n\t" \
    "v_fma_f32 v6, v6, s86, v9\n\t" \
    "L_qkv1_21:\n\t" \
    "s_cbranch_execz L_qkv1_22\n\t" \
    "v_fma_f32 v152, v152, s86, v9\n\t" \
    "v_mfma_f32_16x16x32_bf16 v[184:187], v[108:111], v[164:167], v[184:187]\n\t" \
    "v_fma_f32 v154, v154, s86, v9\n\t" \
    "v_fma_f32 v172, v172, s86, v9\n\t" \
    "v_mfma_f32_16x16x32_bf16 v[204:207], v[116:119], v[208:211], v[204:207]\n\t" \
    "v_fma_f32 v174, v174, s86, v9\n\t" \
    "v_cvt_pk_bf16_f32 v218, v20, v21\n\t" \
    "v_mfma_f32_16x16x32_bf16 v[184:187], v[116:119], v[168:171], v[184:187]\n\t" \
    "v_cvt_pk_bf16_f32 v219, v22, v23\n\t" \
    "s_waitcnt lgkmcnt(0)\n\t" \
    "v_mfma_f32_16x16x32_bf16 v[204:207], v[124:127], v[148:151], v[204:207]\n\t" \
    "v_exp_f32 v8, v8\n\t" \
    "v_mfma_f32_16x16x32_bf16 v[184:187], v[124:127], v[156:159], v[184:187]\n\t" \
    "v_exp_f32 v10, v10\n\t" \
    "v_mfma_f32_16x16x32_bf16 v[204:207], v[132:135], v[188:191], v[204:207]\n\t" \
    "v_exp_f32 v4, v4\n\t" \
    "v_mfma_f32_16x16x32_bf16 v[184:187], v[132:135], v[176:179], v[184:187]\n\t" \
    "v_exp_f32 v6, v6\n\t" \
    "v_mfma_f32_16x16x32_bf16 v[204:207], v[140:143], v[196:199], v[204:207]\n\t" \
    "v_exp_f32 v152, v152\n\t" \
    "v_mfma_f32_16x16x32_bf16 v[184:187], v[140:143], v[12:15], v[184:187]\n\t" \
    "v_exp_f32 v154, v154\n\t" \
    "v_mfma_f32_16x16x32_bf16 v[200:203], v[96:99], v[192:195], v[200:203]\n\t" \
    "v_exp_f32 v172, v172\n\t" \
    "v_mfma_f32_16x16x32_bf16 v[200:203], v[104:107], v[144:147], v[200:203]\n\t" \
    "v_exp_f32 v174, v174\n\t" \
    "v_mfma_f32_16x16x32_bf16 v[200:203], v[112:115], v[208:211], v[200:203]\n\t" \
    "v_cvt_pk_bf16_f32 v204, v204, v205\n\t" \
    "v_cvt_pk_bf16_f32 v205, v206, v207\n\t" \
    "v_mfma_f32_16x16x32_bf16 v[200:203], v[120:123], v[148:151], v[200:203]\n\t" \
    "v_cvt_pk_bf16_f32 v184, v184, v185\n\t" \
    "v_cvt_pk_bf16_f32 v185, v186, v187\n\t" \
    "L_qkv1_22:\n\t" \
    "s_cbranch_execz L_qkv1_23\n\t" \
    "v_mfma_f32_16x16x32_bf16 v[200:203], v[128:131], v[188:191], v[200:203]\n\t" \
    "s_cmp_eq_u32 %[wave], 3\n\t" \
    "s_cbranch_scc1 PV1_%=\n\t" \
    "ds_write_b64 %[sx], v[204:205] offset:3840\n\t" \
    "ds_write_b64 %[sx], v[184:185] offset:4352\n\t" \
    "ds_write_b32 %[fa], v221 offset:20\n\t" \
    "PV1_%=:\n\t" \
    "v_mfma_f32_16x16x32_bf16 v[200:203], v[136:139], v[196:199], v[200:203]\n\t" \
    "v_add_f32 v9, v8, v10\n\t" \
    "v_add_f32 v5, v4, v6\n\t" \
    "v_mfma_f32_16x16x32_bf16 v[212:215], v[96:99], v[0:3], v[212:215]\n\t" \
    "v_add_f32 v153, v152, v154\n\t" \
    "v_add_f32 v173, v172, v174\n\t" \
    "v_mfma_f32_16x16x32_bf16 v[212:215], v[104:107], v[164:167], v[212:215]\n\t" \
    "v_add_f32 v9, v9, v5\n\t" \
    "v_add_f32 v153, v153, v173\n\t" \
    "v_mfma_f32_16x16x32_bf16 v[212:215], v[112:115], v[168:171], v[212:215]\n\t" \
    "v_add_f32 v9, v9, v153\n\t" \
    "ds_read_b32 v222, %[fa] offset:16\n\t" \
    "ds_read_b64 v[102:103], %[sx] offset:1408\n\t" \
    "ds_read_b64 v[162:163], %[sx] offset:1920\n\t" \
    "v_mfma_f32_16x16x32_bf16 v[212:215], v[120:123], v[156:159], v[212:215]\n\t" \
    "v_rcp_f32 v9, v9\n\t" \
    "v_mfma_f32_16x16x32_bf16 v[212:215], v[128:131], v[176:179], v[212:215]\n\t" \
    "v_mul_f32 v8, v8, v9\n\t" \
    "v_mul_f32 v10, v10, v9\n\t" \
    "v_mfma_f32_16x16x32_bf16 v[212:215], v[136:139], v[12:15], v[212:215]\n\t" \
    "v_mul_f32 v4, v4, v9\n\t" \
    "v_mul_f32 v6, v6, v9\n\t" \
    TSA(29) \
    "v_mul_f32 v152, v152, v9\n\t" \
    "v_mul_f32 v154, v154, v9\n\t" \
    "v_mul_f32 v172, v172, v9\n\t" \
    "v_mul_f32 v174, v174, v9\n\t" \
    "L_qkv1_23:\n\t" \
    "s_cbranch_execz L_qkv1_24\n\t" \
    "v_cvt_pk_bf16_f32 v8, v8, v10\n\t" \
    "v_cvt_pk_bf16_f32 v4, v4, v6\n\t" \
    "v_cvt_pk_bf16_f32 v152, v152, v154\n\t" \
    "v_cvt_pk_bf16_f32 v172, v172, v174\n\t" \
    "v_permlane16_swap_b32 v8, v4\n\t" \
    "s_waitcnt lgkmcnt(0)\n\t" \
    "v_cmp_gt_u32 vcc, v221, v222\n\t" \
    "s_cbranch_vccnz VS1_%=\n\t" \
    "VB1_%=:\n\t" \
    "v_permlane16_swap_b32 v152, v172\n\t" \
    "v_cvt_pk_bf16_f32 v206, v200, v201\n\t" \
    "v_cvt_pk_bf16_f32 v207, v202, v203\n\t" \
    "v_permlane32_swap_b32 v4, v172\n\t" \
    "v_permlane32_swap_b32 v8, v152\n\t" \
    "v_perm_b32 v7, v4, v4, v231\n\t" \
    "v_perm_b32 v6, v4, v4, v230\n\t" \
    "v_perm_b32 v5, v4, v4, v229\n\t" \
    "v_perm_b32 v4, v4, v4, v228\n\t" \
    "v_perm_b32 v11, v8, v8, v231\n\t" \
    "v_perm_b32 v10, v8, v8, v230\n\t" \
    "v_mfma_f32_16x16x32_bf16 v[108:111], v[180:183], v[4:7], 0\n\t" \
    "v_perm_b32 v9, v8, v8, v229\n\t" \
    "v_perm_b32 v8, v8, v8, v228\n\t" \
    "v_perm_b32 v175, v172, v172, v231\n\t" \
    "v_perm_b32 v174, v172, v172, v230\n\t" \
    "v_mfma_f32_16x16x32_bf16 v[116:119], v[216:219], v[8:11], 0\n\t" \
    "v_perm_b32 v173, v172, v172, v229\n\t" \
    "v_perm_b32 v172, v172, v172, v228\n\t" \
    "v_perm_b32 v155, v152, v152, v231\n\t" \
    "v_perm_b32 v154, v152, v152, v230\n\t" \
    "v_mfma_f32_16x16x32_bf16 v[20:23], v[180:183], v[172:175], 0\n\t" \
    "v_perm_b32 v153, v152, v152, v229\n\t" \
    "v_perm_b32 v152, v152, v152, v228\n\t" \
    "L_qkv1_24:\n\t" \
    "s_cbranch_execz L_qkv1_25\n\t" \
    "v_mfma_f32_16x16x32_bf16 v[132:135], v[204:207], v[4:7], 0\n\t" \
    "v_cvt_pk_bf16_f32 v100, v200, v201\n\t" \
    "v_mfma_f32_16x16x32_bf16 v[124:127], v[216:219], v[152:155], 0\n\t" \
    "v_cvt_pk_bf16_f32 v101, v202, v203\n\t" \
    "v_cvt_pk_bf16_f32 v186, v212, v213\n\t" \
    "v_cvt_pk_bf16_f32 v187, v214, v215\n\t" \
    "v_mfma_f32_16x16x32_bf16 v[140:143], v[100:103], v[8:11], 0\n\t" \
    "v_cvt_pk_bf16_f32 v160, v212, v213\n\t" \
    "v_mfma_f32_16x16x32_bf16 v[192:195], v[184:187], v[172:175], 0\n\t" \
    "v_cvt_pk_bf16_f32 v161, v214, v215\n\t" \
    "v_cvt_pk_bf16_f32 v108, v108, v109\n\t" \
    "v_cvt_pk_bf16_f32 v109, v22, v23\n\t" \
    "v_mfma_f32_16x16x32_bf16 v[144:147], v[160:163], v[152:155], 0\n\t" \
    "buffer_store_dwordx2 v[108:109], %[ox], %[rs], s81 offen sc1\n\t" \
    "v_cvt_pk_bf16_f32 v116, v116, v117\n\t" \
    "v_cvt_pk_bf16_f32 v117, v126, v127\n\t" \
    "buffer_store_dwordx2 v[116:117], %[ox], %[rs], s80 offen sc1\n\t" \
    "v_cvt_pk_bf16_f32 v132, v132, v133\n\t" \
    "v_cvt_pk_bf16_f32 v133, v134, v135\n\t" \
    "v_cvt_pk_bf16_f32 v140, v140, v141\n\t" \
    "v_cvt_pk_bf16_f32 v141, v142, v143\n\t" \
    "v_cvt_pk_bf16_f32 v134, v192, v193\n\t" \
    "v_cvt_pk_bf16_f32 v135, v194, v195\n\t" \
    "buffer_store_dwordx4 v[132:135], %[l16], %[rs], s79 offen sc1\n\t" \
    "v_cvt_pk_bf16_f32 v142, v144, v145\n\t" \
    "v_cvt_pk_bf16_f32 v143, v146, v147\n\t" \
    "buffer_store_dwordx4 v[140:143], %[l16], %[rs], s78 offen sc1\n\t" \
    "L_qkv1_25:\n\t" \
    "s_cbranch_execz L_back\n\t" \
    "s_branch END_%=\n\t" \
    "R01_%=:\n\t" \
    "s_cmp_eq_u32 s74, 0\n\t" \
    "s_cbranch_scc1 C01_%=\n\t" \
    "s_sub_u32 s74, s74, 1\n\t" \
    "s_sleep 2\n\t" \
    "buffer_load_dwordx4 v[0:3], %[l16], %[rs], s64 offen offset:0 sc1\n\t" \
    "buffer_load_dwordx4 v[4:7], %[l16], %[rs], s68 offen offset:2048 sc1\n\t" \
    "buffer_load_dwordx4 v[8:11], %[l16], %[rs], s64 offen offset:1024 sc1\n\t" \
    "buffer_load_dwordx4 v[12:15], %[l16], %[rs], s68 offen offset:3072 sc1\n\t" \
    "buffer_load_dwordx4 v[16:19], %[l16], %[rs], s64 offen offset:2048 sc1\n\t" \
    "buffer_load_dwordx4 v[20:23], %[l16], %[rs], s69 offen offset:0 sc1\n\t" \
    "buffer_load_dwordx4 v[24:27], %[l16], %[rs], s64 offen offset:3072 sc1\n\t" \
    "buffer_load_dwordx4 v[28:31], %[l16], %[rs], s69 offen offset:1024 sc1\n\t" \
    "buffer_load_dwordx4 v[32:35], %[l16], %[rs], s65 offen offset:0 sc1\n\t" \
    "buffer_load_dwordx4 v[36:39], %[l16], %[rs], s69 offen offset:2048 sc1\n\t" \
    "buffer_load_dwordx4 v[40:43], %[l16], %[rs], s65 offen offset:1024 sc1\n\t" \
    "buffer_load_dwordx4 v[44:47], %[l16], %[rs], s69 offen offset:3072 sc1\n\t" \
    "s_waitcnt vmcnt(6)\n\t" \
    "v_max3_u32 v220, v0, v1, 0\n\t" \
    "v_max3_u32 v220, v2, v3, v220\n\t" \
    "v_max3_u32 v220, v4, v5, v220\n\t" \
    "v_max3_u32 v220, v6, v7, v220\n\t" \
    "v_max3_u32 v220, v8, v9, v220\n\t" \
    "v_max3_u32 v220, v10, v11, v220\n\t" \
    "v_max3_u32 v220, v12, v13, v220\n\t" \
    "v_max3_u32 v220, v14, v15, v220\n\t" \
    "v_max3_u32 v220, v16, v17, v220\n\t" \
    "v_max3_u32 v220, v18, v19, v220\n\t" \
    "v_max3_u32 v220, v20, v21, v220\n\t" \
    "v_max3_u32 v220, v22, v23, v220\n\t" \
    "v_cmp_eq_u32 vcc, -1, v220\n\t" \
    "s_cbranch_vccnz R01_%=\n\t" \
    "s_branch C01_%=\n\t" \
    "R11_%=:\n\t" \
    "s_cmp_eq_u32 s74, 0\n\t" \
    "s_cbranch_scc1 C11_%=\n\t" \
    "s_sub_u32 s74, s74, 1\n\t" \
    "s_sleep 2\n\t" \
    "buffer_load_dwordx4 v[24:27], %[l16], %[rs], s64 offen offset:3072 sc1\n\t" \
    "buffer_load_dwordx4 v[28:31], %[l16], %[rs], s69 offen offset:1024 sc1\n\t" \
    "buffer_load_dwordx4 v[32:35], %[l16], %[rs], s65 offen offset:0 sc1\n\t" \
    "buffer_load_dwordx4 v[36:39], %[l16], %[rs], s69 offen offset:2048 sc1\n\t" \
    "buffer_load_dwordx4 v[40:43], %[l16], %[rs], s65 offen offset:1024 sc1\n\t" \
    "buffer_load_dwordx4 v[44:47], %[l16], %[rs], s69 offen offset:3072 sc1\n\t" \
    "buffer_load_dwordx4 v[48:51], %[l16], %[rs], s65 offen offset:2048 sc1\n\t" \
    "buffer_load_dwordx4 v[52:55], %[l16], %[rs], s70 offen offset:0 sc1\n\t" \
    "buffer_load_dwordx4 v[56:59], %[l16], %[rs], s65 offen offset:3072 sc1\n\t" \
    "buffer_load_dwordx4 v[60:63], %[l16], %[rs], s70 offen offset:1024 sc1\n\t" \
    "buffer_load_dwordx4 v[64:67], %[l16], %[rs], s66 offen offset:0 sc1\n\t" \
    "buffer_load_dwordx4 v[68:71], %[l16], %[rs], s70 offen offset:2048 sc1\n\t" \
    "s_waitcnt vmcnt(6)\n\t" \
    "v_max3_u32 v220, v24, v25, 0\n\t" \
    "v_max3_u32 v220, v26, v27, v220\n\t" \
    "v_max3_u32 v220, v28, v29, v220\n\t" \
    "v_max3_u32 v220, v30, v31, v220\n\t" \
    "v_max3_u32 v220, v32, v33, v220\n\t" \
    "v_max3_u32 v220, v34, v35, v220\n\t" \
    "v_max3_u32 v220, v36, v37, v220\n\t" \
    "v_max3_u32 v220, v38, v39, v220\n\t" \
    "v_max3_u32 v220, v40, v41, v220\n\t" \
    "v_max3_u32 v220, v42, v43, v220\n\t" \
    "v_max3_u32 v220, v44, v45, v220\n\t" \
    "v_max3_u32 v220, v46, v47, v220\n\t" \
    "v_cmp_eq_u32 vcc, -1, v220\n\t" \
    "s_cbranch_vccnz R11_%=\n\t" \
    "s_branch C11_%=\n\t" \
    "R21_%=:\n\t" \
    "s_cmp_eq_u32 s74, 0\n\t" \
    "s_cbranch_scc1 C21_%=\n\t" \
    "s_sub_u32 s74, s74, 1\n\t" \
    "s_sleep 2\n\t" \
    "buffer_load_dwordx4 v[48:51], %[l16], %[rs], s65 offen offset:2048 sc1\n\t" \
    "buffer_load_dwordx4 v[52:55], %[l16], %[rs], s70 offen offset:0 sc1\n\t" \
    "buffer_load_dwordx4 v[56:59], %[l16], %[rs], s65 offen offset:3072 sc1\n\t" \
    "buffer_load_dwordx4 v[60:63], %[l16], %[rs], s70 offen offset:1024 sc1\n\t" \
    "buffer_load_dwordx4 v[64:67], %[l16], %[rs], s66 offen offset:0 sc1\n\t" \
    "buffer_load_dwordx4 v[68:71], %[l16], %[rs], s70 offen offset:2048 sc1\n\t" \
    "buffer_load_dwordx4 v[72:75], %[l16], %[rs], s66 offen offset:1024 sc1\n\t" \
    "buffer_load_dwordx4 v[76:79], %[l16], %[rs], s70 offen offset:3072 sc1\n\t" \
    "buffer_load_dwordx4 v[80:83], %[l16], %[rs], s66 offen offset:2048 sc1\n\t" \
    "buffer_load_dwordx4 v[84:87], %[l16], %[rs], s71 offen offset:0 sc1\n\t" \
    "buffer_load_dwordx4 v[88:91], %[l16], %[rs], s66 offen offset:3072 sc1\n\t" \
    "buffer_load_dwordx4 v[92:95], %[l16], %[rs], s71 offen offset:1024 sc1\n\t" \
    "s_waitcnt vmcnt(6)\n\t" \
    "v_max3_u32 v220, v48, v49, 0\n\t" \
    "v_max3_u32 v220, v50, v51, v220\n\t" \
    "v_max3_u32 v220, v52, v53, v220\n\t" \
    "v_max3_u32 v220, v54, v55, v220\n\t" \
    "v_max3_u32 v220, v56, v57, v220\n\t" \
    "v_max3_u32 v220, v58, v59, v220\n\t" \
    "v_max3_u32 v220, v60, v61, v220\n\t" \
    "v_max3_u32 v220, v62, v63, v220\n\t" \
    "v_max3_u32 v220, v64, v65, v220\n\t" \
    "v_max3_u32 v220, v66, v67, v220\n\t" \
    "v_max3_u32 v220, v68, v69, v220\n\t" \
    "v_max3_u32 v220, v70, v71, v220\n\t" \
    "v_cmp_eq_u32 vcc, -1, v220\n\t" \
    "s_cbranch_vccnz R21_%=\n\t" \
    "s_branch C21_%=\n\t" \
    "R31_%=:\n\t" \
    "s_cmp_eq_u32 s74, 0\n\t" \
    "s_cbranch_scc1 C31_%=\n\t" \
    "s_sub_u32 s74, s74, 1\n\t" \
    "s_sleep 2\n\t" \
    "buffer_load_dwordx4 v[72:75], %[l16], %[rs], s66 offen offset:1024 sc1\n\t" \
    "buffer_load_dwordx4 v[76:79], %[l16], %[rs], s70 offen offset:3072 sc1\n\t" \
    "buffer_load_dwordx4 v[80:83], %[l16], %[rs], s66 offen offset:2048 sc1\n\t" \
    "buffer_load_dwordx4 v[84:87], %[l16], %[rs], s71 offen offset:0 sc1\n\t" \
    "buffer_load_dwordx4 v[88:91], %[l16], %[rs], s66 offen offset:3072 sc1\n\t" \
    "buffer_load_dwordx4 v[92:95], %[l16], %[rs], s71 offen offset:1024 sc1\n\t" \
    "buffer_load_dwordx4 v[96:99], %[l16], %[rs], s67 offen offset:0 sc1\n\t" \
    "buffer_load_dwordx4 v[100:103], %[l16], %[rs], s71 offen offset:2048 sc1\n\t" \
    "buffer_load_dwordx4 v[104:107], %[l16], %[rs], s67 offen offset:1024 sc1\n\t" \
    "buffer_load_dwordx4 v[108:111], %[l16], %[rs], s71 offen offset:3072 sc1\n\t" \
    "buffer_load_dwordx4 v[112:115], %[l16], %[rs], s67 offen offset:2048 sc1\n\t" \
    "buffer_load_dwordx4 v[116:119], %[l16], %[rs], s72 offen offset:0 sc1\n\t" \
    "s_waitcnt vmcnt(6)\n\t" \
    "v_max3_u32 v220, v72, v73, 0\n\t" \
    "v_max3_u32 v220, v74, v75, v220\n\t" \
    "v_max3_u32 v220, v76, v77, v220\n\t" \
    "v_max3_u32 v220, v78, v79, v220\n\t" \
    "v_max3_u32 v220, v80, v81, v220\n\t" \
    "v_max3_u32 v220, v82, v83, v220\n\t" \
    "v_max3_u32 v220, v84, v85, v220\n\t" \
    "v_max3_u32 v220, v86, v87, v220\n\t" \
    "v_max3_u32 v220, v88, v89, v220\n\t" \
    "v_max3_u32 v220, v90, v91, v220\n\t" \
    "v_max3_u32 v220, v92, v93, v220\n\t" \
    "v_max3_u32 v220, v94, v95, v220\n\t" \
    "v_cmp_eq_u32 vcc, -1, v220\n\t" \
    "s_cbranch_vccnz R31_%=\n\t" \
    "s_branch C31_%=\n\t" \
    "R41_%=:\n\t" \
    "s_cmp_eq_u32 s74, 0\n\t" \
    "s_cbranch_scc1 C41_%=\n\t" \
    "s_sub_u32 s74, s74, 1\n\t" \
    "s_sleep 2\n\t" \
    "buffer_load_dwordx4 v[96:99], %[l16], %[rs], s67 offen offset:0 sc1\n\t" \
    "buffer_load_dwordx4 v[100:103], %[l16], %[rs], s71 offen offset:2048 sc1\n\t" \
    "buffer_load_dwordx4 v[104:107], %[l16], %[rs], s67 offen offset:1024 sc1\n\t" \
    "buffer_load_dwordx4 v[108:111], %[l16], %[rs], s71 offen offset:3072 sc1\n\t" \
    "buffer_load_dwordx4 v[112:115], %[l16], %[rs], s67 offen offset:2048 sc1\n\t" \
    "buffer_load_dwordx4 v[116:119], %[l16], %[rs], s72 offen offset:0 sc1\n\t" \
    "buffer_load_dwordx4 v[120:123], %[l16], %[rs], s67 offen offset:3072 sc1\n\t" \
    "buffer_load_dwordx4 v[124:127], %[l16], %[rs], s72 offen offset:1024 sc1\n\t" \
    "buffer_load_dwordx4 v[128:131], %[l16], %[rs], s68 offen offset:0 sc1\n\t" \
    "buffer_load_dwordx4 v[132:135], %[l16], %[rs], s72 offen offset:2048 sc1\n\t" \
    "buffer_load_dwordx4 v[136:139], %[l16], %[rs], s68 offen offset:1024 sc1\n\t" \
    "buffer_load_dwordx4 v[140:143], %[l16], %[rs], s72 offen offset:3072 sc1\n\t" \
    "s_waitcnt vmcnt(6)\n\t" \
    "v_max3_u32 v220, v96, v97, 0\n\t" \
    "v_max3_u32 v220, v98, v99, v220\n\t" \
    "v_max3_u32 v220, v100, v101, v220\n\t" \
    "v_max3_u32 v220, v102, v103, v220\n\t" \
    "v_max3_u32 v220, v104, v105, v220\n\t" \
    "v_max3_u32 v220, v106, v107, v220\n\t" \
    "v_max3_u32 v220, v108, v109, v220\n\t" \
    "v_max3_u32 v220, v110, v111, v220\n\t" \
    "v_max3_u32 v220, v112, v113, v220\n\t" \
    "v_max3_u32 v220, v114, v115, v220\n\t" \
    "v_max3_u32 v220, v116, v117, v220\n\t" \
    "v_max3_u32 v220, v118, v119, v220\n\t" \
    "v_cmp_eq_u32 vcc, -1, v220\n\t" \
    "s_cbranch_vccnz R41_%=\n\t" \
    "s_branch C41_%=\n\t" \
    "R51_%=:\n\t" \
    "s_cmp_eq_u32 s74, 0\n\t" \
    "s_cbranch_scc1 C51_%=\n\t" \
    "s_sub_u32 s74, s74, 1\n\t" \
    "s_sleep 2\n\t" \
    "buffer_load_dwordx4 v[120:123], %[l16], %[rs], s67 offen offset:3072 sc1\n\t" \
    "buffer_load_dwordx4 v[124:127], %[l16], %[rs], s72 offen offset:1024 sc1\n\t" \
    "buffer_load_dwordx4 v[128:131], %[l16], %[rs], s68 offen offset:0 sc1\n\t" \
    "buffer_load_dwordx4 v[132:135], %[l16], %[rs], s72 offen offset:2048 sc1\n\t" \
    "buffer_load_dwordx4 v[136:139], %[l16], %[rs], s68 offen offset:1024 sc1\n\t" \
    "buffer_load_dwordx4 v[140:143], %[l16], %[rs], s72 offen offset:3072 sc1\n\t" \
    "s_waitcnt vmcnt(0)\n\t" \
    "v_max3_u32 v220, v120, v121, 0\n\t" \
    "v_max3_u32 v220, v122, v123, v220\n\t" \
    "v_max3_u32 v220, v124, v125, v220\n\t" \
    "v_max3_u32 v220, v126, v127, v220\n\t" \
    "v_max3_u32 v220, v128, v129, v220\n\t" \
    "v_max3_u32 v220, v130, v131, v220\n\t" \
    "v_max3_u32 v220, v132, v133, v220\n\t" \
    "v_max3_u32 v220, v134, v135, v220\n\t" \
    "v_max3_u32 v220, v136, v137, v220\n\t" \
    "v_max3_u32 v220, v138, v139, v220\n\t" \
    "v_max3_u32 v220, v140, v141, v220\n\t" \
    "v_max3_u32 v220, v142, v143, v220\n\t" \
    "v_cmp_eq_u32 vcc, -1, v220\n\t" \
    "s_cbranch_vccnz R51_%=\n\t" \
    "s_branch C51_%=\n\t" \
    "KS1_%=:\n\t" \
    "s_nop 3\n\t" \
    "s_cmp_lg_u32 %[first], 0\n\t" \
    "s_cbranch_scc1 KW1_%=\n\t" \
    "v_mov_b32 v156, 0\n\t" \
    "v_mov_b32 v157, 0\n\t" \
    "v_mov_b32 v158, 0\n\t" \
    "v_mov_b32 v159, 0\n\t" \
    "v_mov_b32 v168, 0\n\t" \
    "v_mov_b32 v169, 0\n\t" \
    "v_mov_b32 v170, 0\n\t" \
    "v_mov_b32 v171, 0\n\t" \
    "v_mov_b32 v224, 0xff800000\n\t" \
    "v_mov_b32 v225, 0xff800000\n\t" \
    "v_mov_b32 v226, 0xff800000\n\t" \
    "v_mov_b32 v227, 0xff800000\n\t" \
    "s_nop 1\n\t" \
    "s_branch KB1_%=\n\t" \
    "KW1_%=:\n\t" \
    "s_mov_b32 s75, %[ls]\n\t" \
    "KL1_%=:\n\t" \
    "ds_read_b32 v222, %[fa] offset:0\n\t" \
    "s_sub_u32 s75, s75, 1\n\t" \
    "s_cbranch_scc1 KLe1_%=\n\t" \
    "s_waitcnt lgkmcnt(0)\n\t" \
    "v_cmp_gt_u32 vcc, v221, v222\n\t" \
    "s_cbranch_vccnz KL1_%=\n\t" \
    "KLe1_%=:\n\t" \
    "ds_read2_b64 v[156:159], %[sx] offset0:48 offset1:0\n\t" \
    "ds_read2_b64 v[168:171], %[sx] offset0:112 offset1:0\n\t" \
    "s_waitcnt lgkmcnt(0)\n\t" \
    "s_branch KB1_%=\n\t" \
    "VS1_%=:\n\t" \
    "s_nop 3\n\t" \
    "s_cmp_lg_u32 %[first], 0\n\t" \
    "s_cbranch_scc1 VW1_%=\n\t" \
    "v_mov_b32 v102, 0\n\t" \
    "v_mov_b32 v103, 0\n\t" \
    "v_mov_b32 v162, 0\n\t" \
    "v_mov_b32 v163, 0\n\t" \
    "s_nop 1\n\t" \
    "s_branch VB1_%=\n\t" \
    "VW1_%=:\n\t" \
    "s_mov_b32 s75, %[ls]\n\t" \
    "VL1_%=:\n\t" \
    "ds_read_b32 v222, %[fa] offset:16\n\t" \
    "s_sub_u32 s75, s75, 1\n\t" \
    "s_cbranch_scc1 VLe1_%=\n\t" \
    "s_waitcnt lgkmcnt(0)\n\t" \
    "v_cmp_gt_u32 vcc, v221, v222\n\t" \
    "s_cbranch_vccnz VL1_%=\n\t" \
    "VLe1_%=:\n\t" \
    "ds_read_b64 v[102:103], %[sx] offset:1408\n\t" \
    "ds_read_b64 v[162:163], %[sx] offset:1920\n\t" \
    "s_waitcnt lgkmcnt(0)\n\t" \
    "s_branch VB1_%=\n\t" \
    "END_%=:\n\t"
#define W9L 2
#define W9_OUT "=&{v[24:27]}"(wo9[0]), "=&{v[28:31]}"(wo9[1]), "=&{v[32:35]}"(wo9[2]), "=&{v[36:39]}"(wo9[3]), "=&{v[40:43]}"(wo9[4]), "=&{v[44:47]}"(wo9[5]), "=&{v[48:51]}"(wo9[6]), "=&{v[52:55]}"(wo9[7]), "=&{v[56:59]}"(wo9[8]), "=&{v[60:63]}"(wo9[9]), "=&{v[64:67]}"(wo9[10]), "=&{v[68:71]}"(wo9[11]), "=&{v[72:75]}"(wo9[12]), "=&{v[76:79]}"(wo9[13]), "=&{v[80:83]}"(wo9[14]), "=&{v[84:87]}"(wo9[15]), "=&{v[88:91]}"(wo9[16]), "=&{v[92:95]}"(wo9[17])
#define CLOB_QKV "v0", "v1", "v2", "v3", "v4", "v5", "v6", "v7", "v8", "v9", "v10", "v11", "v12", "v13", "v14", "v15", "v16", "v17", "v18", "v19", "v20", "v21", "v22", "v23", "v96", "v97", "v98", "v99", "v100", "v101", "v102", "v103", "v104", "v105", "v106", "v107", "v108", "v109", "v110", "v111", "v112", "v113", "v114", "v115", "v116", "v117", "v118", "v119", "v120", "v121", "v122", "v123", "v124", "v125", "v126", "v127", "v128", "v129", "v130", "v131", "v132", "v133", "v134", "v135", "v136", "v137", "v138", "v139", "v140", "v141", "v142", "v143", "v144", "v145", "v146", "v147", "v148", "v149", "v150", "v151", "v152", "v153", "v154", "v155", "v156", "v157", "v158", "v159", "v160", "v161", "v162", "v163", "v164", "v165", "v166", "v167", "v168", "v169", "v170", "v171", "v172", "v173", "v174", "v175", "v176", "v177", "v178", "v179", "v180", "v181", "v182", "v183", "v184", "v185", "v186", "v187", "v188", "v189", "v190", "v191", "v192", "v193", "v194", "v195", "v196", "v197", "v198", "v199", "v200", "v201", "v202", "v203", "v204", "v205", "v206", "v207", "v208", "v209", "v210", "v211", "v212", "v213", "v214", "v215", "v216", "v217", "v218", "v219", "v220", "v221", "v222", "v223", "s64", "s65", "s66", "s67", "s68", "s69", "s70", "s71", "s72", "s73", "s74", "s75", "s76", "s77", "s78", "s79", "s80", "s81", "s82", "s83", "s84", "s85", "s86", "s87", "s88", "s89"
#define CLOB_CV "v0", "v1", "v2", "v3", "v4", "v5", "v6", "v7", "v8", "v9", "v10", "v11", "v12", "v13", "v14", "v15", "v16", "v17", "v18", "v19", "v20", "v21", "v22", "v23", "v24", "v25", "v26", "v27", "v28", "v29", "v30", "v31", "v32", "v33", "v34", "v35", "v36", "v37", "v38", "v39", "v40", "v41", "v42", "v43", "v44", "v45", "v46", "v47", "v48", "v49", "v50", "v51", "v52", "v53", "v54", "v55", "v56", "v57", "v58", "v59", "v60", "v61", "v62", "v63", "v64", "v65", "v66", "v67", "v68", "v69", "v70", "v71", "v72", "v73", "v74", "v75", "v76", "v77", "v78", "v79", "v80", "v81", "v82", "v83", "v84", "v85", "v86", "v87", "v88", "v89", "v90", "v91", "v92", "v93", "v94", "v95", "v96", "v97", "v98", "v99", "v100", "v101", "v102", "v103", "v104", "v105", "v106", "v107", "v108", "v109", "v110", "v111", "v112", "v113", "v114", "v115", "v116", "v117", "v118", "v119", "v120", "v121", "v122", "v123", "v124", "v125", "v126", "v127", "v128", "v129", "v130", "v131", "v132", "v133", "v134", "v135", "v136", "v137", "v138", "v139", "v140", "v141", "v142", "v143", "v144", "v145", "v146", "v147", "v148", "v149", "v150", "v151", "v152", "v153", "v154", "v155", "v156", "v157", "v158", "v159", "v160", "v161", "v162", "v163", "v164", "v165", "v166", "v167", "v168", "v169", "v170", "v171", "v172", "v173", "v174", "v175", "v176", "v177", "v178", "v179", "v180", "v181", "v182", "v183", "v184", "v185", "v186", "v187"
#define HAP_OUT 1
#define FC4 0
template <int AUX> DI v4u ldf(rsrc_t r, u32 l16, u32 so, int k) { return __builtin_amdgcn_raw_buffer_load_b128(r, l16 + (k & 3) * 1024, so + (k >> 2) * 4096, AUX); }
template <int K0, int K1> DI void ldw(v4u (&w)[2][18], rsrc_t r, u32 l16, u32 so, u32 on) {
#pragma unroll
    for (int k = K0; k < K1; ++k)
        asm volatile("s_cmp_eq_u32 %8, 0\n\ts_cbranch_scc1 1f\n\tbuffer_load_dwordx4 %0, %2, %3, %4 offen offset:%6 sc1\n\tbuffer_load_dwordx4 %1, %2, %3, %5 offen offset:%7 sc1\n\t1:"
                     : "+v"(w[0][k]), "+v"(w[1][k]) : "v"(l16), "s"(r), "s"(so + (k >> 2) * 4096), "s"(so + ((k + 18) >> 2) * 4096), "n"((k & 3) * 1024), "n"(((k + 18) & 3) * 1024), "s"(on) : "memory", "scc");
}
DI u32 fmax4(u32 m, v4u v) { return umax3(umax3(m, v.x, v.y), v.z, v.w); }
DI v4u pk2(v4f a, v4f b) { return v4u{pk(a[0], a[1]), pk(a[2], a[3]), pk(b[0], b[1]), pk(b[2], b[3])}; }
DI v2u pk1(v4f a) { return v2u{pk(a[0], a[1]), pk(a[2], a[3])}; }
DI u32 vr4(u32 a) { return *(volatile LAS u32*)(size_t)a; }
DI v4u vr16(u32 a) { return *(volatile LAS v4u*)(size_t)a; }
DI v2f lo2(v4f a) { return v2f{a[0], a[1]}; }
DI v2f hi2(v4f a) { return v2f{a[2], a[3]}; }
DI v4f up4(v2u d) { return v4f{blo(d.x), bhi(d.x), blo(d.y), bhi(d.y)}; }
DI v2f fma2(v2f a, v2f b, v2f c) { return __builtin_elementwise_fma(a, b, c); }
DI float fmx3(float a, float b, float c) { return fmx(fmx(a, b), c); }
DI void mom6(v2f (&s)[6], v2f x, v2f y) {
    s[0] += x; s[1] = fma2(x, x, s[1]);
    const v2f z = x * y;
    s[2] += z; s[3] = fma2(x, z, s[3]); s[4] = fma2(z, y, s[4]); s[5] = fma2(z, z, s[5]);
}
DI void mom6i(v2f (&s)[6], v2f x, v2f y) { const v2f z = x * y; s[0] = x; s[1] = x * x; s[2] = z; s[3] = x * z; s[4] = z * y; s[5] = z * z; }
DI v4f fold8(const float (&s0)[8], const float (&s1)[8]) {
    float v[8]; v4f part;
#pragma unroll
    for (int k = 0; k < 8; ++k) { const v2u r = sw32(u_(s0[k]), u_(s1[k])); v[k] = f_(r.x) + f_(r.y); }
#pragma unroll
    for (int k = 0; k < 4; ++k) { const v2u r = sw16(u_(v[k]), u_(v[k + 4])); part[k] = f_(r.x) + f_(r.y); }
    return part;
}
DI v4f fold2(const float (&s0)[2], const float (&s1)[2]) {
    v4f part = {0.f, 0.f, 0.f, 0.f};
#pragma unroll
    for (int k = 0; k < 2; ++k) {
        v2u r = sw32(u_(s0[k]), u_(s1[k]));
        r = sw16(u_(f_(r.x) + f_(r.y)), 0u);
        part[k] = f_(r.x) + f_(r.y);
    }
    return part;
}
template <bool AUX, class F> DI void stage_o(const rsrc_t rw, const u32 l16, const u32 src, const int wave, F&& behind) {
    v4u o[5];
    const u32 so = src + spread(wave * 4096), s4 = src + spread(32768) + (wave & 3) * 1024;
    auto get = [&]() {
#pragma unroll
        for (int k = 0; k < 4; ++k) o[k] = ld16<HLD>(rw, l16 + k * 1024, so);
        if (AUX) o[4] = ld16<HLD>(rw, l16, s4); else o[4] = o[0];
    };
    u32 pv = l16 < 64 ? l16 * 64 + 896 : AUX ? s4 - so + 896 : 896;
    OPQV(pv);
    __builtin_amdgcn_s_sleep(D_O);
    if (OPP) prepoll<OPP>(rw, pv, so);
    get();
    SB();
    if (!YL) behind();
    SB();
#pragma unroll 1
    for (int s_ = 0; s_ < SPIN; ++s_) {
        u32 m = 0;
#pragma unroll
        for (int k = 0; k < 5; ++k) m = fmax4(m, o[k]);
        if (!anysent(m)) break;
        __builtin_amdgcn_s_sleep(D_Q);
        CFENCE();
        get();
    }
    SB();
    if (YL == 1) behind();
    SB();
    const u32 d = L_R + l16 + wave * 4096;
#pragma unroll
    for (int k = 0; k < 4; ++k) lw16(d + k * 1024, o[k]);
    if (AUX) lw16(L_R + l16 + 32768 + (wave & 3) * 1024, o[4]);
    SB();
    if (YL == 2 || (YL == 3 && !AUX)) behind();
    SB();
}
DI v2u mark(u32 e) { return v2u{e * 0x9E3779B1u + 0x7F4A7C15u, (e ^ (e >> 15) ^ 0x5BD1E995u) * 0x85EBCA6Bu}; }
DI u32 rep(u32 r) { return NR ? (r >> 1 << 21) + ((r & 1) << 13) : r * 2048; }
DI void arrive(const rsrc_t rw, const u32 off, const int bid, const u32 e, const u32 vote = 0) {
    const int lane = lane_id();
    const v2u t = mark(e);
    if (lane < NREP) sts8(rw, rep(lane), off + bid * 8, v2u{t.x, t.y ^ vote});
}
template <int SLP> DI v4u await_all(const rsrc_t rw, const u32 off, const int bid, const u32 e) {
    v4u vt = {0u, 0u, 0u, 0u};
    const u32 l16 = lane_id() * 16, so = off + rep(bid & (NREP - 1));
    const v2u t = mark(e);
#pragma unroll 1
    for (int s_ = 0; s_ < SPIN; ++s_) {
        CFENCE();
        const v4u a = ld16<SC1>(rw, l16, so), b = ld16<SC1>(rw, l16 + 1024, so);
        vt = v4u{a.y ^ t.y, a.w ^ t.y, b.y ^ t.y, b.w ^ t.y};
        const u32 m = ((a.x ^ t.x) | (a.z ^ t.x) | (b.x ^ t.x)) | (b.z ^ t.x) | (((vt.x | vt.y | vt.z) | vt.w) >> 1);
        if (__builtin_amdgcn_ballot_w64(m != 0u) == 0) break;
        __builtin_amdgcn_s_sleep(SLP);
    }
    CFENCE();
    return vt;
}

DI void fill_o2(const rsrc_t rw, const u32 gb, const int p) {
    const int lane = lane_id();
    const u32 l16 = lane * 16, s1 = vmov(SENT);
    const v4u s = {s1, s1, s1, s1};
#pragma unroll
    for (int k = 0; k < 8; ++k) sts16<HF>(rw, l16, (gb ^ FO2) + B_O2 + (k >> 1) * SLOT + spread((k & 1) * TSL + p * 1024), s);
    sts16<HF>(rw, (lane >> 4) * SLOT + (lane >> 3 & 1) * 34816 + (lane & 7) * 16, (gb ^ FO2) + B_O2 + spread(16384) + p * 128, s);
    sts16(rw, l16, (gb ^ FST) + B_ST + spread(16384 + p * 1024), s);
    CFENCE();
}
template <int M, int W> DI void scrub(const rsrc_t rw, const u32 l16, const u32 id, const int w) {
    const u32 z1 = vmov(0u);
    const v4u z = {z1, z1, z1, z1};
    constexpr int n0 = M & 1 ? 9 : 0, n1 = n0 + (M & 2 ? 10 : 0), n2 = n1 + (M & 4 ? 8 : 0), n = (n2 + W - 1) / W;
#pragma unroll
    for (int k = 0; k < n; ++k) {
        const int i = k * W + w;
        if (i < n2) sts16(rw, l16, i < n0 ? OFF_XN1 + id * 9216 + i * 1024 : i < n1 ? OFF_KVT + id * 9792 + umin((i - n0) * 1024, 8768) : OFF_WQP + id * 7776 + umin((i - n1) * 1024, 6752), z);
    }
}
DI void main_role(const u16* __restrict__ x, char* ws, u16* out, const rsrc_t rw, int wave, int ti, int p TSARG) {
    const v4f z4 = {0.f, 0.f, 0.f, 0.f};
    v4f res; v2u yp;
    u32 sl = 0;
    asm volatile("" : "=v"(res), "=v"(yp));
#pragma unroll 1
    for (int layer = 0; layer < 2; ++layer) {
        OPQS(ti); OPQS(p);
        const int rb = p >> 2, cq = p & 3;
        const int nt9 = 9 * cq + 8, mt0 = 8 * ti + 2 * rb;
        const u32 blk = (ti & 7) << 20, offo = blk + (layer ? B_O2 : B_O1);
        v4u wo9[18];
        const rsrc_t r9 = mkrs(ws + OFF_WOP + nt9 * TSL, wave < 2 ? TSL : 0);
        {
            const int lane = lane_id(), g = lane >> 4, li = lane & 15;
            const u32 l16 = lane * 16, zero = vmov(0u);
            const bool par = li & 1, cur0 = 4 * g + (li & 1) <= li, cur1 = 4 * g + (li & 1) + 2 <= li;
            const u32 zz = 0x0C0C0C0Cu, z0 = zz ^ (0x0D0Cu << ((li & 1) * 16)), z1 = zz ^ (0x0F0Eu << ((li & 1) * 16));
            v4u ps = {cur0 ? z0 : zz, cur1 ? z1 : zz, cur0 ? zz : z0, cur1 ? zz : z1};
            const float ni = -INFINITY;
            v4f mc = {(!par && cur0) ? 0.f : ni, (par && cur0) ? 0.f : ni, (!par && cur1) ? 0.f : ni, (par && cur1) ? 0.f : ni};
            v4f mp = {(!par && !cur0) ? 0.f : ni, (par && !cur0) ? 0.f : ni, (!par && !cur1) ? 0.f : ni, (par && !cur1) ? 0.f : ni};
            const u32 one = (li & 1) ? 0x3F800000u : 0x00003F80u;
            v4u sel = {zero, zero, (g == 2 && li < 2) ? one : 0u, (g == 2 && (li >> 1) == 1) ? one : 0u};
            u32 xrd = 4096 + (g * 12 + XSLOT(li)) * 16, xr2 = xrd + 9 * WKS, wr2 = l16 + 9 * WKS, ox = lane < 16 ? lane * 8 : 0x80000000u;
            u32 sx = L_KVX + wave * KVXS + lane * 8, fa = vmov(F_K + wave * 4), fg = F_W + (lane & 3) * 4;
            asm volatile("" : "+{v[224:227]}"(mp), "+{v[228:231]}"(ps), "+v"(mc), "+v"(sel), "+v"(xrd), "+v"(xr2), "+v"(wr2), "+v"(ox), "+v"(sx), "+v"(fg));
            u32 ab = layer ? blk + (sl ^ FX2) + B_XN2 + wave * SLOT : OFF_XN1 + (8 * ti + 2 * wave) * TSL, first = ti | wave;
            u32 po = offo + wave * SLOT + spread(p * 1024), p1 = offo + wave * SLOT + spread(TSL + p * 1024), xo = offo + wave * SLOT + spread(16384) + p * 128;
            static_assert(F_V == F_K + 16 && F_W == F_K + 32 && F_VR == F_W + 16 && F_SEL == F_W + 32, "flag offsets");
            u32 wv = wave * WKS, w4 = 16 * WKS + umin(wave * 3328, 9728), wsrc = OFF_WQP + p * WSLAB;
            OPQS(wv); OPQS(w4); OPQS(wsrc); OPQS(ab); OPQS(po); OPQS(p1); OPQS(xo); OPQS(first);
            if (layer) { CFENCE(); __builtin_amdgcn_s_barrier(); __builtin_amdgcn_s_sleep(D_X); }
            else {
                TS(45);
                bar();
            }
            TS(2 + 20 * layer);
            asm volatile(A_QKV : "+{v[224:227]}"(mp), "+{v[232:235]}"(res), "+{v[236:237]}"(yp), [sl] "+s"(sl), W9_OUT TSA_OPS
                : "{v[228:231]}"(ps), [mc] "v"(mc), [sel] "v"(sel), [l16] "v"(l16), [wr2] "v"(wr2), [xrd] "v"(xrd), [xr2] "v"(xr2), [sx] "v"(sx), [fa] "v"(fa), [ox] "v"(ox), [fg] "v"(fg),
                  [rs] "s"(rw), [ab] "s"(ab), [wave] "s"(wave), [first] "s"(first), [po] "s"(po), [p1] "s"(p1), [xo] "s"(xo), [gs] "n"(SPIN), [ls] "n"(LSPIN), [wv] "s"(wv), [w4] "s"(w4), [ws] "s"(wsrc), [ly] "s"(layer), [r9] "s"(r9), [f1] "n"(FO1), [f2] "n"(FO2)
                : CLOB_QKV, "vcc", "scc", "memory" TSA_CLOB);
        }
        PADB(L_m0);
        sl = __builtin_amdgcn_readfirstlane(sl);
        OPQS(wave);
        TS(11 + 20 * layer);
        const int lane = lane_id(), g = lane >> 4, li = lane & 15;
        const u32 l16 = lane * 16;
        v2u xr;
        asm volatile("" : "=v"(xr));
        u32 yd[4];
#pragma unroll
        for (int r = 0; r < 4; ++r) asm volatile("" : "=v"(yd[r]));
        const u32 yvo = g * 16384 + (li & 14) * 2, yso = OFF_YT + nt9 * 65536 + (mt0 + wave) * 32;
        if (wave < 2) {
            if (!(W9L >> layer & 1)) {
#pragma unroll
                for (int k = 0; k < 18; ++k) asm volatile("buffer_load_dwordx4 %0, %1, %2, %3 offen offset:%4 sc1" : "+v"(wo9[k]) : "v"(l16), "s"(r9), "s"((k >> 2) * 4096), "n"((k & 3) * 1024) : "memory");
            }
            if (!layer) xr = ld8(mkrs(x, FR), li * 1152 + g * 8, (mt0 + wave) * TSL + nt9 * 32);
        }
        __builtin_amdgcn_s_setprio(0);
        PAD();
        bar();
        TS(12 + 20 * layer);
        if (PB1 && layer) VMCNT(0); else
        stage_o<false>(rw, l16, offo + (sl ^ (layer ? FO2 : FO1)) + rb * SLOT, wave, [&]() {
            if (wave < 2 && !layer) {
#pragma unroll
                for (int r = 0; r < 4; ++r) asm volatile("buffer_load_dword %0, %1, %2, %3 offen sc1" : "+v"(yd[r]) : "v"(yvo), "s"(rw), "s"(yso + r * 4096) : "memory");
            }
        });
        TS(13 + 20 * layer);
        __builtin_amdgcn_s_setprio(PR_W);
        bar();
        TS(14 + 20 * layer);
        u32 sr = L_R + l16;
        OPQV(sr);
        if (wave < 2) {
            v4f c = z4;
            v4u o0[18];
            const u32 so = sr + wave * TSL, sxo = L_R + wave * TSL + g * 256 + li * 8;
            auto of = [&](int k) { return k < 16 ? lr16(so + k * 1024) : cat(lr8(sxo + k * 1024), lr8(sxo + k * 1024 + 128)); };
#pragma unroll
            for (int k = 0; k < 6; ++k) o0[k] = of(k);
            SB();
#pragma unroll
            for (int k = 0; k < 18; ++k) {
                c = mfma(wo9[k], o0[k], c); if (k + 6 < 18) o0[k + 6] = of(k + 6); if (k % 8 == 0) PAD(); SB();
                if (k == B3M) { __builtin_amdgcn_s_barrier(); SB(); }
            }
            if (!layer) {
                const u32 ysel = (li & 1) ? 0x07060302u : 0x05040100u;
                asm volatile("s_waitcnt vmcnt(0)" : "+v"(yd[0]), "+v"(yd[1]), "+v"(yd[2]), "+v"(yd[3]));
                yp = v2u{__builtin_amdgcn_perm(yd[1], yd[0], ysel), __builtin_amdgcn_perm(yd[3], yd[2], ysel)};
                res = up4(xr);
            }
            res += c;
            v4f part;
            if (M2 && layer) {
                PADB(L_e1);
                const v2f s0 = lo2(res) + hi2(res), s1 = fma2(hi2(res), hi2(res), lo2(res) * lo2(res));
                const float a0 = s0[0] + s0[1], a1 = s1[0] + s1[1];
                const float u[2] = {wave ? 0.f : a0, wave ? 0.f : a1}, w[2] = {wave ? a0 : 0.f, wave ? a1 : 0.f};
                SB(); PADX(); SB();
                part = fold2(u, w);
                SB(); PADT(); SB();
            } else {
            const v4f y = up4(yp);
            v2f s[6];
            mom6i(s, lo2(res), lo2(y)); mom6(s, hi2(res), hi2(y));
            SB(); PAD(); SB();
            float v[8], zs[8] = {0.f, 0.f, 0.f, 0.f, 0.f, 0.f, 0.f, 0.f};
#pragma unroll
            for (int k = 0; k < 6; ++k) v[k] = s[k][0] + s[k][1];
            v[6] = (y[0] + y[1]) + (y[2] + y[3]); v[7] = fmaf(y[0], y[0], y[1] * y[1]) + fmaf(y[2], y[2], y[3] * y[3]);
            SB(); PAD(); SB();
            part = wave ? fold8(zs, v) : fold8(v, zs);
            }
            CFENCE();
            if (B3M > 17) { asm volatile("" :: "v"(part)); __builtin_amdgcn_s_barrier(); CFENCE(); }
            lwf(sr + (4 + wave) * 1024, part);
        } else {
            if (wave == 2 && !layer) { fill_o2(rw, blk + sl, p); if (HQ3 == 4) HOP(L_qkv1_18); }
            if (SCR0 && wave == 3 && !layer) scrub<SCR0, SD0>(rw, l16, ti * 16 + p, 0);
            if (SCRH && layer && wave == 2) scrub<SCRH, 2>(rw, l16, ti * 16 + p, 0);
            if (SCRG && layer && wave == 2) scrub<SCRG, SD1>(rw, l16, ti * 16 + p, SD1 - 1);
            if (SCR1 && layer) scrub<SCR1, 2>(rw, l16, ti * 16 + p, wave - 2);
            __builtin_amdgcn_s_barrier();
        }
        TS(15 + 20 * layer);
        __builtin_amdgcn_s_setprio(2);
        PAD();
        bar();
        TS(16 + 20 * layer);
        if (wave == 3) {
            v4f t = ((lrf(sr) + lrf(sr + 1024)) + (lrf(sr + 2048) + lrf(sr + 3072))) + (lrf(sr + 4096) + lrf(sr + 5120));
            const u32 sto = blk + (sl ^ FST) + B_ST + spread(layer * 16384 + rb * 4096);
            sts16(rw, l16, sto + cq * 1024, v4u{san(u_(t[0])), san(u_(t[1])), san(u_(t[2])), san(u_(t[3]))});
            TS(17 + 20 * layer);
            PAD();
            __builtin_amdgcn_s_sleep(D_ST);
            v4u q4[4];
#pragma unroll 1
            for (int s_ = 0; s_ < SPIN; ++s_) {
                CFENCE();
#pragma unroll
                for (int k = 0; k < 4; ++k) q4[k] = ldf<SC1>(rw, l16, sto, k);
                if (!anysent(fmax4(fmax4(fmax4(fmax4(0u, q4[0]), q4[1]), q4[2]), q4[3]))) break;
            }
            t = (__builtin_bit_cast(v4f, q4[0]) + __builtin_bit_cast(v4f, q4[1])) + (__builtin_bit_cast(v4f, q4[2]) + __builtin_bit_cast(v4f, q4[3]));
            TS(18 + 20 * layer);
            PAD();
            float lo[4], hi[4];
            t *= 1.f / 576.f;
#pragma unroll
            for (int k = 0; k < 4; ++k) { const v2u r = sw16(u_(t[k]), u_(t[k])); lo[k] = f_(r.x); hi[k] = f_(r.y); }
            const float m1 = lo[0], r1 = __builtin_amdgcn_rsqf(fmaxf(lo[1] - m1 * m1, 0.f) + 1e-5f);
            SB(); PAD(); SB();
            const float m2 = m1 + r1 * (lo[2] - m1 * hi[2]);
            const float e2 = fmaf(r1, fmaf(r1, fmaf(m1, fmaf(m1, hi[3], -(hi[0] + hi[0])), hi[1]), 2.f * fmaf(-m1, lo[2], lo[3])), lo[1]);
            const float r2 = __builtin_amdgcn_rsqf(fmaxf(e2 - m2 * m2, 0.f) + 1e-5f);
            const float sa = (g & 1) ? (layer ? 1.f : r2) : r1, sb = (g & 1) ? (layer ? 0.f : -m2 * r2) : -m1 * r1;
            lw16(L_STAT + l16, v4u{u_(sa), u_(sa), u_(sb), u_(sb)});
        }
        PAD();
        if (!layer) VMCNT(0);
        bar();
        TS(19 + 20 * layer);
        if (wave < 2) {
            const v4f s1 = lrf(L_STAT + wave * 512 + li * 16), s2 = lrf(L_STAT + wave * 512 + 256 + li * 16), y = up4(yp);
            const v2f l = fma2(fma2(lo2(res), lo2(s1), hi2(s1)), lo2(y), lo2(res)), h = fma2(fma2(hi2(res), lo2(s1), hi2(s1)), hi2(y), hi2(res));
            res = v4f{l[0], l[1], h[0], h[1]};
            const v2f ol = fma2(l, lo2(s2), hi2(s2)), oh = fma2(h, lo2(s2), hi2(s2));
            const v2u d = {san(pk(ol[0], ol[1])), san(pk(oh[0], oh[1]))};
            SB(); PAD(); SB();
            if (!layer) {
                const v2u e0 = sw16(d.x, d.x), e1 = sw16(d.y, d.y);
                sts16<HS>(rw, ((g >> 1) * 16 + li) * 16 + ((u32)g << 31), blk + (sl ^ FX2) + B_XN2 + rb * SLOT + spread(wave * TSL + nt9 * 512), v4u{e0.x, e1.x, e0.y, e1.y});
            } else __builtin_amdgcn_raw_buffer_store_b64(d, mkrs(out, FR), li * 1152 + g * 8, (mt0 + wave) * TSL + nt9 * 32, OAUX);
        }
        else if (SCR2 && layer) scrub<SCR2, 2>(rw, l16, ti * 16 + p, wave - 2);
        else if (SCRH && layer && wave == 3) scrub<SCRH, 2>(rw, l16, ti * 16 + p, 1);
        TS(20 + 20 * layer);
    }
    TSOUT();
    PADT();
}

template <class G> DI void halo(const rsrc_t rw, int lane, const int aw, const int ti, const u32 sel, const int layer, v4u (&a)[9], G&& mid TSARG_R) {
    OPQV(lane);
    const int g = lane >> 4, li = lane & 15;
    const int nh = (aw & 1) ^ 1, kh = aw >> 1;
    const v4f z4 = {0.f, 0.f, 0.f, 0.f};
    const u32 l16 = lane * 16;
    u32 sx = L_KVX + lane * 8, r = L_RED + nh * 3072 + l16;
    u32 wb = l16 + kh * 9 * WKS + 2048 + nh * 2816, xb = 4096 + (g * 12 + XSLOT(li)) * 16 + kh * 9 * WKS;
    OPQV(wb); OPQV(xb); OPQV(r); OPQV(sx);
    v4f c0 = z4, c1 = z4, c2 = z4;
    if (!layer) flag_wait_all<1>(F_W + (lane & (nh ? 7 : 3)) * 4, 4);
    if (ti) {
        if (layer) {
            const u32 ab = ((ti - 1 & 7) << 20) + ((sel >> 1 & 8192) ^ FX2) + B_XN2 + 3 * SLOT, ao = TSL + kh * 9216;
            __builtin_amdgcn_s_sleep(D_H);
            if (HPP) prepoll<4>(rw, spread(ao + (lane < 18 ? lane : 0) * 512), ab);
#pragma unroll 1
            for (int s_ = 0; s_ < SPIN; ++s_) {
                CFENCE();
                u32 m = 0;
#pragma unroll
                for (int k = 0; k < 9; ++k) a[k] = ld16<HLD>(rw, l16, ab + spread(ao + k * 1024));
#pragma unroll
                for (int k = 0; k < 9; ++k) m = fmax4(m, a[k]);
                if (!anysent(m)) break;
            }
        }
        TS(7 + 20 * layer);
        v4u bb[27];
        if (nh == 0) {
            auto rd = [&](int j) { bb[j] = j % 3 == 2 ? lr16(xb + (j / 3) * WKS) : lr16(wb + (j / 3) * WKS + (j % 3) * 1024); };
#pragma unroll
            for (int j = 0; j < HL; ++j) rd(j);
            SB();
#pragma unroll
            for (int j = 0; j < 27; ++j) {
                if (j % 3 == 0) c0 = mfma(bb[j], a[j / 3], c0); else if (j % 3 == 1) c1 = mfma(bb[j], a[j / 3], c1); else c2 = mfma(bb[j], a[j / 3], c2);
                SB(); if (j + HL < 27) rd(j + HL); if (j % 9 == 0) PAD(); SB();
            }
        } else {
            auto rd = [&](int j) { bb[j] = lr16(wb + (j / 2) * WKS + (j % 2) * 1024); };
#pragma unroll
            for (int j = 0; j < HL; ++j) rd(j);
            SB();
#pragma unroll
            for (int j = 0; j < 18; ++j) {
                if (j % 2 == 0) c0 = mfma(a[j / 2], bb[j], c0); else c1 = mfma(a[j / 2], bb[j], c1);
                SB(); if (j + HL < 18) rd(j + HL); if (j % 9 == 0) PAD(); SB();
            }
        }
        PAD();
        if (kh) {
            if (!layer && aw == 2) flag_wait<1>(F_C2, 1);
            lwf(r, c0); lwf(r + 1024, c1);
            if (nh == 0) lwf(r + 2048, c2);
            flag_put(F_HR + nh * 4, layer + 1);
        }
    }
    PAD();
    mid();
    PAD();
    if (ti && !kh) {
        v4f d0, d1, d2;
        const u32 fa = vmov(F_HR + nh * 4);
#pragma unroll 1
        for (int i = 0; i < LSPIN; ++i) {
            u32 c;
            asm volatile("ds_read_b32 %0, %4\n\tds_read_b128 %1, %5\n\tds_read_b128 %2, %5 offset:1024\n\tds_read_b128 %3, %5 offset:2048\n\ts_waitcnt lgkmcnt(0)"
                         : "=&v"(c), "=&v"(d0), "=&v"(d1), "=&v"(d2) : "v"(fa), "v"(r) : "memory");
            if (__builtin_amdgcn_readfirstlane(c) > (u32)layer) break;
            if (RSP) flag_waitv<1>(fa, layer + 1);
        }
        JT();
        c0 += d0; c1 += d1; c2 += d2;
        lw8(sx + 384 + nh * 1024, pk1(c0)); lw8(sx + 896 + nh * 1024, pk1(c1));
        if (nh == 0 && lane < 48) lw8(sx, pk1(c2));
        flag_put(F_K + nh * 16, layer + 1);
    }
    PAD();
}

DI void aux_role(const u16* __restrict__ x, u32 dd, u32 dd2, char* ws, u16* out, const rsrc_t rw,
                 const int wave, int ti, int p, const int cidx, const u32 ep, const u32 near TSARG) {
    const v4f z4 = {0.f, 0.f, 0.f, 0.f};
    const int aw = wave - 4;
    int lane = lane_id();
    v4u ha[9];
    {
        const int g = lane >> 4, li = lane & 15;
        const int chq = CHQ(cidx);
        const u32 l16 = lane * 16;
        u32 hab = OFF_XN1 + (ti ? 8 * ti - 1 : 0) * TSL + (aw >> 1) * 9216, fc = vmov(F_CA);
        static_assert(F_C2 == F_CA + 4, "flag offsets");
        const u32 cb = L_R + ((aw + 1) >> 1) * CBS;
        u32 hb = cb + 224 - 64 * g + 8 * ((li & 3) - (li >> 2)), ub = cb + CB_U + 8 + (li & 3) * 1088 + (li >> 2) * 8 + 16 * g;
        u32 ua = cb + CB_U + 64 + (lane & 3) * 1088 + (lane >> 2) * 2, ya = L_YS + aw * 1024 + lane * 2, yr = L_YS + aw * 1024 + l16;
        u32 hb2 = hb - CBS, ub2 = ub - CBS, ua2 = ua - CBS;
        u32 yo = OFF_YT + (aw < 3 ? 2 * cidx + ((aw + 1) >> 1) : chq) * 4096, yo2 = OFF_YT + (2 * cidx + (aw >> 1)) * 4096;
        OPQV(hb); OPQV(ub); OPQV(ua); OPQV(ya); OPQV(yr); OPQV(hb2); OPQV(ub2); OPQV(ua2); OPQS(yo); OPQS(yo2);
        u32 cs = aw < 3 ? aw : 3 + (cidx >> 3 & 3);
        OPQS(hab); OPQS(dd); OPQS(dd2); OPQS(cs);
        v4u vt;
        asm volatile("" : "=v"(vt));
        if (aw == 0) {
            __builtin_amdgcn_s_sleep(D_P);
            vt = await_all<D_R>(rw, OFF_F1, NR ? (ti & 6) | near : cidx, ep);
        }
        if (!FC4) __builtin_amdgcn_s_waitcnt(0x0F70);
        TS(45);
        if (aw == 0) {
#pragma unroll
            for (int i = 0; i < 3; ++i) dma4u(rw, (lane >> 1) * 128 + (lane & 1) * 16, OFF_KVT + (i < 2 ? 2 * cidx + i : chq) * KVS, L_R + i * CBS + CB_U + 64);
        }
        bar();
        TS(46);
        if (aw == 0) {
            u64 bl[4];
#pragma unroll
            for (int i = 0; i < 4; ++i) bl[i] = __builtin_amdgcn_ballot_w64(vt[i] & 1);
            auto half = [&](int t) {
                const u64 m = 0x0055005500550055ull << ((t >> 2 & 1) + 4 * (t & 2));
                u32 n = 0;
#pragma unroll
                for (int i = 0; i < 4; ++i) n += __builtin_popcountll(bl[i] & m);
                return (u32)(n > 32) ^ (t >> 3 & 1);
            };
            const u32 w = vmov(1u | half(ti) << 13 | half(ti - 1) << 14);
            if (lane == 0) lw16(F_SEL, v4u{w, w, w, w});
            CFENCE();
        }
        TS(3);
        TC(58);
        asm volatile(A_CV : [h0] "=&v"(ha[0]), [h1] "=&v"(ha[1]), [h2] "=&v"(ha[2]), [h3] "=&v"(ha[3]), [h4] "=&v"(ha[4]), [h5] "=&v"(ha[5]), [h6] "=&v"(ha[6]), [h7] "=&v"(ha[7]),
            [h8] "=&v"(ha[8]), [hab] "+s"(hab) : [hb] "v"(hb), [ub] "v"(ub), [ua] "v"(ua), [ya] "v"(ya), [yr] "v"(yr), [l16] "v"(l16), [rs] "s"(rw), [yo] "s"(yo), [dd] "s"(dd), [fc] "v"(fc),
            [hb2] "v"(hb2), [ub2] "v"(ub2), [ua2] "v"(ua2), [yo2] "s"(yo2), [dd2] "s"(dd2), [m1] "s"(1ull << 63), [m2] "s"(3ull << 62), [m3] "s"(7ull << 61), [cs] "s"(cs), [ls] "n"(LSPIN)
            : CLOB_CV, "vcc", "scc", "memory");
        PADB(L_a0);
        TC(59);
        __builtin_amdgcn_s_setprio(3);
        TS(5);
    }
    v4u wo[2][18];
    v4f res[2][2];
    v2u yp[2][2];
    u32 sel = __builtin_amdgcn_readfirstlane(vr4(F_SEL));
    auto arr2 = [&]() { if (flag_inc_rtn(F_CA) == 4 + 3 * FC4) arrive(rw, OFF_F2, cidx, ep); };
    {
        const u32 l16 = lane * 16;
#pragma unroll
        for (int k = 0; k < 18; ++k) asm volatile("" : "=v"(wo[0][k]), "=v"(wo[1][k]));
        if (!(aw & 1)) {
            u32 vs = OFF_WQP + p * WSLAB + (aw >> 1) * 8 * WKS + 4864, vd = (aw >> 1) * 8 * WKS + 4864;
#pragma unroll
            for (int k = 0; k < 8; ++k) { dma2k<1>(rw, l16, vs + k * WKS, vd + k * WKS); if (k == 3) PAD(); }
            PAD();
        } else {
            const u32 s1 = vmov(SENT), gb = ((ti & 7) << 20) + (sel & 8192), fo = (gb ^ FX2) + B_XN2 + (p >> 2) * SLOT, f0 = (p & 3) * 9216;
            const v4u s = {s1, s1, s1, s1};
#pragma unroll
            for (int k = 0; k < 4; ++k) sts16<HF>(rw, l16, fo + spread(f0 + (aw >> 1) * 4096 + k * 1024), s);
            sts16<HF>(rw, l16, aw >> 1 ? (gb ^ FST) + B_ST + spread(p * 1024) : fo + spread(f0 + 8192), s);
        }
#pragma unroll
        for (int i = 0; i < 4; ++i) yp[i >> 1][i & 1] = ld8(mkrs(x, FR), (lane & 15) * 1152 + (lane >> 4) * 8 + (i & 1) * 32, (8 * ti + 2 * (p >> 2) + (i >> 1)) * TSL + (9 * (p & 3) + 2 * aw) * 32);
        CFENCE();
        ldw<0, WOV>(wo, rw, l16, OFF_WOP + (9 * (p & 3) + 2 * aw) * TSL, ~aw & 1);
        TS(6);
        if (!(aw & 1)) {
            vmcnt<2 * WOV>();
            flag_put(F_VR + aw * 4, 5u); flag_put(F_VR + aw * 4 + 4, 5u);
            arr2();
        }
        PAD();
#pragma unroll
        for (int i = 0; i < 4; ++i) asm volatile("" : "=v"(res[i >> 1][i & 1]));
    }
#pragma unroll 1
    for (int layer = 0; layer < 2; ++layer) {
        OPQV(lane); OPQS(ti); OPQS(p); OPQS(sel);
        const int g = lane >> 4, li = lane & 15, rb = p >> 2, cq = p & 3;
        const u32 gs = ((ti & 7) << 20) + (sel & 8192) + rb * SLOT;
        const u32 l16 = lane * 16;
        const int nt = 9 * cq + 2 * aw, mt0 = 8 * ti + 2 * rb;
        float ys[2][2] = {{0.f, 0.f}, {0.f, 0.f}};
        __builtin_amdgcn_s_setprio(3);
        halo(rw, lane, aw, ti, sel, layer, ha, [&]() { if (!layer && (aw & 1)) { VMCNT(0); arr2(); } if (SCRM && layer) scrub<SCRM, 4>(rw, l16, ti * 16 + p, aw); } TSPASS);
        __builtin_amdgcn_s_setprio(0);
        TS(8 + 20 * layer);
        if (!layer) {
#pragma unroll
            for (int i = 0; i < 4; ++i) { res[i >> 1][i & 1] = up4(yp[i >> 1][i & 1]); OPQV(res[i >> 1][i & 1]); }
        }
        ldw<0, WOV>(wo, rw, l16, OFF_WOP + nt * TSL, aw & ~layer & 1);
        ldw<WOV, 18>(wo, rw, l16, OFF_WOP + nt * TSL, layer ^ 1);
        if (!layer) {
            TS(9);
            if (aw == HQ1) HOP(L_qkv1_0);
            if (aw == HQ2) HOP(L_qkv1_9);
            if (aw == HQ3) HOP(L_qkv1_18);
            if (aw == POLL2) {
                await_all<4>(rw, OFF_F2, NR ? (ti & 6) | (sel >> 13 & 1) : cidx, ep);
                if (cidx == 0 && lane == 0) __builtin_amdgcn_raw_buffer_store_b32(ep + 1u, rw, 0u, OFF_EP, 0);
            }
            if (WOW) VMCNT(0);
        }
        if (SCRUB && layer) scrub<SCRUB, 4>(rw, l16, ti * 16 + p, aw);
        if (SCRK && layer && aw >= 2) scrub<SCRK, 2>(rw, l16, ti * 16 + p, aw - 2);
        if (SCRL && layer && aw < 2) scrub<SCRL, 2>(rw, l16, ti * 16 + p, aw);
        if (M2 == 1 && layer && aw == 3) { HOP(L_e0); HOP(L_e1); if (PB1) HOP(L_e2); }
        if (TOUCH && layer) { u32 t = ld4(mkrs(out, FR), li * 1152, mt0 * TSL + nt * 32); OPQV(t); }
        const u32 so2 = (gs ^ FO2) + B_O2;
#pragma unroll
        for (int k = 0; k < 9; ++k) asm volatile("" : "=v"(ha[k]));
        if (PB1 && layer) {
            flag_wait_all<2>(F_W + (lane & 3) * 4, 6);
            __builtin_amdgcn_s_sleep(D_A);
            u32 pend = 511;
#pragma unroll 1
            for (int s_ = 0; s_ < SPIN; ++s_) {
#pragma unroll
                for (int k = 0; k < 9; ++k)
                    asm volatile("s_bitcmp0_b32 %4, %5\n\ts_cbranch_scc1 1f\n\tbuffer_load_dwordx4 %0, %1, %2, %3 offen sc1\n\t1:"
                                 : "+v"(ha[k]) : "v"(l16), "s"(rw), "s"(so2 + spread(aw * 1024 + k * 4096)), "s"(pend), "n"(k) : "memory", "scc");
                asm volatile("s_waitcnt vmcnt(0)" : "+v"(ha[0]), "+v"(ha[1]), "+v"(ha[2]), "+v"(ha[3]), "+v"(ha[4]), "+v"(ha[5]), "+v"(ha[6]), "+v"(ha[7]), "+v"(ha[8]));
                pend = 0;
#pragma unroll
                for (int k = 0; k < 9; ++k) pend |= (u32)anysent(umax(umax3(ha[k].x, ha[k].y, ha[k].z), ha[k].w)) << k;
                if (!pend) break;
                __builtin_amdgcn_s_sleep(D_B);
            }
            JT();
        }
        TS(11 + 20 * layer);
        PADE(L_a1);
        bar();
        TS(12 + 20 * layer);
        u32 yd[2][2][4];
#pragma unroll
        for (int i = 0; i < 16; ++i) asm volatile("" : "=v"(yd[i >> 3][i >> 2 & 1][i & 3]));
        auto gety = [&]() {
            const u32 yvo = g * 16384 + (li & 14) * 2;
#pragma unroll
            for (int j = 0; j < 2; ++j)
#pragma unroll
                for (int r = 0; r < 4; ++r)
#pragma unroll
                    for (int m = 0; m < 2; ++m) yd[m][j][r] = ld4<SC1>(rw, yvo + m * 32, OFF_YT + ((nt + j) * 16 + r) * 4096 + mt0 * 32);
        };
        if (PB1 && layer) {
            PADB(L_e2);
#pragma unroll
            for (int k = 0; k < 9; ++k) lw16(L_R + l16 + aw * 1024 + k * 4096, ha[k]);
            PADT();
        } else
        stage_o<true>(rw, l16, (layer ? (gs ^ FO2) + B_O2 : (gs ^ FO1) + B_O1), wave, [&]() { if (!layer) gety(); });
        __builtin_amdgcn_s_setprio(2);
        TS(13 + 20 * layer);
        bar();
        TS(14 + 20 * layer);
        u32 sr = L_R + l16;
        OPQV(sr);
        {
            v4u o0[18], o1[18];
            constexpr int PD = PDW;
            const u32 yv = g * 16384 + (li & 14) * 2 + ((u32)layer << 31), yb = OFF_YT + nt * 65536 + mt0 * 32;
            auto gy = [&](int i) { yd[i & 1][i >> 3][i >> 1 & 3] = ld4<SC1>(rw, yv + (i & 1) * 32, yb + ((i >> 3) * 16 + (i >> 1 & 3)) * 4096); };
            u32 sxo = L_R + 16384 + g * 256 + li * 8, sx1 = sxo + TSL;
            OPQV(sxo); OPQV(sx1);
            auto of = [&](int m, int k) { const u32 a = (m ? sx1 : sxo) + (k - 16) * 1024; return k < 16 ? lr16(sr + m * TSL + k * 1024) : cat(lr8(a), lr8(a + 128)); };
#pragma unroll
            for (int k = 0; k < PD; ++k) { o0[k] = of(0, k); o1[k] = of(1, k); }
            SB();
#pragma unroll
            for (int k = 0; k < 18; ++k) {
                res[0][0] = mfma(wo[0][k], o0[k], res[0][0]); SB();
                if (YL == 3 && k < 16 / YN) { for (int i = 0; i < YN; i += 2) gy(k * YN + i); SB(); }
                res[0][1] = mfma(wo[1][k], o0[k], res[0][1]); SB(); if (k + PD < 18) o0[k + PD] = of(0, k + PD); SB();
                res[1][0] = mfma(wo[0][k], o1[k], res[1][0]); SB();
                if (YL == 3 && k < 16 / YN) { for (int i = 1; i < YN; i += 2) gy(k * YN + i); SB(); }
                res[1][1] = mfma(wo[1][k], o1[k], res[1][1]); SB(); if (k + PD < 18) o1[k + PD] = of(1, k + PD); if (k % 4 == 0) PAD(); SB();
                if (k == B3A) { __builtin_amdgcn_s_barrier(); SB(); }
            }
        }
        TS(10 + 20 * layer);
        if (!layer) {
            const u32 ysel = (li & 1) ? 0x07060302u : 0x05040100u;
#pragma unroll
            for (int m = 0; m < 2; ++m)
#pragma unroll
                for (int j = 0; j < 2; ++j) {
                    yp[m][j] = v2u{__builtin_amdgcn_perm(yd[m][j][1], yd[m][j][0], ysel), __builtin_amdgcn_perm(yd[m][j][3], yd[m][j][2], ysel)};
                    const v4f y = up4(yp[m][j]);
                    ys[m][0] += (y[0] + y[1]) + (y[2] + y[3]); ys[m][1] += fmaf(y[0], y[0], y[1] * y[1]) + fmaf(y[2], y[2], y[3] * y[3]);
                    if (j) { SB(); PAD(); SB(); }
                }
        }
        v4f part;
        if (M2 && layer) {
            PADB(L_e0);
            float a[2][2];
#pragma unroll
            for (int m = 0; m < 2; ++m) {
                const v2f x0 = lo2(res[m][0]), x1 = hi2(res[m][0]), x2 = lo2(res[m][1]), x3 = hi2(res[m][1]);
                const v2f s0 = ((x0 + x1) + x2) + x3, s1 = fma2(x3, x3, fma2(x2, x2, fma2(x1, x1, x0 * x0)));
                a[m][0] = s0[0] + s0[1]; a[m][1] = s1[0] + s1[1];
                SB(); PADX(); SB();
            }
            part = fold2(a[0], a[1]);
            SB(); PADT(); SB();
        } else {
        float v[2][8];
#pragma unroll
        for (int m = 0; m < 2; ++m) {
            v2f s[6];
            const v4f y0 = up4(yp[m][0]), y1 = up4(yp[m][1]);
            mom6i(s, lo2(res[m][0]), lo2(y0)); mom6(s, hi2(res[m][0]), hi2(y0)); mom6(s, lo2(res[m][1]), lo2(y1));
            SB(); PAD(); SB();
            mom6(s, hi2(res[m][1]), hi2(y1));
#pragma unroll
            for (int k = 0; k < 6; ++k) v[m][k] = s[k][0] + s[k][1];
            v[m][6] = ys[m][0]; v[m][7] = ys[m][1];
        }
        SB(); PAD(); SB();
        part = fold8(v[0], v[1]);
        }
        CFENCE();
        if (B3A > 17) { asm volatile("" :: "v"(part)); __builtin_amdgcn_s_barrier(); CFENCE(); }
        lwf(sr + aw * 1024, part);
        TS(15 + 20 * layer);
        PAD();
        bar();
        u32 xa[2];
#pragma unroll
        for (int m = 0; m < 2; ++m) { xa[m] = spread(m * TSL + nt * 512 + (g & 1) * 512 + ((g >> 1) * 16 + li) * 16); OPQV(xa[m]); }
        bar();
        TS(19 + 20 * layer);
#pragma unroll
        for (int m = 0; m < 2; ++m) {
            const v4f s1 = lrf(L_STAT + m * 512 + li * 16), s2 = lrf(L_STAT + m * 512 + 256 + li * 16);
            const v2f a = lo2(s1), b = hi2(s1), e = lo2(s2), f = hi2(s2);
            v2u d[2];
#pragma unroll
            for (int j = 0; j < 2; ++j) {
                const v4f y = up4(yp[m][j]), xv = res[m][j];
                const v2f l = fma2(fma2(lo2(xv), a, b), lo2(y), lo2(xv)), h = fma2(fma2(hi2(xv), a, b), hi2(y), hi2(xv));
                res[m][j] = v4f{l[0], l[1], h[0], h[1]};
                const v2f ol = fma2(l, e, f), oh = fma2(h, e, f);
                d[j] = v2u{san(pk(ol[0], ol[1])), san(pk(oh[0], oh[1]))};
            }
            SB(); PAD(); SB();
            const v2u e0 = sw16(d[0].x, d[1].x), e1 = sw16(d[0].y, d[1].y);
            const v4u o16 = {e0.x, e1.x, e0.y, e1.y};
            if (!layer) sts16<HS>(rw, xa[m], (gs ^ FX2) + B_XN2, o16);
            else sts16<OAUX>(mkrs(out, FR), li * 1152 + (g & 1) * 32 + (g >> 1) * 16, (mt0 + m) * TSL + nt * 32, o16);
        }
        TS(20 + 20 * layer);
        if (!layer) { CFENCE(); __builtin_amdgcn_s_barrier(); CFENCE(); }
#pragma unroll
        for (int k = 0; k < 9; ++k) asm volatile("" : "=v"(ha[k]));
    }
    TSOUT();
}

DI void st16(rsrc_t r, u32 off, v4u v) { __builtin_amdgcn_raw_buffer_store_b128(v, r, off, 0, PS); }
DI v2f up2(u32 d) { return v2f{blo(d), bhi(d)}; }
DI u32 prepare(const u16* __restrict__ x, const u16* __restrict__ wqkv, const u16* __restrict__ wo, const u16* __restrict__ bk, const u16* __restrict__ bv,
                const u16* __restrict__ sk, const u16* __restrict__ hk, const char* ws, const rsrc_t rw, const int wave, const int bid TSARG_R) {
    const int lane = lane_id();
    u32 ep = 0;
    auto fill = [&]() {
        TS(41);
    };
    if (wave == 0) {
        const rsrc_t rx = mkrs(x, FR);
        const int row = bid * 8 + (lane & 7), c = lane >> 3;
        v4u d[9];
#pragma unroll
        for (int i = 0; i < 9; ++i) d[i] = ld16(rx, row * 1152 + (c + 8 * i) * 16);
        fill();
        v2f f[36], s = {0.f, 0.f}, q = {0.f, 0.f};
#pragma unroll
        for (int i = 0; i < 36; ++i) { f[i] = up2(d[i >> 2][i & 3]); s += f[i]; q = fma2(f[i], f[i], q); }
        TS(47);
        const v2u r = sw32(u_(s[0] + s[1]), u_(q[0] + q[1]));
        float t = f_(r.x) + f_(r.y);
        t += f_(__builtin_amdgcn_update_dpp(0, u_(t), 0x128, 0xF, 0xF, false));
        const v2u r2 = sw16(u_(t), u_(t));
        t = (f_(r2.x) + f_(r2.y)) * (1.f / 576.f);
        const v2u r3 = sw32(u_(t), u_(t));
        const float mean = f_(r3.x), rs = __builtin_amdgcn_rsqf(fmaxf(fmaf(-mean, mean, f_(r3.y)), 0.f) + 1e-5f), off = -mean * rs;
        const v2f a = {rs, rs}, o = {off, off};
        const u32 dst = OFF_XN1 + (row >> 4) * TSL + (c >> 2) * 1024 + ((c & 3) * 16 + (row & 15)) * 16;
#pragma unroll
        for (int i = 0; i < 9; ++i) {
            v4u w;
#pragma unroll
            for (int j = 0; j < 4; ++j) { const v2f e = fma2(f[4 * i + j], a, o); w[j] = pk(e[0], e[1]); }
            st16(rw, dst + i * 2048, w);
        }
    } else if (wave < 3) {
        const int g = lane >> 4, li = lane & 15;
        const int v = 2 * QW + wave - 1, ks = v / 24, s = v % 24 >> 3, pp = v & 7;
        const u32 sc = 1024 + wave * 8192, vo = lane / 9 * 3456 + lane % 9 * 16;
        const rsrc_t r = mkrs(wqkv, 576 * 1728 * 2);
#pragma unroll
        for (int i = 0; i < 5; ++i) dma1k(r, vo + (32 * ks + 7 * i) * 3456, s * 1152 + pp * 144, sc + i * 1008);
        fill();
        const u32 a = sc + 144 * (8 * g + (li >> 2)), am = a + 8 * (li & 3), ax = a + 72 * (li & 1) + 32;
        const u32 dst = (v < 432 ? OFF_WQP : 0x80000000u) + 2 * pp * WSLAB;
        VMCNT(0);
        TS(47);
        v4u t[5];
#pragma unroll
        for (int n = 0; n < 4; ++n) t[n] = cat(trd(am + 72 * (n >> 1) + 40 * (n & 1)), trd(am + 72 * (n >> 1) + 40 * (n & 1) + 576));
        t[4] = cat(trd(ax), trd(ax + 576));
#pragma unroll
        for (int n = 0; n < 4; ++n) st16(rw, dst + ks * WKS + (s < 2 ? 2048 * s : 4864) + lane * 16 + (n >> 1) * WSLAB + (n & 1) * 1024, t[n]);
        st16(rw, li < 8 ? dst + (li >> 2) * WSLAB + ks * WKS + 4096 + (g * 12 + 4 * s + (li & 3)) * 16 : 0x80000000u, t[4]);
    } else if (wave == 3) {
        const int g = lane >> 4, li = lane & 15;
        const int ks = QO / 9, cg = QO % 9;
        const bool mn = ks < 16;
        const u32 sc = 1024 + wave * 8192, vo = (mn ? 36 * ks + (lane >> 3) : 36 * (8 * (ks - 16) + (lane >> 5)) + 16 + (lane >> 3 & 3)) * 1152 + (lane & 7) * 16 + cg * 128;
        const rsrc_t r = mkrs(wo, 576 * 576 * 2);
#pragma unroll
        for (int i = 0; i < 4; ++i) dma1k(r, vo, (mn ? 8 * i + 4 * (i >> 1) : 72 * i) * 1152, sc + i * 1024);
        fill();
        const u32 a = sc + 128 * ((mn ? 4 : 8) * g + (li >> 2)) + 8 * (li & 3), a2 = a + (mn ? 2048 : 512);
        const u32 dst = (QO < 162 ? OFF_WOP : 0x80000000u) + 4 * cg * TSL + ks * 1024 + lane * 16;
        VMCNT(0);
        TS(47);
        v4u t[4];
#pragma unroll
        for (int n = 0; n < 4; ++n) t[n] = cat(trd(a + 32 * n), trd(a2 + 32 * n));
#pragma unroll
        for (int n = 0; n < 4; ++n) st16(rw, dst + n * TSL, t[n]);
    } else {
        const bool real = wave == 5 || wave == 6 || (wave == 7 && QK >= 192);
        const int v = wave < 7 ? 2 * bid + (wave & 1) : 320 + QK, tb = v / 18, cb = v % 18;
        const int ch = 32 * cb + 4 * (lane & 7), t0 = 64 * tb + (lane & 56);
        v2u kk[11], vv[8];
        v4u sv[2];
        if (real) {
            const rsrc_t rk = mkrs(bk, FR), rv = mkrs(bv, FR), rs = mkrs(sk, 576 * 8);
            sv[0] = ld16(rs, ch * 8); sv[1] = ld16(rs, ch * 8 + 16);
#pragma unroll
            for (int i = 0; i < 3; ++i) { u32 a = (u32)(((t0 - 3 + i) * 576 + ch) * 2); OPQV(a); kk[i] = ld8(rk, a); }
#pragma unroll
            for (int i = 0; i < 8; ++i) { kk[i + 3] = ld8(rk, ((t0 + i) * 576 + ch) * 2); vv[i] = ld8(rv, ((t0 + i) * 576 + ch) * 2); }
        } else {
            asm volatile("" : "=v"(sv[0]), "=v"(sv[1]));
#pragma unroll
            for (int i = 0; i < 11; ++i) asm volatile("" : "=v"(kk[i]));
#pragma unroll
            for (int i = 0; i < 8; ++i) asm volatile("" : "=v"(vv[i]));
#pragma unroll
            for (int i = 0; i < 3; ++i) lw8(L_R + i * CBS + (lane < 32 ? CB_U + (lane >> 3) * 1088 + (lane & 7) * 8 : (lane & 3) * 8), v2u{0u, 0u});
            *(LAS v2u*)(smem + L_FLG + (lane & 15) * 8) = v2u{0u, 0u};
        }
        ep = sld(ws + OFF_EP, 0);
        fill();
        v2f e[2][8];
#pragma unroll
        for (int w = 0; w < 2; ++w) {
            const v4u s = sv[w];
            const v2f c0 = {blo(s.x), blo(s.z)}, c1 = {bhi(s.x), bhi(s.z)}, c2 = {blo(s.y), blo(s.w)}, c3 = {bhi(s.y), bhi(s.w)};
            v2f k2[11];
#pragma unroll
            for (int i = 0; i < 11; ++i) k2[i] = up2(kk[i][w]);
#pragma unroll
            for (int i = 0; i < 8; ++i) e[w][i] = fma2(k2[i + 3], c0, fma2(k2[i + 2], c1, fma2(k2[i + 1], c2, k2[i] * c3))) * up2(vv[i][w]);
        }
        TS(47);
        const u32 sc = 1024 + wave * 8192, wa = sc + (lane & 7) * 544 + (lane >> 3) * 4;
#pragma unroll
        for (int n = 0; n < 4; ++n)
#pragma unroll
            for (int r = 0; r < 4; ++r) *(LAS u32*)(size_t)(wa + n * 128 + r * 32) = pk(e[n >> 1][r][n & 1], e[n >> 1][r + 4][n & 1]);
        CFENCE();
        const u32 ra = sc + (lane >> 5) * 544 + (lane & 31) * 16, dst = (real ? OFF_KVT : 0x80000000u) + (32 * cb + (lane >> 3)) * KVS + tb * 128 + (lane & 7) * 16;
#pragma unroll
        for (int n = 0; n < 4; ++n) st16(rw, dst + 8 * n * KVS, vr16(ra + n * 1088));
        OPQS(ep);
    }
    return ep;
}

__global__ __launch_bounds__(512, 2) void k_main(const u16* __restrict__ x, const u16* __restrict__ wqkv, const u16* __restrict__ wo, const u16* __restrict__ bk, const u16* __restrict__ bv,
                                                 const u16* __restrict__ sk, const u16* __restrict__ hk, const u16* __restrict__ dv, char* ws, u16* out) {
    TSTOP;
    const int wave = __builtin_amdgcn_readfirstlane(threadIdx.x >> 6);
    const int bid = blockIdx.x, xcd = bid & 7, rr = bid >> 3;
    {
        u64 pc; u32 sz;
        asm volatile("s_getpc_b64 %0\n\tL_here:\n\ts_max_i32 %1, L_last-L_here, 0" : "=s"(pc), "=s"(sz) :: "scc");
        asm volatile("s_mov_b32 m0, 0\n\ts_nop 3\n\tbuffer_load_dword %0, %1, 0 offen lds" :: "v"(threadIdx.x * 128), "s"(mkrs((const void*)pc, wave == (rr & 7) ? sz : 0u)) : "memory");
    }
    const int ti = MAP_TI(xcd, rr), p = MAP_P(xcd, rr);
    TSDECL;
    asm volatile("" :: "s"(x), "s"(wqkv), "s"(wo), "s"(bk), "s"(bv), "s"(sk), "s"(hk), "s"(dv), "s"(ws));
    ws = (char*)(((size_t)ws + 0x1FFFFF) & ~(size_t)0x1FFFFF);
    const rsrc_t rw = mkrs(ws, WS_END);
    TS(48);
    const u32 pr = ((ti & 7) << 20) + B_PR + ((ti >> 3) * 16 + p) * 128;
    u32 tl;
    asm volatile("" : "=v"(tl));
    if (wave == 4) {
        const u32 l = lane_id(), o = vmov(1u); sts16(rw, (l & 8) << 10 | (l & 7) << 4 | (l & 48) << 26, pr, v4u{o, o, o, o});
        if (NR == 1) asm volatile("buffer_load_dword %0, %1, %2, %3 offen" : "=v"(tl) : "v"(l << 21 | (l & 60) << 26), "s"(rw), "s"(OFF_F1 + SLOT) : "memory");
    }
    const u32 e = prepare(x, wqkv, wo, bk, bv, sk, hk, ws, rw, wave, bid TSPASS);
    TS(42);
    u32 vote = 0;
    if (wave == 4) {
        u32 o1, o2;
        asm volatile("s_mov_b32 %1, 0\n\ts_mov_b32 %2, 0\n\ts_waitcnt vmcnt(%9) lgkmcnt(0)\n\ts_load_dword %1, %3, %4\n\ts_load_dword %2, %3, %5\n\ts_waitcnt lgkmcnt(1)\n\ts_sleep %6\n\ts_nop %7\n\ts_mov_b32 %0, %2\n\ts_waitcnt lgkmcnt(0)"
                     : "=&s"(vote), "=&s"(o1), "=&s"(o2) : "s"(ws), "s"(pr + (p & 1) * 8192), "s"(pr + (~p & 1) * 8192), "n"(D_V), "n"(D_W), "v"(tl), "n"(HPOS ? 4 : 16) : "memory", "scc");
        vote = (vote ^ p ^ ti >> 3) & 1;
        TS(49);
    }
    u32 dd, dd2;
    {
        const u32 d2 = sld(dv, bid), dq = sld(dv, CHQ(bid) >> 1);
        dd = (wave == 4 ? d2 << 16 : wave < 7 ? d2 : dq << (16 - 16 * (bid & 1))) & 0xFFFF0000u;
        dd2 = (wave == 5 ? d2 << 16 : d2) & 0xFFFF0000u;
    }
    OPQS(dd); OPQS(dd2);
    v2u mk = mark(e);
    mk.y ^= vote;
    u32 ma = lane_id(), mo = OFF_F1 + bid * 8;
    ma = wave == 4 && ma < NREP ? rep(ma) : 0x80000000u;
    OPQV(mk); OPQV(ma); OPQS(mo);
    if (HPOS || wave != 4) __builtin_amdgcn_s_waitcnt(0x0F70);
    TS(43);
    bar();
    if (wave == 4) sts8(rw, ma, mo, mk);
    CFENCE();
    TS(44);
    if (wave > 4) dma4k(mkrs(hk, FR), lane_id() * 16, (wave < 7 ? 2 * bid + wave - 5 : CHQ(bid)) * 4096, L_R + (wave - 5) * CBS + 32);
    if (wave == 0) { HOP(L_qkv0_0); if (!HS0) { HOP(L_qkv0_9); HOP(L_qkv0_18); } }
    else if (wave == 1) { if (HS0) HOP(L_qkv0_9); HOP(L_a0); }
    else if (wave == 2) { if (HS0) HOP(L_qkv0_18); HOP(L_m0); HOP(L_a1); }
    else if (wave == 3) HOP(L_cv0_0);
    else if (wave == 5) HOP(L_cv1_0);
    else if (wave == 6) HOP(L_cv2_0);
    else if (wave == 7) { const int v = rr & 3; if (v == 0) HOP(L_cv3_0); else if (v == 1) HOP(L_cv4_0); else if (v == 2) HOP(L_cv5_0); else HOP(L_cv6_0); }
    if (wave >= 4) { __builtin_amdgcn_s_setprio(PR_C); aux_role(x, dd, dd2, ws, out, rw, wave, ti, p, bid, e, vote ^ ti >> 3 TSPASS); PADT(); asm volatile("L_last:"); __builtin_amdgcn_endpgm(); }
    __builtin_amdgcn_s_setprio(PR_M);
    main_role(x, ws, out, rw, wave, ti, p TSPASS);
}

extern "C" void kernel_launch(void* const* d_in, const int* in_sizes, int n_in, void* d_out, int out_size, void* d_ws, size_t ws_size,
                              hipStream_t stream) {
    k_main<<<256, 512, 0, stream>>>((const u16*)d_in[0], (const u16*)d_in[1], (const u16*)d_in[2], (const u16*)d_in[4], (const u16*)d_in[5], (const u16*)d_in[6],
                                    (const u16*)d_in[7], (const u16*)d_in[8], (char*)d_ws + (WOFF ? ws_size - ((size_t)WOFF << 20) : 0), (u16*)d_out);
}
```
